```python
import torch

def _softmax(x, axis=-1):
    return torch.softmax(x, dim=axis)
def _gelu(x):
    return torch.nn.functional.gelu(x, approximate="tanh")
def _silu(x):
    return torch.nn.functional.silu(x)
def _sigmoid(x):
    return torch.sigmoid(x)
def _layernorm(x, axis=-1, eps=1e-5):
    m = x.mean(dim=axis, keepdim=True)
    v = x.var(dim=axis, keepdim=True, unbiased=False)
    return (x - m) / torch.sqrt(v + eps)
def _scatter_add(tgt, idx, src, axis=0):
    return tgt.clone().index_add_(axis, idx, src)
def _segment_sum(src, seg, n):
    out = torch.zeros((n,) + src.shape[1:], dtype=src.dtype)
    return out.index_add_(0, seg, src)
def _pad(x, pw):
    flat = tuple(v for pair in reversed(pw) for v in pair)
    return torch.nn.functional.pad(x, flat)

kernel_name = 'parallel_sum__ssm_conv_s5__geglu_ffn__prompt'

def setup_inputs():

    _w = (lambda a: (lambda t: t.to(torch.bfloat16) if t.is_floating_point() else t)(torch.as_tensor(a)))
    def _A_setup():
        B, S, D = 1, 2048, 576
        P = D
        Lambda_re = -0.5 * (lambda _x: torch.exp(torch.as_tensor(_x)))((lambda *s: torch.randn(*s, dtype=torch.bfloat16) * 0.8)(P))
        Lambda_im = (lambda *s: torch.randn(*s, dtype=torch.bfloat16) * 0.8)(P) * 1.0
        B_re = (lambda *s: torch.randn(*s, dtype=torch.bfloat16) * 0.8)(P, D) * (D ** -0.5)
        B_im = (lambda *s: torch.randn(*s, dtype=torch.bfloat16) * 0.8)(P, D) * (D ** -0.5)
        C_re = (lambda *s: torch.randn(*s, dtype=torch.bfloat16) * 0.8)(D, P) * (P ** -0.5)
        C_im = (lambda *s: torch.randn(*s, dtype=torch.bfloat16) * 0.8)(D, P) * (P ** -0.5)
        log_dt = (lambda *s: torch.randn(*s, dtype=torch.bfloat16) * 0.8)(P) * 0.3 - 2.0
        Dskip = (lambda *s: torch.randn(*s, dtype=torch.bfloat16) * 0.8)(D)
        return {
            "x": _w((lambda *s: torch.randn(*s, dtype=torch.bfloat16) * 0.05)(B, S, D)),
            "Lambda_re": _w(Lambda_re),
            "Lambda_im": _w(Lambda_im),
            "B_re": _w(B_re),
            "B_im": _w(B_im),
            "C_re": _w(C_re),
            "C_im": _w(C_im),
            "log_dt": _w(log_dt),
            "Dskip": _w(Dskip),
        }
    _A_in = _A_setup()

    def _B_setup():
        B, S, D, F = 1, 2048, 576, 4608
        return {
            "x": _w((lambda *s: torch.randn(*s, dtype=torch.bfloat16) * 0.05)(B, S, D)),
            "w_gate": _w((lambda *s: torch.randn(*s, dtype=torch.bfloat16) * 0.05)(D, F)),
            "w_up": _w((lambda *s: torch.randn(*s, dtype=torch.bfloat16) * 0.05)(D, F)),
            "w_down": _w((lambda *s: torch.randn(*s, dtype=torch.bfloat16) * 0.05)(F, D)),
        }
    _B_in = _B_setup()

    return {
        "A_x": _A_in["x"],
        "A_Lambda_re": _A_in["Lambda_re"],
        "A_Lambda_im": _A_in["Lambda_im"],
        "A_B_re": _A_in["B_re"],
        "A_B_im": _A_in["B_im"],
        "A_C_re": _A_in["C_re"],
        "A_C_im": _A_in["C_im"],
        "A_log_dt": _A_in["log_dt"],
        "A_Dskip": _A_in["Dskip"],
        "B_x": _B_in["x"],
        "B_w_gate": _B_in["w_gate"],
        "B_w_up": _B_in["w_up"],
        "B_w_down": _B_in["w_down"],
    }

def reference(A_x, A_Lambda_re, A_Lambda_im, A_B_re, A_B_im, A_C_re, A_C_im, A_log_dt, A_Dskip, B_x, B_w_gate, B_w_up, B_w_down):
    def _A_ref(x, Lambda_re, Lambda_im, B_re, B_im, C_re, C_im, log_dt, Dskip):
        Bsz, L, H = x.shape
        Lam = Lambda_re + 1j * Lambda_im
        Bmat = B_re + 1j * B_im
        Cmat = C_re + 1j * C_im
        dt = torch.exp(log_dt)
        Lam_bar = torch.exp(dt * Lam)
        B_bar = ((Lam_bar - 1.0) / Lam)[:, None] * Bmat
        Bu = torch.einsum("bld,pd->blp", x + 0j, B_bar)
        h = torch.zeros_like(Bu[:, 0, :])
        hs = []
        for t in range(L):
            h = Lam_bar[None, :] * h + Bu[:, t, :]
            hs.append(h)
        hseq = torch.stack(hs, 1)
        y = 2.0 * torch.einsum("blp,dp->bld", hseq, Cmat).real + x * Dskip[None, None, :]
        return y

    def _B_ref(x, w_gate, w_up, w_down):
        return (_gelu(x @ w_gate) * (x @ w_up)) @ w_down

    x = A_x
    xn = _layernorm(x, axis=-1)
    return x + _A_ref(xn, A_Lambda_re, A_Lambda_im, A_B_re, A_B_im, A_C_re, A_C_im, A_log_dt, A_Dskip) + _B_ref(xn, B_w_gate, B_w_up, B_w_down)
```

```cpp
#include <hip/hip_runtime.h>
#include <stdint.h>
typedef unsigned u32;
constexpr unsigned WS_END = 54298624u;
struct Srd { const void* p; u32 n; u32 f; };
struct Args { Srd d[10]; const void* p[4]; };

static __device__ const __attribute__((aligned(64))) u32 work_tab[4608] = {
0u,0u,0u,10u,708379136u,756822548u,389946885u,0u,2243284116u,221159u,3998663930u,3717u,404964711u,4294967295u,67108935u,37617664u,
0u,1024u,6u,7u,992612880u,906834979u,269488151u,269488144u,3621313827u,0u,1141842562u,811081185u,674518640u,4294967295u,117440832u,20365312u,
0u,2304u,0u,10u,742065154u,790508566u,288888839u,33686018u,129887510u,214918u,1751049596u,3748u,944072449u,4294967295u,67109447u,38502400u,
0u,3328u,6u,7u,1026298898u,806303269u,303174161u,303174162u,3519085989u,0u,3323413252u,819594625u,1213624338u,4294967295u,117441344u,21250048u,
0u,4608u,0u,10u,775751172u,689452560u,322574849u,67372036u,2175110544u,216998u,3930490366u,3780u,1483145507u,4294967295u,67109959u,39387136u,
0u,5632u,6u,7u,1059984916u,839462951u,336860179u,336860180u,3553139751u,0u,1075798918u,828116384u,1752175156u,4294967295u,117441856u,22134784u,
0u,6912u,0u,10u,675217414u,723136530u,356260867u,101058054u,61713426u,219079u,1817093240u,3813u,2013340485u,4294967295u,67110471u,40271872u,
0u,7936u,6u,7u,958926870u,873148961u,370546197u,370546198u,3587226785u,0u,3255239168u,836638144u,135410774u,4294967295u,117442368u,23019520u,
0u,0u,3u,10u,722536968u,891567107u,523118093u,134744072u,2379632387u,229097u,3998663930u,3717u,404964711u,4294967295u,67108951u,37765120u,
0u,1024u,1u,7u,202447384u,1041579011u,404232223u,404232216u,3757661955u,0u,1141842562u,811081185u,674518640u,4294967295u,117440848u,20512768u,
0u,2304u,3u,10u,756222986u,925253125u,422060047u,168430090u,266235781u,222856u,1751049596u,3748u,944072449u,4294967295u,67109463u,38649856u,
0u,3328u,1u,7u,236133402u,941047301u,437918233u,437918234u,3655434117u,0u,3323413252u,819594625u,1213624338u,4294967295u,117441360u,21397504u,
0u,4608u,3u,10u,789909004u,824195079u,455746057u,202116108u,2311458311u,224936u,3930490366u,3780u,1483145507u,4294967295u,67109975u,39534592u,
0u,5632u,1u,7u,135601692u,974206983u,471604251u,471604252u,3689487879u,0u,1075798918u,828116384u,1752175156u,4294967295u,117441872u,22282240u,
0u,6912u,3u,10u,689375246u,857881089u,489432075u,235802126u,198061697u,227017u,1817093240u,3813u,2013340485u,4294967295u,67110487u,40419328u,
0u,7936u,1u,7u,169285662u,1007892993u,505290269u,505290270u,3723574913u,0u,3255239168u,836638144u,135410774u,4294967295u,117442384u,23166976u,
1u,0u,6u,12u,422122000u,69995322u,255198781u,269498407u,3172123386u,1042759521u,0u,0u,0u,0u,131u,48234496u,
0u,1024u,4u,7u,337191456u,790430765u,538976295u,538976288u,3887976749u,0u,1141842562u,811081185u,674518640u,4294967295u,117440864u,20660224u,
1u,2304u,6u,12u,455808018u,103681340u,154140735u,303184417u,1058726780u,1050748672u,0u,0u,0u,0u,547u,48234496u,
0u,3328u,4u,7u,370877474u,689899055u,572662305u,572662306u,3785748911u,0u,3323413252u,819594625u,1213624338u,4294967295u,117441376u,21544960u,
1u,4608u,6u,12u,489494036u,2625342u,187826745u,336870435u,3103949822u,1059270432u,0u,0u,0u,0u,1043u,48234496u,
0u,5632u,4u,7u,270345764u,723058729u,606348323u,606348324u,3819802665u,0u,1075798918u,828116384u,1752175156u,4294967295u,117441888u,22429696u,
1u,6912u,6u,12u,523178006u,36309304u,221512763u,370556453u,990552696u,1067792193u,0u,0u,0u,0u,1539u,48234496u,
0u,7936u,4u,7u,304029734u,756744747u,640034341u,640034342u,3853889707u,0u,3255239168u,836638144u,135410774u,4294967295u,117442400u,23314432u,
0u,0u,1u,11u,553720344u,204747539u,1060048405u,404232232u,2503167187u,15269619u,670719764u,62u,404964711u,4294967295u,50331717u,44695552u,
0u,1280u,7u,10u,840051497u,352723995u,538394414u,690563369u,3998509339u,229759u,1086090060u,58u,674518640u,4294967295u,50331975u,38060032u,
0u,2304u,1u,11u,587406362u,238433557u,958990359u,437918250u,389770581u,15394450u,2715975574u,62u,944072449u,4294967295u,50332229u,45580288u,
0u,3584u,7u,10u,873737515u,385885725u,571554088u,724249387u,1750862237u,231710u,3265530830u,58u,1213624338u,4294967295u,50332487u,38944768u,
0u,4608u,1u,11u,621092380u,137377559u,992676369u,471604268u,2434993623u,15527602u,602545680u,63u,1483145507u,4294967295u,50332741u,46465024u,
0u,5888u,7u,10u,907423533u,285351967u,605240106u,757935405u,3930335263u,233790u,1152133704u,59u,1752175156u,4294967295u,50332999u,39829504u,
0u,6912u,1u,11u,654776350u,171061521u,1026362387u,505290286u,321596497u,15660755u,2784116370u,63u,2013340485u,4294967295u,50333253u,47349760u,
0u,8192u,7u,10u,806365487u,319037977u,638926124u,791621423u,1816938649u,235871u,3333704394u,59u,135410774u,4294967295u,50333511u,40714240u,
0u,0u,4u,11u,154214944u,623065883u,121050653u,538976304u,2643869403u,15736693u,670719764u,62u,404964711u,4294967295u,50331733u,44843008u,
0u,1280u,2u,10u,203043633u,487468067u,673122102u,825307441u,4134857507u,238065u,1086090060u,58u,674518640u,4294967295u,50331991u,38207488u,
0u,2304u,4u,11u,187900962u,656751901u,19992607u,572662322u,530472797u,15861524u,2715975574u,62u,944072449u,4294967295u,50332245u,45727744u,
0u,3584u,2u,10u,236729651u,520629797u,706281776u,858993459u,1887210405u,240016u,3265530830u,58u,1213624338u,4294967295u,50332503u,39092224u,
0u,4608u,4u,11u,221586980u,556220191u,53678617u,606348340u,2575728607u,15994676u,602545680u,63u,1483145507u,4294967295u,50332757u,46612480u,
0u,5888u,2u,10u,136197941u,420096039u,739967794u,892679477u,4066683431u,242096u,1152133704u,59u,1752175156u,4294967295u,50333015u,39976960u,
0u,6912u,4u,11u,255270950u,589379865u,87364635u,640034358u,462298713u,16127829u,2784116370u,63u,2013340485u,4294967295u,50333269u,47497216u,
0u,8192u,2u,10u,169357623u,453782049u,773653812u,926365495u,1953286817u,244177u,3333704394u,59u,135410774u,4294967295u,50333527u,40861696u,
0u,256u,7u,10u,722545192u,891567124u,523642381u,673720360u,2379632404u,229225u,4135012075u,3847u,404964711u,4294967295u,67108946u,41304064u,
0u,1280u,5u,10u,337787705u,622212141u,807866174u,960051513u,4271205677u,246387u,1086090060u,58u,674518640u,4294967295u,50332007u,38354944u,
0u,2560u,7u,10u,756231210u,925253142u,422584335u,707406378u,266235798u,222984u,1887397741u,3878u,944072449u,4294967295u,67109458u,42188800u,
0u,3584u,5u,10u,371473723u,655373871u,841025848u,993737531u,2023558575u,248338u,3265530830u,58u,1213624338u,4294967295u,50332519u,39239680u,
0u,4864u,7u,10u,789917228u,824195088u,456270345u,741092396u,2311458320u,225064u,4066838511u,3910u,1483145507u,4294967295u,67109970u,43073536u,
0u,5888u,5u,10u,270942013u,554840105u,874711866u,1027423549u,4203031593u,250418u,1152133704u,59u,1752175156u,4294967295u,50333031u,40124416u,
0u,7168u,7u,10u,689383470u,857881106u,489956363u,774778414u,198061714u,227145u,1953441385u,3943u,2013340485u,4294967295u,67110482u,43958272u,
0u,8192u,5u,10u,304101695u,588526123u,908397884u,1061109567u,2089634987u,252499u,3333704394u,59u,135410774u,4294967295u,50333543u,41009152u,
0u,256u,2u,11u,553726512u,204737306u,1060572693u,808464400u,2503164634u,13696883u,670719764u,62u,404964711u,4294967295u,50331749u,44990464u,
1u,1280u,0u,12u,704774913u,87831836u,268447518u,16849208u,3726101852u,913850379u,0u,0u,0u,0u,275u,48234496u,
0u,2560u,2u,11u,587412530u,238423324u,959514647u,842150418u,389768028u,13821714u,2715975574u,62u,944072449u,4294967295u,50332261u,45875200u,
1u,3584u,0u,12u,738460931u,121517854u,302131480u,50535226u,1478487518u,922372138u,0u,0u,0u,0u,771u,48234496u,
0u,4864u,2u,11u,621098548u,137367326u,993200657u,875836436u,2434991070u,13954866u,602545680u,63u,1483145507u,4294967295u,50332773u,46759936u,
1u,5888u,0u,12u,772146949u,20459800u,335817498u,84221244u,3657927768u,930893898u,0u,0u,0u,0u,1315u,48234496u,
0u,7168u,2u,11u,654782518u,171051288u,1026886675u,909522454u,321593944u,14088019u,2784116370u,63u,2013340485u,4294967295u,50333285u,47644672u,
1u,8192u,0u,12u,671088903u,54145818u,369503516u,117907262u,1544531162u,939415659u,0u,0u,0u,0u,1811u,48234496u,
1u,256u,5u,13u,422132280u,69993234u,120522301u,941627431u,3172122834u,371662577u,0u,0u,0u,0u,35u,48234496u,
1u,1280u,3u,12u,973736713u,218381587u,403191590u,151593233u,3862187859u,944857229u,0u,0u,0u,0u,291u,48234496u,
1u,2560u,5u,13u,455818298u,103679252u,19464255u,975313441u,1058726228u,379651728u,0u,0u,0u,0u,531u,48234496u,
1u,3584u,3u,12u,1007422731u,252067605u,436875552u,185279251u,1614573525u,953378988u,0u,0u,0u,0u,787u,48234496u,
1u,4864u,5u,13u,489504316u,2623254u,53150265u,1008999459u,3103949270u,388173488u,0u,0u,0u,0u,1027u,48234496u,
1u,5888u,3u,12u,1041108749u,151009559u,470561570u,218965269u,3794013783u,961900748u,0u,0u,0u,0u,1283u,48234496u,
1u,7168u,5u,13u,523188286u,36307216u,86836283u,1042685477u,990552144u,396695249u,0u,0u,0u,0u,1571u,48234496u,
1u,8192u,3u,12u,940050703u,184695569u,504247588u,252651287u,1680617169u,970422509u,0u,0u,0u,0u,1827u,48234496u,
0u,256u,0u,10u,154206720u,623063827u,255727133u,0u,2643868883u,212965u,3998663930u,3717u,404964711u,4294967295u,67108967u,37912576u,
0u,1536u,6u,8u,187306769u,924718636u,286337047u,286331153u,400418220u,58u,2511420122u,13738363u,674518640u,4294967295u,100663622u,27443200u,
0u,2560u,0u,10u,187892738u,656749845u,154669087u,33686018u,530472277u,206724u,1751049596u,3748u,944072449u,4294967295u,67109479u,38797312u,
0u,3840u,6u,8u,220992787u,823660590u,320023057u,320017171u,2445640750u,58u,398023516u,13871386u,1213624338u,4294967295u,100664134u,28327936u,
0u,4864u,0u,10u,221578756u,556218135u,188355097u,67372036u,2575728087u,208804u,3930490366u,3780u,1483145507u,4294967295u,67109991u,39682048u,
0u,6144u,6u,8u,254678805u,857346600u,353709075u,353703189u,332244136u,59u,2443279326u,14004538u,1752175156u,4294967295u,100664646u,29212672u,
0u,7168u,0u,10u,255262726u,589377809u,222041115u,101058054u,462298193u,210885u,1817093240u,3813u,2013340485u,4294967295u,67110503u,40566784u,
0u,8448u,6u,8u,153620759u,891032618u,387395093u,387389207u,2513814826u,59u,329849432u,14137691u,135410774u,4294967295u,100665158u,30097408u,
0u,256u,3u,10u,574163464u,756824859u,388898309u,134744072u,2243284699u,220903u,4135012075u,3847u,404964711u,4294967295u,67108930u,41156608u,
0u,1536u,1u,10u,973216537u,637875203u,19922975u,421075225u,530078979u,198400u,1222438228u,60u,674518640u,4294967295u,50331970u,41598976u,
0u,2560u,3u,10u,607849482u,790510877u,287840263u,168430090u,129888093u,214662u,1887397741u,3878u,944072449u,4294967295u,67109442u,42041344u,
0u,3840u,1u,10u,1006902555u,537343493u,53608985u,454761243u,2575334789u,200480u,3401878998u,60u,1213624338u,4294967295u,50332482u,42483712u,
0u,4864u,3u,10u,641535500u,689454879u,321526273u,202116108u,2175111135u,216742u,4066838511u,3910u,1483145507u,4294967295u,67109954u,42926080u,
0u,6144u,1u,10u,1040588573u,570503175u,87295003u,488447261u,461904903u,202561u,1288481872u,61u,1752175156u,4294967295u,50332994u,43368448u,
0u,7168u,3u,10u,541001742u,723138841u,355212291u,235802126u,61714009u,218823u,1953441385u,3943u,2013340485u,4294967295u,67110466u,43810816u,
0u,8448u,1u,10u,939530527u,604189185u,120981021u,522133279u,2643475585u,204641u,3470052562u,61u,135410774u,4294967295u,50333506u,44253184u,
0u,512u,6u,8u,725160464u,221061899u,269485846u,269488144u,3593918667u,49u,1154294905u,13163757u,404964711u,4294967295u,100663363u,23461888u,
0u,1536u,4u,10u,839525153u,772619309u,154667047u,555819297u,666427181u,206722u,1222438228u,60u,674518640u,4294967295u,50331986u,41746432u,
0u,2816u,6u,8u,758846482u,254747917u,303169808u,303174162u,1346304333u,48u,3335865595u,13288589u,944072449u,4294967295u,100663875u,24346624u,
0u,3840u,4u,10u,873211171u,672087599u,188353057u,589505315u,2711682991u,208802u,3401878998u,60u,1213624338u,4294967295u,50332498u,42631168u,
0u,5120u,6u,8u,792532500u,153691919u,336855826u,336860180u,3525745103u,48u,1088251261u,13421740u,1483145507u,4294967295u,100664387u,25231360u,
0u,6144u,4u,10u,906897189u,705247273u,222039075u,623191333u,598253097u,210883u,1288481872u,61u,1752175156u,4294967295u,50333010u,43515904u,
0u,7424u,6u,8u,691998742u,187375881u,370541844u,370546198u,1412347977u,49u,3267692031u,13554892u,2013340485u,4294967295u,100664899u,26116096u,
0u,8448u,4u,10u,805839143u,738933291u,255725093u,656877351u,2779823787u,212963u,3470052562u,61u,135410774u,4294967295u,50333522u,44400640u,
0u,512u,1u,8u,167909912u,104143923u,404229918u,404232216u,3726333747u,51u,1154294905u,13163757u,404964711u,4294967295u,100663379u,23609344u,
0u,1536u,7u,8u,202517289u,907346997u,690556991u,690563369u,1071206709u,52u,2375073426u,13205753u,674518640u,4294967295u,100663619u,23904256u,
0u,2816u,1u,8u,201595930u,3612213u,437913880u,437918234u,1476622261u,50u,3335865595u,13288589u,944072449u,4294967295u,100663891u,24494080u,
0u,3840u,7u,8u,236203307u,806815287u,724243001u,724249387u,3116462519u,52u,261676820u,13338776u,1213624338u,4294967295u,100664131u,24788992u,
0u,5120u,1u,8u,235281948u,36771895u,471599898u,471604252u,3658159671u,50u,1088251261u,13421740u,1483145507u,4294967295u,100664403u,25378816u,
0u,6144u,7u,8u,135671597u,839974961u,757929019u,757935405u,1003032625u,53u,2306932630u,13471928u,1752175156u,4294967295u,100664643u,25673728u,
0u,7424u,1u,8u,134223902u,70457905u,505285916u,505290270u,1544763057u,51u,3267692031u,13554892u,2013340485u,4294967295u,100664915u,26263552u,
0u,8448u,7u,8u,168831279u,873660979u,791615037u,791621423u,3184603315u,53u,193502736u,13605081u,135410774u,4294967295u,100665155u,26558464u,
0u,512u,4u,8u,319889952u,238887995u,538973990u,538976288u,3862681915u,53u,1154294905u,13163757u,404964711u,4294967295u,100663395u,23756800u,
0u,1536u,2u,8u,337261361u,1042089533u,825300999u,825307441u,133812669u,54u,2375073426u,13205753u,674518640u,4294967295u,100663635u,24051712u,
0u,2816u,4u,8u,353575970u,138356285u,572657952u,572662306u,1612970429u,52u,3335865595u,13288589u,944072449u,4294967295u,100663907u,24641536u,
0u,3840u,2u,8u,370947379u,941555775u,858987009u,858993459u,2179067967u,54u,261676820u,13338776u,1213624338u,4294967295u,100664147u,24936448u,
0u,5120u,4u,8u,387261988u,171515967u,606343970u,606348324u,3794507839u,52u,1088251261u,13421740u,1483145507u,4294967295u,100664419u,25526272u,
0u,6144u,2u,8u,270415669u,974717497u,892673027u,892679477u,65638585u,55u,2306932630u,13471928u,1752175156u,4294967295u,100664659u,25821184u,
0u,7424u,4u,8u,286728230u,205201977u,640029988u,640034342u,1681111225u,53u,3267692031u,13554892u,2013340485u,4294967295u,100664931u,26411008u,
0u,8448u,2u,8u,303575351u,1008403515u,926359045u,926365495u,2247209275u,55u,193502736u,13605081u,135410774u,4294967295u,100665171u,26705920u,
0u,512u,7u,9u,553724456u,487336980u,673656622u,673720360u,4000631572u,3707u,2238724257u,221303u,404964711u,4294967295u,83886145u,30539776u,
0u,1536u,5u,8u,472005433u,790957604u,960045071u,960051513u,264131492u,56u,2375073426u,13205753u,674518640u,4294967295u,100663651u,24199168u,
0u,2816u,7u,9u,587410474u,521022998u,706816296u,707406378u,1753017238u,3610u,125327651u,223254u,944072449u,4294967295u,83886657u,31424512u,
0u,3840u,5u,8u,505691451u,690423846u,993731081u,993737531u,2309386790u,56u,261676820u,13338776u,1213624338u,4294967295u,100664163u,25083904u,
0u,5120u,7u,9u,621096492u,419964944u,740502314u,741092396u,3932457488u,3642u,2170583461u,225334u,1483145507u,4294967295u,83887169u,32309248u,
0u,6144u,5u,8u,405159741u,723585568u,1027417099u,1027423549u,195957408u,57u,2306932630u,13471928u,1752175156u,4294967295u,100664675u,25968640u,
0u,7424u,7u,9u,654780462u,453650962u,774188332u,774778414u,1819060882u,3675u,57153575u,227415u,2013340485u,4294967295u,83887681u,33193984u,
0u,8448u,5u,8u,438319423u,757271586u,1061103117u,1061109567u,2377528098u,57u,193502736u,13605081u,135410774u,4294967295u,100665187u,26853376u,
0u,512u,2u,9u,154219056u,86324227u,805320502u,808464432u,4128660739u,3085u,2238724257u,221303u,404964711u,4294967295u,83886161u,30687232u,
0u,1792u,0u,8u,840565505u,1042089533u,16848903u,16843009u,133812669u,54u,2511420122u,13738363u,674518640u,4294967295u,100663654u,27738112u,
0u,2816u,2u,9u,187905074u,120010245u,839004464u,842150450u,1881046405u,3116u,125327651u,223254u,944072449u,4294967295u,83886673u,31571968u,
0u,4096u,0u,8u,874251523u,941555775u,50534913u,50529027u,2179067967u,54u,398023516u,13871386u,1213624338u,4294967295u,100664166u,28622848u,
0u,5120u,2u,9u,221591092u,18952199u,872690482u,875836468u,4060486663u,3148u,2170583461u,225334u,1483145507u,4294967295u,83887185u,32456704u,
0u,6400u,0u,8u,907937541u,974717497u,84220931u,84215045u,65638585u,55u,2443279326u,14004538u,1752175156u,4294967295u,100664678u,29507584u,
0u,7424u,2u,9u,255275062u,52638209u,906376500u,909522486u,1947090049u,3181u,57153575u,227415u,2013340485u,4294967295u,83887697u,33341440u,
0u,8704u,0u,8u,806879495u,1008403515u,117906949u,117901063u,2247209275u,55u,329849432u,14137691u,135410774u,4294967295u,100665190u,30392320u,
0u,512u,5u,10u,422132280u,352599067u,136265534u,943208504u,4266938139u,205311u,4135012075u,3847u,404964711u,4294967295u,67108962u,41451520u,
0u,1792u,3u,8u,203557641u,790957604u,151592975u,151587081u,264131492u,56u,2647768290u,14270973u,674518640u,4294967295u,100663617u,30982144u,
0u,2816u,5u,10u,455818298u,386285085u,169425208u,976894522u,2019323805u,207262u,1887397741u,3878u,944072449u,4294967295u,67109474u,42336256u,
0u,4096u,3u,8u,237243659u,690423846u,185278985u,185273099u,2309386790u,56u,534371684u,14403996u,1213624338u,4294967295u,100664129u,31866880u,
0u,5120u,5u,10u,489504316u,285227039u,203111226u,1010580540u,4198764063u,209342u,4066838511u,3910u,1483145507u,4294967295u,67109986u,43220992u,
0u,6400u,3u,8u,136711949u,723585568u,218965003u,218959117u,195957408u,57u,2579627494u,14537148u,1752175156u,4294967295u,100664641u,32751616u,
0u,7424u,5u,10u,523188286u,318913049u,236797244u,1044266558u,2085367449u,211423u,1953441385u,3943u,2013340485u,4294967295u,67110498u,44105728u,
0u,8704u,3u,8u,169871631u,757271586u,252651021u,252645135u,2377528098u,57u,466197600u,14670301u,135410774u,4294967295u,100665153u,33636352u,
0u,768u,0u,8u,319881728u,104143931u,3862u,0u,3592116027u,51u,1290643009u,13696367u,404964711u,4294967295u,100663366u,27000832u,
0u,1792u,6u,8u,338301713u,924718636u,286337047u,286331153u,400418220u,58u,2647768290u,14270973u,674518640u,4294967295u,100663633u,31129600u,
0u,3072u,0u,8u,353567746u,3612221u,33687824u,33686018u,1342404541u,50u,3472213699u,13821199u,944072449u,4294967295u,100663878u,27885568u,
0u,4096u,6u,8u,371987731u,823660590u,320023057u,320017171u,2445640750u,58u,534371684u,14403996u,1213624338u,4294967295u,100664145u,32014336u,
0u,5376u,0u,8u,387253764u,36771903u,67373842u,67372036u,3523941951u,50u,1224599365u,13954350u,1483145507u,4294967295u,100664390u,28770304u,
0u,6400u,6u,8u,271456021u,857346600u,353709075u,353703189u,332244136u,59u,2579627494u,14537148u,1752175156u,4294967295u,100664657u,32899072u,
0u,7680u,0u,8u,286720006u,70457913u,101059860u,101058054u,1410545337u,51u,3404040135u,14087502u,2013340485u,4294967295u,100664902u,29655040u,
0u,8704u,6u,8u,304615703u,891032618u,387395093u,387389207u,2513814826u,59u,466197600u,14670301u,135410774u,4294967295u,100665169u,33783808u,
0u,768u,3u,8u,454625800u,238887986u,134747934u,134744072u,3728464178u,53u,1290643009u,13696367u,404964711u,4294967295u,100663382u,27148288u,
0u,1792u,1u,10u,855776025u,637875228u,825229343u,421075225u,530079004u,247552u,1222438228u,60u,674518640u,4294967295u,50332002u,41893888u,
0u,3072u,3u,8u,488311818u,138356276u,168431896u,168430090u,1478752692u,52u,3472213699u,13821199u,944072449u,4294967295u,100663894u,28033024u,
0u,4096u,1u,10u,889462043u,537343518u,858915353u,454761243u,2575334814u,249632u,3401878998u,60u,1213624338u,4294967295u,50332514u,42778624u,
0u,5376u,3u,8u,521997836u,171515958u,202117914u,202116108u,3660290102u,52u,1224599365u,13954350u,1483145507u,4294967295u,100664406u,28917760u,
0u,6400u,1u,10u,923148061u,570503192u,892601371u,488447261u,461904920u,251713u,1288481872u,61u,1752175156u,4294967295u,50333026u,43663360u,
0u,7680u,3u,8u,421464078u,205201968u,235803932u,235802126u,1546893488u,53u,3404040135u,14087502u,2013340485u,4294967295u,100664918u,29802496u,
0u,8704u,1u,10u,822090015u,604189210u,926287389u,522133279u,2643475610u,253793u,3470052562u,61u,135410774u,4294967295u,50333538u,44548096u,
0u,768u,6u,9u,556339728u,352599060u,270479142u,269488144u,3864284948u,3579u,2238724257u,221303u,404964711u,4294967295u,83886177u,30834688u,
0u,1792u,4u,10u,973742881u,772619309u,959973415u,555819297u,666427181u,255874u,1358786396u,62u,674518640u,4294967295u,50331973u,45137920u,
0u,3072u,6u,9u,590025746u,386285078u,303638816u,303174162u,1616670614u,3482u,125327651u,223254u,944072449u,4294967295u,83886689u,31719424u,
0u,4096u,4u,10u,1007428899u,672087599u,993659425u,589505315u,2711682991u,257954u,3538227166u,62u,1213624338u,4294967295u,50332485u,46022656u,
0u,5376u,6u,9u,623711764u,285227024u,337324834u,336860180u,3796110864u,3514u,2170583461u,225334u,1483145507u,4294967295u,83887201u,32604160u,
0u,6400u,4u,10u,1041114917u,705247273u,1027345443u,623191333u,598253097u,260035u,1424830040u,63u,1752175156u,4294967295u,50332997u,46907392u,
0u,7680u,6u,9u,657395734u,318913042u,371010852u,370546198u,1682714258u,3547u,57153575u,227415u,2013340485u,4294967295u,83887713u,33488896u,
0u,8704u,4u,10u,940056871u,738933291u,1061031461u,656877351u,2779823787u,262115u,3606400730u,63u,135410774u,4294967295u,50333509u,47792128u,
0u,768u,1u,9u,151067160u,487336963u,405223230u,404232216u,4269067011u,3709u,2375072386u,229625u,404964711u,4294967295u,83886148u,34078720u,
0u,1792u,7u,8u,991046441u,907346997u,690556991u,690563369u,1071206709u,52u,2511420122u,13738363u,674518640u,4294967295u,100663638u,27590656u,
0u,3072u,1u,9u,184753178u,521022981u,438382904u,437918234u,2021452677u,3612u,261675780u,231576u,944072449u,4294967295u,83886660u,34963456u,
0u,4096u,7u,8u,1024732459u,806815287u,724243001u,724249387u,3116462519u,52u,398023516u,13871386u,1213624338u,4294967295u,100664150u,28475392u,
0u,5376u,1u,9u,218439196u,419964935u,472068922u,471604252u,4200892935u,3644u,2306931590u,233656u,1483145507u,4294967295u,83887172u,35848192u,
0u,6400u,7u,8u,1058418477u,839974961u,757929019u,757935405u,1003032625u,53u,2443279326u,14004538u,1752175156u,4294967295u,100664662u,29360128u,
0u,7680u,1u,9u,252123166u,453650945u,505754940u,505290270u,2087496321u,3677u,193501696u,235737u,2013340485u,4294967295u,83887684u,36732928u,
0u,8704u,7u,8u,957360431u,873660979u,791615037u,791621423u,3184603315u,53u,329849432u,14137691u,135410774u,4294967295u,100665174u,30244864u,
0u,768u,4u,9u,724640288u,86324235u,540032814u,538976288u,3994443019u,3855u,2375072386u,229625u,404964711u,4294967295u,83886164u,34226176u,
0u,2048u,2u,8u,840053553u,907346997u,825299007u,825307441u,1071206709u,52u,2647768290u,14270973u,674518640u,4294967295u,100663649u,31277056u,
0u,3072u,4u,9u,758326306u,120010253u,573716776u,572662306u,1746828685u,3886u,261675780u,231576u,944072449u,4294967295u,83886676u,35110912u,
0u,4352u,2u,8u,873739571u,806815287u,858985017u,858993459u,3116462519u,52u,534371684u,14403996u,1213624338u,4294967295u,100664161u,32161792u,
0u,5376u,4u,9u,792012324u,18952207u,607402794u,606348324u,3926268943u,3918u,2306931590u,233656u,1483145507u,4294967295u,83887188u,35995648u,
0u,6656u,2u,8u,907425589u,839974961u,892671035u,892679477u,1003032625u,53u,2579627494u,14537148u,1752175156u,4294967295u,100664673u,33046528u,
0u,7680u,4u,9u,691478566u,52638217u,641088812u,640034342u,1812872329u,3951u,193501696u,235737u,2013340485u,4294967295u,83887700u,36880384u,
0u,8960u,2u,8u,806367543u,873660979u,926357053u,926365495u,3184603315u,53u,466197600u,14670301u,135410774u,4294967295u,100665185u,33931264u,
0u,768u,7u,9u,976824872u,221061939u,674760502u,673720360u,4130789619u,3969u,2375072386u,229625u,404964711u,4294967295u,83886180u,34373632u,
0u,2048u,5u,8u,203045689u,1042089533u,960043015u,960051513u,133812669u,54u,2784116458u,14803583u,674518640u,4294967295u,100663620u,34521088u,
0u,3072u,7u,9u,1010510890u,254747957u,708444464u,707406378u,1883175285u,4000u,261675780u,231576u,944072449u,4294967295u,83886692u,35258368u,
0u,4352u,5u,8u,236731707u,941555775u,993729025u,993737531u,2179067967u,54u,670719852u,14936606u,1213624338u,4294967295u,100664132u,35405824u,
0u,5376u,7u,9u,1044196908u,153691959u,742130482u,741092396u,4062616055u,4032u,2306931590u,233656u,1483145507u,4294967295u,83887204u,36143104u,
0u,6656u,5u,8u,136199997u,974717497u,1027415043u,1027423549u,65638585u,55u,2715975662u,15069758u,1752175156u,4294967295u,100664644u,36290560u,
0u,7680u,7u,9u,943663150u,187375921u,775816500u,774778414u,1949218929u,4065u,193501696u,235737u,2013340485u,4294967295u,83887716u,37027840u,
0u,8960u,5u,8u,169359679u,1008403515u,1061101061u,1061109567u,2247209275u,55u,602545768u,15202911u,135410774u,4294967295u,100665156u,37175296u,
0u,1024u,2u,7u,859386416u,637873179u,808464399u,808464432u,3482868507u,0u,4062747753u,808393432u,404964711u,4294967295u,117440608u,20217856u,
0u,2048u,0u,8u,337773313u,790957604u,16850959u,16843009u,264131492u,56u,2784116458u,14803583u,674518640u,4294967295u,100663636u,34668544u,
0u,3328u,2u,7u,893072434u,537341469u,842150409u,842150450u,3380640669u,0u,1949351147u,816382713u,944072449u,4294967295u,117441120u,21102592u,
0u,4352u,0u,8u,371459331u,690423846u,50536969u,50529027u,2309386790u,56u,670719852u,14936606u,1213624338u,4294967295u,100664148u,35553280u,
0u,5632u,2u,7u,926758452u,570501151u,875836427u,875836468u,3414694431u,0u,4130921837u,824904345u,1483145507u,4294967295u,117441632u,21987328u,
0u,6656u,0u,8u,270927621u,723585568u,84222987u,84215045u,195957408u,57u,2715975662u,15069758u,1752175156u,4294967295u,100664660u,36438016u,
0u,7936u,2u,7u,826224694u,604187161u,909522445u,909522486u,3448781465u,0u,1881210351u,833426104u,2013340485u,4294967295u,117442144u,22872064u,
0u,8960u,0u,8u,304087303u,757271586u,117909005u,117901063u,2377528098u,57u,602545768u,15202911u,135410774u,4294967295u,100665172u,37322752u,
0u,1024u,5u,8u,556350008u,772158748u,943204407u,943208504u,934833500u,58u,1290643009u,13696367u,404964711u,4294967295u,100663398u,27295744u,
0u,2048u,3u,8u,472517385u,924718636u,151595031u,151587081u,400418220u,58u,2784116458u,14803583u,674518640u,4294967295u,100663652u,34816000u,
0u,3328u,5u,8u,590036026u,671102750u,976890417u,976894522u,2980056542u,58u,3472213699u,13821199u,944072449u,4294967295u,100663910u,28180480u,
0u,4352u,3u,8u,506203403u,823660590u,185281041u,185273099u,2445640750u,58u,670719852u,14936606u,1213624338u,4294967295u,100664164u,35700736u,
0u,5632u,5u,8u,623722044u,704786712u,1010576435u,1010580540u,866659416u,59u,1224599365u,13954350u,1483145507u,4294967295u,100664422u,29065216u,
0u,6656u,3u,8u,405671693u,857346600u,218967059u,218959117u,332244136u,59u,2715975662u,15069758u,1752175156u,4294967295u,100664676u,36585472u,
0u,7936u,5u,8u,657406014u,738472730u,1044262453u,1044266558u,3048230106u,59u,3404040135u,14087502u,2013340485u,4294967295u,100664934u,29949952u,
0u,8960u,3u,8u,438831375u,891032618u,252653077u,252645135u,2513814826u,59u,602545768u,15202911u,135410774u,4294967295u,100665188u,37470208u,
0u,1024u,0u,7u,151060992u,621690123u,63u,0u,4287950667u,0u,4062747753u,808393432u,404964711u,4294967295u,117440576u,19922944u,
0u,2048u,6u,10u,721556241u,637875235u,691011615u,286331153u,530079011u,239360u,1358786396u,62u,674518640u,4294967295u,50331989u,45285376u,
0u,3328u,0u,7u,184747010u,654851853u,33686073u,33686018u,4187787213u,0u,1949351147u,816382713u,944072449u,4294967295u,117441088u,20807680u,
0u,4352u,6u,10u,755242259u,537343525u,724697625u,320017171u,2575334821u,241440u,3538227166u,62u,1213624338u,4294967295u,50332501u,46170112u,
0u,5632u,0u,7u,218433028u,554318095u,67372091u,67372036u,4219776591u,0u,4130921837u,824904345u,1483145507u,4294967295u,117441600u,21692416u,
0u,6656u,6u,10u,788928277u,570503207u,758383643u,353703189u,461904935u,243521u,1424830040u,63u,1752175156u,4294967295u,50333013u,47054848u,
0u,7936u,0u,7u,252116998u,588004105u,101058109u,101058054u,4253863625u,0u,1881210351u,833426104u,2013340485u,4294967295u,117442112u,22577152u,
0u,8960u,6u,10u,687870231u,604189217u,792069661u,387389207u,2643475617u,245601u,3606400730u,63u,135410774u,4294967295u,50333525u,47939584u,
0u,1024u,3u,7u,724634120u,504767507u,134744071u,134744072u,3346622739u,0u,4062747753u,808393432u,404964711u,4294967295u,117440592u,20070400u,
0u,2048u,1u,10u,973740825u,772619309u,557320231u,421075225u,666427181u,231298u,1358786396u,62u,674518640u,4294967295u,50332005u,45432832u,
0u,3328u,3u,7u,758320138u,403711509u,168430081u,168430090u,3244362133u,0u,1949351147u,816382713u,944072449u,4294967295u,117441104u,20955136u,
0u,4352u,1u,10u,1007426843u,672087599u,591006241u,454761243u,2711682991u,233378u,3538227166u,62u,1213624338u,4294967295u,50332517u,46317568u,
0u,5632u,3u,7u,792006156u,437395479u,202116099u,202116108u,3278448663u,0u,4130921837u,824904345u,1483145507u,4294967295u,117441616u,21839872u,
0u,6656u,1u,10u,1041112861u,705247273u,624692259u,488447261u,598253097u,235459u,1424830040u,63u,1752175156u,4294967295u,50333029u,47202304u,
0u,7936u,3u,7u,691472398u,471081489u,235802117u,235802126u,3312535697u,0u,1881210351u,833426104u,2013340485u,4294967295u,117442128u,22724608u,
0u,8960u,1u,10u,940054815u,738933291u,658378277u,522133279u,2779823787u,237539u,3606400730u,63u,135410774u,4294967295u,50333541u,48087040u,
2u,0u,1u,5u,0u,0u,0u,0u,0u,0u,0u,0u,0u,0u,0u,0u,
2u,0u,1u,5u,6u,0u,0u,0u,0u,0u,0u,0u,0u,0u,0u,0u,
2u,0u,1u,5u,12u,0u,0u,0u,0u,0u,0u,0u,0u,0u,0u,0u,
2u,1u,1u,5u,2u,0u,0u,0u,0u,0u,0u,0u,0u,0u,0u,0u,
2u,1u,1u,5u,8u,0u,0u,0u,0u,0u,0u,0u,0u,0u,0u,0u,
2u,1u,1u,5u,14u,0u,0u,0u,0u,0u,0u,0u,0u,0u,0u,0u,
2u,2u,1u,5u,4u,0u,0u,0u,0u,0u,0u,0u,0u,0u,0u,0u,
2u,2u,1u,5u,10u,0u,0u,0u,0u,0u,0u,0u,0u,0u,0u,0u,
2u,0u,8u,4u,1u,0u,0u,0u,0u,0u,0u,0u,0u,0u,0u,0u,
2u,0u,8u,4u,7u,0u,0u,0u,0u,0u,0u,0u,0u,0u,0u,0u,
2u,0u,8u,4u,13u,0u,0u,0u,0u,0u,0u,0u,0u,0u,0u,0u,
2u,1u,8u,4u,3u,0u,0u,0u,0u,0u,0u,0u,0u,0u,0u,0u,
2u,1u,8u,4u,9u,0u,0u,0u,0u,0u,0u,0u,0u,0u,0u,0u,
2u,1u,8u,4u,15u,0u,0u,0u,0u,0u,0u,0u,0u,0u,0u,0u,
2u,2u,8u,4u,5u,0u,0u,0u,0u,0u,0u,0u,0u,0u,0u,0u,
2u,2u,8u,4u,11u,0u,0u,0u,0u,0u,0u,0u,0u,0u,0u,0u,
2u,0u,6u,3u,2u,0u,0u,0u,0u,0u,0u,0u,0u,0u,0u,0u,
2u,0u,6u,3u,8u,0u,0u,0u,0u,0u,0u,0u,0u,0u,0u,0u,
2u,0u,6u,3u,14u,0u,0u,0u,0u,0u,0u,0u,0u,0u,0u,0u,
2u,1u,6u,3u,4u,0u,0u,0u,0u,0u,0u,0u,0u,0u,0u,0u,
2u,1u,6u,3u,10u,0u,0u,0u,0u,0u,0u,0u,0u,0u,0u,0u,
2u,2u,6u,3u,0u,0u,0u,0u,0u,0u,0u,0u,0u,0u,0u,0u,
2u,2u,6u,3u,6u,0u,0u,0u,0u,0u,0u,0u,0u,0u,0u,0u,
2u,2u,6u,3u,12u,0u,0u,0u,0u,0u,0u,0u,0u,0u,0u,0u,
2u,0u,4u,2u,3u,0u,0u,0u,0u,0u,0u,0u,0u,0u,0u,0u,
2u,0u,4u,2u,9u,0u,0u,0u,0u,0u,0u,0u,0u,0u,0u,0u,
2u,0u,4u,2u,15u,0u,0u,0u,0u,0u,0u,0u,0u,0u,0u,0u,
2u,1u,4u,2u,5u,0u,0u,0u,0u,0u,0u,0u,0u,0u,0u,0u,
2u,1u,4u,2u,11u,0u,0u,0u,0u,0u,0u,0u,0u,0u,0u,0u,
2u,2u,4u,2u,1u,0u,0u,0u,0u,0u,0u,0u,0u,0u,0u,0u,
2u,2u,4u,2u,7u,0u,0u,0u,0u,0u,0u,0u,0u,0u,0u,0u,
2u,2u,4u,2u,13u,0u,0u,0u,0u,0u,0u,0u,0u,0u,0u,0u,
2u,0u,2u,1u,4u,0u,0u,0u,0u,0u,0u,0u,0u,0u,0u,0u,
2u,0u,2u,1u,10u,0u,0u,0u,0u,0u,0u,0u,0u,0u,0u,0u,
2u,1u,2u,1u,0u,0u,0u,0u,0u,0u,0u,0u,0u,0u,0u,0u,
2u,1u,2u,1u,6u,0u,0u,0u,0u,0u,0u,0u,0u,0u,0u,0u,
2u,1u,2u,1u,12u,0u,0u,0u,0u,0u,0u,0u,0u,0u,0u,0u,
2u,2u,2u,1u,2u,0u,0u,0u,0u,0u,0u,0u,0u,0u,0u,0u,
2u,2u,2u,1u,8u,0u,0u,0u,0u,0u,0u,0u,0u,0u,0u,0u,
2u,2u,2u,1u,14u,0u,0u,0u,0u,0u,0u,0u,0u,0u,0u,0u,
2u,0u,0u,0u,5u,0u,0u,0u,0u,0u,0u,0u,0u,0u,0u,0u,
2u,0u,0u,0u,11u,0u,0u,0u,0u,0u,0u,0u,0u,0u,0u,0u,
2u,1u,0u,0u,1u,0u,0u,0u,0u,0u,0u,0u,0u,0u,0u,0u,
2u,1u,0u,0u,7u,0u,0u,0u,0u,0u,0u,0u,0u,0u,0u,0u,
2u,1u,0u,0u,13u,0u,0u,0u,0u,0u,0u,0u,0u,0u,0u,0u,
2u,2u,0u,0u,3u,0u,0u,0u,0u,0u,0u,0u,0u,0u,0u,0u,
2u,2u,0u,0u,9u,0u,0u,0u,0u,0u,0u,0u,0u,0u,0u,0u,
2u,2u,0u,0u,15u,0u,0u,0u,0u,0u,0u,0u,0u,0u,0u,0u,
3u,0u,3u,0u,3u,0u,3u,0u,3u,0u,3u,0u,3u,0u,3u,0u,
3u,0u,3u,0u,3u,0u,3u,0u,3u,0u,3u,0u,3u,0u,3u,0u,
3u,0u,3u,0u,3u,0u,3u,0u,3u,0u,3u,0u,3u,0u,3u,0u,
3u,0u,3u,0u,3u,0u,3u,0u,3u,0u,3u,0u,3u,0u,3u,0u,
3u,0u,3u,0u,3u,0u,3u,0u,3u,0u,3u,0u,3u,0u,3u,0u,
3u,0u,3u,0u,3u,0u,3u,0u,3u,0u,3u,0u,3u,0u,3u,0u,
3u,0u,3u,0u,3u,0u,3u,0u,3u,0u,3u,0u,3u,0u,3u,0u,
224u,0u,3u,0u,226u,0u,3u,0u,228u,0u,3u,0u,230u,0u,3u,0u,
3u,0u,3u,0u,3u,0u,3u,0u,3u,0u,3u,0u,3u,0u,3u,0u,
3u,0u,3u,0u,3u,0u,3u,0u,3u,0u,3u,0u,3u,0u,3u,0u,
3u,0u,3u,0u,3u,0u,3u,0u,3u,0u,3u,0u,3u,0u,3u,0u,
3u,0u,3u,0u,3u,0u,3u,0u,3u,0u,3u,0u,3u,0u,3u,0u,
3u,0u,3u,0u,3u,0u,3u,0u,3u,0u,3u,0u,3u,0u,3u,0u,
3u,0u,3u,0u,3u,0u,3u,0u,3u,0u,3u,0u,3u,0u,3u,0u,
3u,0u,3u,0u,3u,0u,3u,0u,3u,0u,3u,0u,3u,0u,3u,0u,
3u,0u,3u,0u,3u,0u,3u,0u,3u,0u,3u,0u,3u,0u,3u,0u,
3u,0u,3u,0u,3u,0u,3u,0u,3u,0u,3u,0u,3u,0u,3u,0u,
3u,0u,3u,0u,3u,0u,3u,0u,3u,0u,3u,0u,3u,0u,3u,0u,
3u,0u,3u,0u,3u,0u,3u,0u,3u,0u,3u,0u,3u,0u,3u,0u,
3u,0u,3u,0u,3u,0u,3u,0u,3u,0u,3u,0u,3u,0u,3u,0u,
3u,0u,3u,0u,3u,0u,3u,0u,3u,0u,3u,0u,3u,0u,3u,0u,
3u,0u,3u,0u,3u,0u,3u,0u,3u,0u,3u,0u,3u,0u,3u,0u,
3u,0u,3u,0u,3u,0u,3u,0u,3u,0u,3u,0u,3u,0u,3u,0u,
3u,0u,3u,0u,3u,0u,3u,0u,3u,0u,3u,0u,3u,0u,3u,0u,
3u,0u,3u,0u,3u,0u,3u,0u,3u,0u,3u,0u,3u,0u,3u,0u,
3u,0u,3u,0u,3u,0u,3u,0u,3u,0u,3u,0u,3u,0u,3u,0u,
3u,0u,3u,0u,3u,0u,3u,0u,3u,0u,3u,0u,3u,0u,3u,0u,
3u,0u,3u,0u,3u,0u,3u,0u,3u,0u,3u,0u,3u,0u,3u,0u,
3u,0u,3u,0u,3u,0u,3u,0u,3u,0u,3u,0u,3u,0u,3u,0u,
3u,0u,3u,0u,3u,0u,3u,0u,3u,0u,3u,0u,3u,0u,3u,0u,
3u,0u,3u,0u,3u,0u,3u,0u,3u,0u,3u,0u,3u,0u,3u,0u,
3u,0u,3u,0u,3u,0u,3u,0u,3u,0u,3u,0u,3u,0u,3u,0u};

__global__ __launch_bounds__(512, 2) void fused_k(Args args) {
  __shared__ __attribute__((aligned(1024))) char smem[163840];
  asm volatile(
".macro M_ d,a,b\nv_mfma_f32_16x16x32_bf16 v[\\d:\\d+3],v[\\a:\\a+3],v[\\b:\\b+3],v[\\d:\\d+3]\n.endm\n.macro Z_ d,a,b\nv_mfma_f32_16x16x32_bf16 v[\\d:\\d+3],v[\\a:\\a+3],v[\\b:\\b+3],0\n.endm\ns_load_dwordx16 s[8:23],%0,0\ns_mov_b64 s[98:99],%0\n"
"s_mov_b32 s95,%1\ns_mov_b64 s[72:73],%4\nv_lshrrev_b32 v246,6,v0\nv_and_b32 v0,63,v0\nv_readfirstlane_b32 s76,v246\nv_lshlrev_b32 v224,4,v0\nv_lshrrev_b32 v244,3,v0\nv_and_b32 v245,7,v0\n"
"s_lshl_b32 s77,s76,10\nv_and_b32 v246,1,v246\nv_lshlrev_b32 v246,2,v246\nv_xor_b32 v246,v246,v245\nv_and_b32 v247,2,v244\nv_xor_b32 v247,v247,v246\nv_lshlrev_b32 v247,4,v247\nv_mul_u32_u24 v238,9216,v244\n"
"v_add_u32 v238,v238,v247\nv_lshrrev_b32 v247,1,v244\nv_xor_b32 v247,v247,v246\nv_lshlrev_b32 v247,4,v247\nv_mul_u32_u24 v241,1152,v244\nv_add_u32 v241,v241,v247\ns_getpc_b64 s[2:3]\ns_lshr_b32 s6,s95,3\n"
"s_lshl_b32 s6,s6,11\ns_lshl_b32 s7,s76,8\ns_add_u32 s6,s6,s7\ns_min_u32 s6,s6,.end_%=-.ssm_%=+.ssm_%=-.beg_%=-256\n.beg_%=:\nv_lshl_add_u32 v246,v0,7,s6\ns_mov_b64 exec,3\nglobal_load_dword v1,v246,s[2:3]\n"
"s_mov_b64 exec,-1\ns_lshr_b32 s3,s95,3\ns_mov_b32 s5,0x2208210\ns_bitcmp1_b32 s95,0\ns_cmov_b32 s5,0x2082084\ns_lshr_b32 s5,s5,s3\ns_cbranch_scc0 .ssm_%=\ns_and_b32 s0,s76,3\n"
"s_lshr_b32 s1,s76,2\ns_bcnt1_i32_b32 s5,s5\ns_and_b32 s2,s95,7\ns_mul_i32 s2,s2,1152\ns_lshr_b32 s2,s2,8\ns_sub_u32 s2,s2,s5\ns_add_u32 s2,s2,5\ns_lshl_b32 s83,s2,8\n"
"s_lshl_b32 s4,s95,4\ns_and_b32 s4,s4,16\ns_add_u32 s4,s4,s95\ns_and_b32 s4,s4,56\ns_lshr_b32 s3,s4,1\ns_lshr_b32 s82,0x52741630,s3\ns_and_b32 s82,s82,15\ns_mul_i32 s80,s82,589824\n"
"s_mul_i32 s2,s0,73728\ns_add_u32 s80,s80,s2\ns_lshl_b32 s2,s1,7\ns_add_u32 s2,s2,s83\ns_add_u32 s80,s80,s2\ns_lshl_b32 s2,s1,1\ns_add_u32 s2,s2,s95\ns_cmp_eq_u32 s5,5\n"
"s_cselect_b32 s3,6,7\ns_and_b32 s2,s2,s3\ns_or_b32 s2,s2,s4\ns_mul_i32 s81,s2,36864\ns_mul_i32 s2,s0,9216\ns_add_u32 s81,s81,s2\ns_lshl_b32 s2,s82,7\ns_add_u32 s81,s81,s2\n"
"s_mov_b32 s84,s82\ns_waitcnt lgkmcnt(0)\ns_add_u32 m0,s77,73728\ns_nop 0\nbuffer_load_dwordx4 v238,s[16:19],s80 offen lds\ns_add_u32 m0,s77,81920\ns_nop 0\nbuffer_load_dwordx4 v238,s[20:23],s80 offen lds\n"
"s_add_u32 m0,s77,36864\ns_nop 0\nbuffer_load_dwordx4 v241,s[8:11],s81 offen lds\ns_add_u32 s84,s84,1\ns_cmp_eq_u32 s84,9\ns_cselect_b32 s3,0xfffffb80,0\ns_add_u32 s81,s81,128\ns_add_u32 s81,s81,s3\n"
"v_lshrrev_b32 v105,3,v224\ns_lshl_b32 s7,s95,3\ns_add_u32 s7,s7,s76\ns_mul_i32 s7,s7,1152\ns_waitcnt lgkmcnt(0)\ns_lshl_b32 s6,s95,6\ns_load_dwordx16 s[56:71],s[72:73],s6\ns_lshl_b32 s6,s95,3\n"
"s_add_u32 s6,s6,16384\ns_load_dwordx2 s[100:101],s[72:73],s6\ns_load_dwordx16 s[24:39],s[98:99],64\ns_load_dwordx16 s[40:55],s[98:99],128\nbuffer_load_dwordx4 v[96:99],v224,s[8:11],s7 offen\nbuffer_load_ushort v100,v105,s[8:11],s7 offen offset:1024\nbuffer_load_dword v104,off,s[12:15],0 sc1\ns_add_u32 m0,s77,90112\n"
"s_add_u32 s83,s80,294912\nbuffer_load_dwordx4 v238,s[16:19],s83 offen lds\ns_add_u32 m0,s77,98304\ns_nop 0\nbuffer_load_dwordx4 v238,s[20:23],s83 offen lds\ns_add_u32 s82,s82,1\ns_cmp_eq_u32 s82,9\ns_cselect_b32 s3,0xffaf0000,0\n"
"s_add_u32 s80,s80,589824\ns_add_u32 s80,s80,s3\nv_and_b32 v246,15,v0\nv_bfe_u32 v247,v0,4,2\nv_lshrrev_b32 v248,2,v246\nv_lshl_add_u32 v248,v247,3,v248\nv_lshlrev_b32 v248,7,v248\nv_and_b32 v249,3,v0\n"
"v_lshl_add_u32 v250,v249,3,v248\nv_bfe_u32 v251,v0,3,2\nv_xor_b32 v249,s0,v251\nv_lshl_add_u32 v248,v249,5,v250\ns_lshl_b32 s2,s1,12\ns_add_u32 s5,s2,73728\nv_add_u32 v239,s5,v248\nv_add_u32 v240,65536,v239\n"
"s_mul_i32 s2,s0,3\ns_add_u32 s2,s2,0\ns_and_b32 s4,s2,3\ns_lshr_b32 s2,s2,2\ns_lshl_b32 s2,s2,13\ns_add_u32 s2,s2,s5\nv_xor_b32 v248,s4,v251\nv_lshl_add_u32 v248,v248,5,v250\n"
"v_add_u32 v242,s2,v248\ns_mul_i32 s2,s0,3\ns_add_u32 s2,s2,1\ns_and_b32 s4,s2,3\ns_lshr_b32 s2,s2,2\ns_lshl_b32 s2,s2,13\ns_add_u32 s2,s2,s5\nv_xor_b32 v248,s4,v251\n"
"v_lshl_add_u32 v248,v248,5,v250\nv_add_u32 v243,s2,v248\ns_mul_i32 s2,s0,3\ns_add_u32 s2,s2,2\ns_and_b32 s4,s2,3\ns_lshr_b32 s2,s2,2\ns_lshl_b32 s2,s2,13\ns_add_u32 s2,s2,s5\n"
"v_xor_b32 v248,s4,v251\nv_lshl_add_u32 v248,v248,5,v250\nv_add_u32 v253,s2,v248\ns_add_u32 m0,s77,106496\ns_nop 0\nbuffer_load_dwordx4 v238,s[16:19],s80 offen lds\nv_lshrrev_b32 v248,1,v246\nv_xor_b32 v248,v248,v247\n"
"v_lshlrev_b32 v248,4,v248\nv_lshl_add_u32 v248,v246,7,v248\nv_add_u32 v237,36864,v248\nv_xor_b32 v226,64,v237\ns_waitcnt lgkmcnt(0)\ns_mov_b64 s[48:49],s[100:101]\nv_lshrrev_b32 v248,4,v0\nv_lshlrev_b32 v183,4,v248\n"
"v_xor_b32 v248,v248,v0\nv_bfe_i32 v248,v248,1,1\nv_not_b32 v248,v248\nv_and_b32 v220,0x3f803f80,v248\nv_mov_b32 v221,v220\nv_mov_b32 v222,v220\nv_mov_b32 v223,v220\nv_and_b32 v248,1,v0\n"
"v_mul_u32_u24 v248,0xaaaa,v248\nv_add_u32 v182,0x4444,v248\nv_mov_b32 v216,0\nv_mov_b32 v217,0\nv_mov_b32 v218,0\nv_mov_b32 v219,0\ns_add_u32 s2,s77,147456\nv_add_u32 v183,s2,v183\n"
"v_lshl_add_u32 v181,v246,2,s2\ns_add_u32 s2,s76,s58\ns_sub_u32 s7,s2,9\ns_cselect_b32 s2,s2,s7\ns_lshl_b32 s2,s2,7\nv_lshrrev_b32 v248,1,v244\nv_xor_b32 v248,v248,v245\nv_lshlrev_b32 v248,4,v248\n"
"v_mul_u32_u24 v249,1152,v244\nv_add3_u32 v227,v248,v249,s2\nv_xor_b32 v228,64,v227\nv_add_u32 v228,8192,v228\ns_lshl_b32 s2,s0,5\nv_xor_b32 v248,s2,v248\ns_add_u32 s2,8,s58\ns_sub_u32 s7,s2,9\n"
"s_cselect_b32 s2,s2,s7\ns_lshl_b32 s2,s2,7\ns_mul_i32 s4,s76,4608\ns_add_u32 s2,s2,s4\nv_add3_u32 v229,v248,v249,s2\nv_and_b32 v248,127,v238\nv_add_u32 v254,v248,v249\ns_add_u32 m0,s77,114688\n"
"s_nop 0\nbuffer_load_dwordx4 v238,s[20:23],s80 offen lds\ns_lshl_b32 s78,s76,11\ns_mul_i32 s2,s1,18432\ns_lshl_b32 s4,s0,9\ns_add_u32 s2,s2,s4\ns_add_u32 s2,s2,16384\ns_sub_u32 s79,s2,s78\n"
"s_mov_b64 s[72:73],s[8:9]\ns_mov_b64 s[74:75],s[10:11]\ns_lshr_b32 s2,s57,6\ns_lshr_b32 s5,s76,1\ns_add_u32 s2,s2,s5\ns_mul_i32 s5,s2,57\ns_lshr_b32 s5,s5,9\ns_mul_i32 s4,s5,9\n"
"s_sub_u32 s2,s2,s4\ns_lshl_b32 s2,s2,11\ns_lshr_b32 s4,s5,1\ns_mul_i32 s4,s4,36864\ns_add_u32 s2,s2,s4\ns_and_b32 s5,s5,1\ns_lshl_b32 s4,s5,10\ns_add_u32 s2,s2,s4\n"
"s_and_b32 s4,s76,1\ns_lshl_b32 s4,s4,9\ns_add_u32 s2,s2,s4\ns_add_u32 s2,s2,1048576\ns_xor_b32 s5,s5,1\nv_and_b32 v248,1,v247\nv_xor_b32 v248,s5,v248\nv_mul_u32_u24 v248,18432,v248\n"
"v_lshrrev_b32 v249,1,v247\nv_lshl_add_u32 v248,v249,8,v248\nv_lshl_add_u32 v248,v246,4,v248\nv_add_u32 v230,s2,v248\ns_add_u32 m0,s77,45056\ns_nop 0\nbuffer_load_dwordx4 v241,s[8:11],s81 offen lds\ns_add_u32 s84,s84,1\n"
"s_cmp_eq_u32 s84,9\ns_cselect_b32 s3,0xfffffb80,0\ns_add_u32 s81,s81,128\ns_add_u32 s81,s81,s3\ns_lshr_b32 s2,s57,2\ns_lshl_b32 s4,s76,3\ns_add_u32 s2,s2,s4\ns_lshl_b32 s2,s2,3\n"
"s_add_u32 s50,s2,53100544\nv_lshlrev_b32 v248,4,v246\nv_add_u32 v231,156160,v248\ns_bfe_u32 s101,s70,262152\ns_mul_i32 s2,s101,2304\ns_add_u32 s2,s2,53100544\nv_lshrrev_b32 v248,1,v224\nv_cmp_gt_u32 vcc,288,v248\n"
"s_nop 1\nv_cndmask_b32 v248,0,v248,vcc\nv_lshl_add_u32 v236,v248,3,s2\ns_mov_b32 s90,36864\ns_mov_b32 s91,0\ns_mov_b64 s[92:93],0\ns_bfe_u32 s87,s60,524296\ns_bfe_u32 s86,s60,524304\n"
"s_lshr_b32 s85,s60,24\ns_mov_b32 s88,64\ns_cmp_lt_u32 s76,4\ns_cselect_b64 s[6:7],s[60:61],s[62:63]\ns_lshl_b32 s4,s0,4\ns_lshr_b64 s[6:7],s[6:7],s4\nv_bfe_u32 v248,v0,5,1\nv_lshlrev_b32 v248,3,v248\n"
"v_lshrrev_b32 v248,v248,s6\nv_and_b32 v248,255,v248\nv_and_b32 v249,511,v224\nv_lshl_add_u32 v252,v248,9,v249\nv_add_u32 v252,4096,v252\ns_add_u32 m0,s77,122880\ns_add_u32 s83,s80,294912\nbuffer_load_dwordx4 v238,s[16:19],s83 offen lds\n"
"s_bfe_u32 s51,s70,262144\ns_bfe_u32 s2,s70,131076\ns_mul_i32 s100,s2,147456\ns_mul_i32 s2,s2,384\ns_lshl_b32 s4,s101,1\ns_add_u32 s4,s4,s1\ns_mul_i32 s4,s4,331776\ns_add_u32 s2,s2,s4\n"
"s_mul_i32 s4,s0,9216\ns_add_u32 s61,s2,s4\ns_mul_i32 s4,s51,36864\ns_add_u32 s94,s61,s4\ns_mul_i32 s2,s76,18432\ns_add_u32 s71,s71,s2\ns_add_u32 s100,s100,s2\ns_mul_i32 s2,s101,2048\n"
"s_add_u32 s100,s100,s2\ns_add_u32 s100,s100,19922944\ns_add_u32 s52,s77,73728\ns_mov_b32 s55,0\ns_add_u32 s53,s52,73728\ns_add_u32 m0,s77,131072\ns_nop 0\nbuffer_load_dwordx4 v238,s[20:23],s83 offen lds\n"
"s_add_u32 s82,s82,1\ns_cmp_eq_u32 s82,9\ns_cselect_b32 s3,0xffaf0000,0\ns_add_u32 s80,s80,589824\ns_add_u32 s80,s80,s3\ns_waitcnt vmcnt(10)\ns_barrier\nds_read_b64_tr_b16 v[0:1],v239\n"
"ds_read_b64_tr_b16 v[2:3],v239 offset:512\nds_read_b64_tr_b16 v[4:5],v239 offset:8192\nds_read_b64_tr_b16 v[6:7],v239 offset:8704\nds_read_b128 v[184:187],v237 offset:4096\nds_read_b128 v[188:191],v237\nds_read_b128 v[192:195],v237 offset:2048\nds_read_b128 v[196:199],v237 offset:6144\ns_waitcnt lgkmcnt(3)\n"
"Z_ 144,0,184\ns_add_u32 m0,s77,139264\ns_nop 0\nbuffer_load_dwordx4 v238,s[16:19],s80 offen lds\ns_waitcnt vmcnt(8)\nv_readfirstlane_b32 s96,v104\nv_lshlrev_b32 v120,16,v100\nZ_ 148,4,184\n"
"s_waitcnt vmcnt(6)\ns_barrier\nv_lshlrev_b32 v112,16,v96\nv_readfirstlane_b32 s97,v120\ns_waitcnt lgkmcnt(2)\nZ_ 160,0,188\nds_read_b64_tr_b16 v[8:9],v239 offset:16384\nds_read_b64_tr_b16 v[10:11],v239 offset:16896\n"
"v_and_b32 v113,0xffff0000,v96\nv_lshlrev_b32 v114,16,v97\nZ_ 164,4,188\ns_add_u32 m0,s77,73728\ns_add_u32 s83,s80,294912\nbuffer_load_dwordx4 v238,s[16:19],s83 offen lds\nds_read_b64_tr_b16 v[12:13],v239 offset:24576\nds_read_b64_tr_b16 v[14:15],v239 offset:25088\n"
"v_and_b32 v115,0xffff0000,v97\nv_lshlrev_b32 v116,16,v98\nv_smfmac_f32_16x16x64_bf16 v[216:219],v[220:223],v[0:7],v182\ns_add_u32 m0,s77,147456\ns_nop 0\nbuffer_load_dwordx4 v238,s[20:23],s80 offen lds\nds_read_b128 v[200:203],v226 offset:4096\nds_read_b128 v[204:207],v226\n"
"v_and_b32 v117,0xffff0000,v98\nv_lshlrev_b32 v118,16,v99\ns_waitcnt lgkmcnt(7)\nZ_ 168,0,192\nds_read_b128 v[184:187],v226 offset:2048\nds_read_b128 v[188:191],v226 offset:6144\nv_and_b32 v119,0xffff0000,v99\nv_subrev_f32 v112,s97,v112\n"
"Z_ 172,4,192\ns_add_u32 m0,s77,81920\ns_nop 0\nbuffer_load_dwordx4 v238,s[20:23],s83 offen lds\ns_add_u32 s82,s82,1\ns_cmp_eq_u32 s82,9\ns_cselect_b32 s3,0xffaf0000,0\ns_add_u32 s80,s80,589824\n"
"s_add_u32 s80,s80,s3\nv_subrev_f32 v113,s97,v113\nv_subrev_f32 v114,s97,v114\nv_subrev_f32 v115,s97,v115\nv_subrev_f32 v116,s97,v116\ns_waitcnt lgkmcnt(8)\nZ_ 152,0,196\nv_subrev_f32 v117,s97,v117\n"
"v_subrev_f32 v118,s97,v118\nZ_ 156,4,196\ns_add_u32 m0,s77,53248\ns_nop 0\nbuffer_load_dwordx4 v241,s[8:11],s81 offen lds\ns_add_u32 s84,s84,1\ns_cmp_eq_u32 s84,9\ns_cselect_b32 s3,0xfffffb80,0\n"
"s_add_u32 s81,s81,128\ns_add_u32 s81,s81,s3\nv_subrev_f32 v119,s97,v119\nv_subrev_f32 v120,s97,v120\ns_waitcnt lgkmcnt(3)\nM_ 144,8,200\nv_add_f32 v108,v112,v113\nv_mul_f32 v110,v112,v112\n"
"M_ 148,12,200\ns_waitcnt vmcnt(7)\ns_barrier\nv_add_f32 v109,v114,v115\nv_mul_f32 v111,v113,v113\ns_waitcnt lgkmcnt(2)\nM_ 160,8,204\nds_read_b64_tr_b16 v[16:17],v239 offset:32768\n"
"ds_read_b64_tr_b16 v[18:19],v239 offset:33280\nv_fmac_f32 v110,v114,v114\nv_fmac_f32 v111,v115,v115\nM_ 164,12,204\ns_add_u32 m0,s77,90112\ns_nop 0\nbuffer_load_dwordx4 v238,s[16:19],s80 offen lds\nds_read_b64_tr_b16 v[20:21],v239 offset:40960\n"
"ds_read_b64_tr_b16 v[22:23],v239 offset:41472\nv_add_f32 v108,v108,v116\nv_fmac_f32 v110,v116,v116\nv_smfmac_f32_16x16x64_bf16 v[216:219],v[220:223],v[8:15],v182\nds_read_b128 v[192:195],v237 offset:12288\nds_read_b128 v[196:199],v237 offset:8192\nv_add_f32 v109,v109,v117\nv_fmac_f32 v111,v117,v117\n"
"s_waitcnt lgkmcnt(7)\nM_ 168,8,184\nds_read_b128 v[200:203],v237 offset:10240\nds_read_b128 v[204:207],v237 offset:14336\nv_add_f32 v108,v108,v118\nv_fmac_f32 v110,v118,v118\nM_ 172,12,184\ns_add_u32 m0,s77,98304\n"
"s_nop 0\nbuffer_load_dwordx4 v238,s[20:23],s80 offen lds\nv_add_f32 v109,v109,v119\nv_fmac_f32 v111,v119,v119\nv_add_f32 v108,v108,v120\nv_fmac_f32 v110,v120,v120\ns_waitcnt lgkmcnt(8)\nM_ 152,8,188\n"
"s_add_u32 m0,s77,61440\ns_nop 0\nbuffer_load_dwordx4 v241,s[8:11],s81 offen lds\ns_add_u32 s84,s84,1\ns_cmp_eq_u32 s84,9\ns_cselect_b32 s3,0xfffffb80,0\ns_add_u32 s81,s81,128\ns_add_u32 s81,s81,s3\n"
"v_add_f32 v108,v108,v109\nv_add_f32 v110,v110,v111\nM_ 156,12,188\nv_mov_b32 v97,s96\nv_add_f32_dpp v108,v108,v108 quad_perm:[1,0,3,2] row_mask:0xf bank_mask:0xf\ns_waitcnt lgkmcnt(3)\nM_ 144,16,192\nv_add_f32_dpp v110,v110,v110 quad_perm:[1,0,3,2] row_mask:0xf bank_mask:0xf\n"
"v_add_u32 v97,1,v97\nM_ 148,20,192\ns_waitcnt vmcnt(8)\ns_barrier\nv_add_f32_dpp v108,v108,v108 quad_perm:[2,3,0,1] row_mask:0xf bank_mask:0xf\nv_add_f32_dpp v110,v110,v110 quad_perm:[2,3,0,1] row_mask:0xf bank_mask:0xf\ns_waitcnt lgkmcnt(2)\nM_ 160,16,196\n"
"ds_read_b64_tr_b16 v[24:25],v239 offset:49152\nds_read_b64_tr_b16 v[26:27],v239 offset:49664\nv_add_u32 v99,0x10000,v97\nv_add_f32_dpp v108,v108,v108 row_half_mirror row_mask:0xf bank_mask:0xf\nM_ 164,20,196\ns_add_u32 m0,s77,106496\ns_add_u32 s83,s80,294912\nbuffer_load_dwordx4 v238,s[16:19],s83 offen lds\n"
"ds_read_b64_tr_b16 v[28:29],v239 offset:57344\nds_read_b64_tr_b16 v[30:31],v239 offset:57856\nv_add_f32_dpp v110,v110,v110 row_half_mirror row_mask:0xf bank_mask:0xf\nv_add_f32_dpp v108,v108,v108 row_mirror row_mask:0xf bank_mask:0xf\nv_smfmac_f32_16x16x64_bf16 v[216:219],v[220:223],v[16:23],v182\nds_read_b128 v[184:187],v226 offset:12288\nds_read_b128 v[188:191],v226 offset:8192\nv_add_f32_dpp v110,v110,v110 row_mirror row_mask:0xf bank_mask:0xf\n"
"v_add_f32_dpp v108,v108,v108 row_bcast:15 row_mask:0xa bank_mask:0xf\ns_waitcnt lgkmcnt(7)\nM_ 168,16,200\nds_read_b128 v[192:195],v226 offset:10240\nds_read_b128 v[196:199],v226 offset:14336\nv_add_f32_dpp v110,v110,v110 row_bcast:15 row_mask:0xa bank_mask:0xf\nv_add_f32_dpp v108,v108,v108 row_bcast:31 row_mask:0xc bank_mask:0xf\nM_ 172,20,200\n"
"s_add_u32 m0,s77,114688\ns_nop 0\nbuffer_load_dwordx4 v238,s[20:23],s83 offen lds\ns_add_u32 s82,s82,1\ns_cmp_eq_u32 s82,9\ns_cselect_b32 s3,0xffaf0000,0\ns_add_u32 s80,s80,589824\ns_add_u32 s80,s80,s3\n"
"v_add_f32_dpp v110,v110,v110 row_bcast:31 row_mask:0xc bank_mask:0xf\nv_mul_f32 v108,0x3ae38e39,v108\nv_mul_f32 v110,0x3ae38e39,v110\nv_fma_f32 v110,-v108,v108,v110\ns_waitcnt lgkmcnt(8)\nM_ 152,16,204\nv_add_f32 v108,s97,v108\nv_max_f32 v110,0,v110\n"
"s_add_u32 s97,s96,65537\ns_add_u32 s96,s96,1\nM_ 156,20,204\nv_cvt_pk_bf16_f32 v96,v108,v108\nv_lshlrev_b32 v96,16,v96\ns_waitcnt lgkmcnt(3)\nM_ 144,24,184\nv_cvt_pk_bf16_f32 v110,v110,v110\n"
"v_lshlrev_b32 v110,16,v110\nM_ 148,28,184\ns_waitcnt vmcnt(5)\ns_barrier\nv_add_f32 v110,0x3727c5ac,v110\nv_cvt_pk_bf16_f32 v110,v110,v110\ns_waitcnt lgkmcnt(2)\nM_ 160,24,188\n"
"ds_read_b64_tr_b16 v[32:33],v240\nds_read_b64_tr_b16 v[34:35],v240 offset:512\nv_lshlrev_b32 v110,16,v110\nM_ 164,28,188\ns_add_u32 m0,s77,122880\ns_nop 0\nbuffer_load_dwordx4 v238,s[16:19],s80 offen lds\nds_read_b64_tr_b16 v[36:37],v240 offset:8192\n"
"ds_read_b64_tr_b16 v[38:39],v240 offset:8704\nv_sqrt_f32 v110,v110\ns_nop 0\nv_cvt_pk_bf16_f32 v110,v110,v110\nv_smfmac_f32_16x16x64_bf16 v[216:219],v[220:223],v[24:31],v182\nds_read_b128 v[200:203],v237 offset:20480\nds_read_b128 v[204:207],v237 offset:16384\nv_lshlrev_b32 v110,16,v110\n"
"s_waitcnt lgkmcnt(7)\nM_ 168,24,192\nds_read_b128 v[184:187],v237 offset:18432\nds_read_b128 v[188:191],v237 offset:22528\nv_rcp_f32 v98,v110\ns_lshl_b32 s7,s95,3\ns_add_u32 s7,s7,s76\ns_lshl_b32 s7,s7,4\n"
"s_add_u32 s7,s7,3088\ns_bfm_b64 exec,1,63\nbuffer_store_dwordx4 v[96:99],v224,s[12:15],s7 offen sc1\ns_mov_b64 exec,-1\nv_mov_b32 v234,s96\nM_ 172,28,192\ns_add_u32 m0,s77,131072\ns_nop 0\n"
"buffer_load_dwordx4 v238,s[20:23],s80 offen lds\nv_mov_b32 v235,s97\ns_waitcnt lgkmcnt(8)\nM_ 152,24,196\ns_add_u32 m0,s77,36864\ns_nop 0\nbuffer_load_dwordx4 v241,s[8:11],s81 offen lds\ns_add_u32 s84,s84,1\n"
"s_cmp_eq_u32 s84,9\ns_cselect_b32 s3,0xfffffb80,0\ns_add_u32 s81,s81,128\ns_add_u32 s81,s81,s3\nM_ 156,28,196\ns_waitcnt lgkmcnt(3)\nM_ 144,32,200\nM_ 148,36,200\n"
"s_barrier\ns_waitcnt lgkmcnt(2)\nM_ 160,32,204\nds_read_b64_tr_b16 v[40:41],v239\nds_read_b64_tr_b16 v[42:43],v239 offset:512\nM_ 164,36,204\ns_add_u32 m0,s77,139264\ns_add_u32 s83,s80,294912\n"
"buffer_load_dwordx4 v238,s[16:19],s83 offen lds\nds_read_b64_tr_b16 v[44:45],v239 offset:8192\nds_read_b64_tr_b16 v[46:47],v239 offset:8704\nv_smfmac_f32_16x16x64_bf16 v[216:219],v[220:223],v[32:39],v182\nds_read_b128 v[192:195],v226 offset:20480\nds_read_b128 v[196:199],v226 offset:16384\ns_waitcnt lgkmcnt(7)\nM_ 168,32,184\n"
"ds_read_b128 v[200:203],v226 offset:18432\nds_read_b128 v[204:207],v226 offset:22528\nM_ 172,36,184\ns_add_u32 m0,s77,147456\ns_nop 0\nbuffer_load_dwordx4 v238,s[20:23],s83 offen lds\ns_add_u32 s82,s82,1\ns_cmp_eq_u32 s82,9\n"
"s_cselect_b32 s3,0xffaf0000,0\ns_add_u32 s80,s80,589824\ns_add_u32 s80,s80,s3\ns_waitcnt lgkmcnt(8)\nM_ 152,32,188\nM_ 156,36,188\ns_waitcnt lgkmcnt(3)\nM_ 144,40,192\n"
"M_ 148,44,192\ns_waitcnt vmcnt(8)\ns_barrier\ns_waitcnt lgkmcnt(2)\nM_ 160,40,196\nds_read_b64_tr_b16 v[48:49],v239 offset:16384\nds_read_b64_tr_b16 v[50:51],v239 offset:16896\nM_ 164,44,196\n"
"s_add_u32 m0,s77,73728\ns_nop 0\nbuffer_load_dwordx4 v238,s[16:19],s80 offen lds\nds_read_b64_tr_b16 v[52:53],v239 offset:24576\nds_read_b64_tr_b16 v[54:55],v239 offset:25088\nv_smfmac_f32_16x16x64_bf16 v[216:219],v[220:223],v[40:47],v182\nds_read_b128 v[184:187],v237 offset:28672\nds_read_b128 v[188:191],v237 offset:24576\n"
"s_waitcnt lgkmcnt(7)\nM_ 168,40,200\nds_read_b128 v[192:195],v237 offset:26624\nds_read_b128 v[196:199],v237 offset:30720\nM_ 172,44,200\ns_add_u32 m0,s77,81920\ns_nop 0\nbuffer_load_dwordx4 v238,s[20:23],s80 offen lds\n"
"s_waitcnt lgkmcnt(8)\nM_ 152,40,204\ns_add_u32 m0,s77,45056\ns_nop 0\nbuffer_load_dwordx4 v241,s[8:11],s81 offen lds\ns_add_u32 s84,s84,1\ns_cmp_eq_u32 s84,9\ns_cselect_b32 s3,0xfffffb80,0\n"
"s_add_u32 s81,s81,128\ns_add_u32 s81,s81,s3\nM_ 156,44,204\ns_waitcnt lgkmcnt(3)\nM_ 144,48,184\nM_ 148,52,184\ns_waitcnt vmcnt(9)\ns_barrier\n"
"s_waitcnt lgkmcnt(2)\nM_ 160,48,188\nds_read_b64_tr_b16 v[56:57],v239 offset:32768\nds_read_b64_tr_b16 v[58:59],v239 offset:33280\nM_ 164,52,188\ns_add_u32 m0,s77,90112\ns_add_u32 s83,s80,294912\nbuffer_load_dwordx4 v238,s[16:19],s83 offen lds\n"
"ds_read_b64_tr_b16 v[60:61],v239 offset:40960\nds_read_b64_tr_b16 v[62:63],v239 offset:41472\nv_smfmac_f32_16x16x64_bf16 v[216:219],v[220:223],v[48:55],v182\nds_read_b128 v[200:203],v226 offset:28672\nds_read_b128 v[204:207],v226 offset:24576\ns_waitcnt lgkmcnt(7)\nM_ 168,48,192\nds_read_b128 v[184:187],v226 offset:26624\n"
"ds_read_b128 v[188:191],v226 offset:30720\nM_ 172,52,192\ns_add_u32 m0,s77,98304\ns_nop 0\nbuffer_load_dwordx4 v238,s[20:23],s83 offen lds\ns_add_u32 s82,s82,1\ns_cmp_eq_u32 s82,9\ns_cselect_b32 s3,0xffaf0000,0\n"
"s_add_u32 s80,s80,589824\ns_add_u32 s80,s80,s3\ns_waitcnt lgkmcnt(8)\nM_ 152,48,196\nM_ 156,52,196\ns_waitcnt lgkmcnt(3)\nM_ 144,56,200\nM_ 148,60,200\n"
"s_waitcnt vmcnt(7)\ns_barrier\ns_waitcnt lgkmcnt(2)\nM_ 160,56,204\nds_read_b64_tr_b16 v[64:65],v239 offset:49152\nds_read_b64_tr_b16 v[66:67],v239 offset:49664\nM_ 164,60,204\ns_add_u32 m0,s77,106496\n"
"s_nop 0\nbuffer_load_dwordx4 v238,s[16:19],s80 offen lds\nds_read_b64_tr_b16 v[68:69],v239 offset:57344\nds_read_b64_tr_b16 v[70:71],v239 offset:57856\nv_smfmac_f32_16x16x64_bf16 v[216:219],v[220:223],v[56:63],v182\nds_read_b128 v[192:195],v237 offset:4096\nds_read_b128 v[196:199],v237\ns_waitcnt lgkmcnt(7)\n"
"M_ 168,56,184\nds_read_b128 v[200:203],v237 offset:2048\nds_read_b128 v[204:207],v237 offset:6144\nM_ 172,60,184\ns_add_u32 m0,s77,114688\ns_nop 0\nbuffer_load_dwordx4 v238,s[20:23],s80 offen lds\ns_waitcnt lgkmcnt(8)\n"
"M_ 152,56,188\ns_add_u32 m0,s77,53248\ns_nop 0\nbuffer_load_dwordx4 v241,s[8:11],s81 offen lds\ns_add_u32 s84,s84,1\ns_cmp_eq_u32 s84,9\ns_cselect_b32 s3,0xfffffb80,0\ns_add_u32 s81,s81,128\n"
"s_add_u32 s81,s81,s3\nM_ 156,60,188\ns_waitcnt lgkmcnt(3)\nM_ 144,64,192\nM_ 148,68,192\ns_waitcnt vmcnt(8)\ns_barrier\ns_waitcnt lgkmcnt(2)\n"
"M_ 160,64,196\nds_read_b64_tr_b16 v[72:73],v240\nds_read_b64_tr_b16 v[74:75],v240 offset:512\nM_ 164,68,196\ns_add_u32 m0,s77,122880\ns_add_u32 s83,s80,294912\nbuffer_load_dwordx4 v238,s[16:19],s83 offen lds\nds_read_b64_tr_b16 v[76:77],v240 offset:8192\n"
"ds_read_b64_tr_b16 v[78:79],v240 offset:8704\nv_smfmac_f32_16x16x64_bf16 v[216:219],v[220:223],v[64:71],v182\nds_read_b128 v[184:187],v226 offset:4096\nds_read_b128 v[188:191],v226\ns_waitcnt lgkmcnt(7)\nM_ 168,64,200\nds_read_b128 v[192:195],v226 offset:2048\nds_read_b128 v[196:199],v226 offset:6144\n"
"M_ 172,68,200\ns_add_u32 m0,s77,131072\ns_nop 0\nbuffer_load_dwordx4 v238,s[20:23],s83 offen lds\ns_add_u32 s82,s82,1\ns_cmp_eq_u32 s82,9\ns_cselect_b32 s3,0xffaf0000,0\ns_add_u32 s80,s80,589824\n"
"s_add_u32 s80,s80,s3\ns_waitcnt lgkmcnt(8)\nM_ 152,64,204\nM_ 156,68,204\ns_waitcnt lgkmcnt(3)\nM_ 144,72,184\nM_ 148,76,184\ns_waitcnt vmcnt(7)\n"
"s_barrier\ns_waitcnt lgkmcnt(2)\nM_ 160,72,188\nds_read_b64_tr_b16 v[80:81],v239\nds_read_b64_tr_b16 v[82:83],v239 offset:512\nM_ 164,76,188\ns_add_u32 m0,s77,139264\ns_nop 0\n"
"buffer_load_dwordx4 v238,s[16:19],s80 offen lds\nds_read_b64_tr_b16 v[84:85],v239 offset:8192\nds_read_b64_tr_b16 v[86:87],v239 offset:8704\nv_smfmac_f32_16x16x64_bf16 v[216:219],v[220:223],v[72:79],v182\nds_read_b128 v[200:203],v237 offset:12288\nds_read_b128 v[204:207],v237 offset:8192\ns_waitcnt lgkmcnt(7)\nM_ 168,72,192\n"
"ds_read_b128 v[184:187],v237 offset:10240\nds_read_b128 v[188:191],v237 offset:14336\nM_ 172,76,192\ns_add_u32 m0,s77,147456\ns_nop 0\nbuffer_load_dwordx4 v238,s[20:23],s80 offen lds\ns_waitcnt lgkmcnt(8)\nM_ 152,72,196\n"
"s_add_u32 m0,s77,61440\ns_nop 0\nbuffer_load_dwordx4 v241,s[8:11],s81 offen lds\ns_add_u32 s84,s84,1\ns_cmp_eq_u32 s84,9\ns_cselect_b32 s3,0xfffffb80,0\ns_add_u32 s81,s81,128\ns_add_u32 s81,s81,s3\n"
"M_ 156,76,196\ns_waitcnt lgkmcnt(3)\nM_ 144,80,200\nM_ 148,84,200\ns_waitcnt vmcnt(8)\ns_barrier\ns_waitcnt lgkmcnt(2)\nM_ 160,80,204\n"
"ds_read_b64_tr_b16 v[88:89],v239 offset:16384\nds_read_b64_tr_b16 v[90:91],v239 offset:16896\nM_ 164,84,204\ns_add_u32 m0,s77,73728\ns_add_u32 s83,s80,294912\nbuffer_load_dwordx4 v238,s[16:19],s83 offen lds\nds_read_b64_tr_b16 v[92:93],v239 offset:24576\nds_read_b64_tr_b16 v[94:95],v239 offset:25088\n"
"v_smfmac_f32_16x16x64_bf16 v[216:219],v[220:223],v[80:87],v182\nbuffer_load_dwordx4 v[128:131],v252,s[12:15],0 offen sc1\nds_read_b128 v[192:195],v226 offset:12288\nds_read_b128 v[196:199],v226 offset:8192\ns_waitcnt lgkmcnt(7)\nM_ 168,80,184\nds_read_b128 v[200:203],v226 offset:10240\nds_read_b128 v[204:207],v226 offset:14336\n"
"M_ 172,84,184\ns_add_u32 m0,s77,81920\ns_nop 0\nbuffer_load_dwordx4 v238,s[20:23],s83 offen lds\ns_add_u32 s82,s82,1\ns_cmp_eq_u32 s82,9\ns_cselect_b32 s3,0xffaf0000,0\ns_add_u32 s80,s80,589824\n"
"s_add_u32 s80,s80,s3\ns_waitcnt lgkmcnt(8)\nM_ 152,80,188\nM_ 156,84,188\ns_waitcnt lgkmcnt(3)\nM_ 144,88,192\nM_ 148,92,192\ns_waitcnt vmcnt(8)\n"
"s_barrier\ns_waitcnt lgkmcnt(2)\nM_ 160,88,196\nds_read_b64_tr_b16 v[96:97],v239 offset:32768\nds_read_b64_tr_b16 v[98:99],v239 offset:33280\nM_ 164,92,196\ns_add_u32 m0,s77,90112\ns_nop 0\n"
"buffer_load_dwordx4 v238,s[16:19],s80 offen lds\nds_read_b64_tr_b16 v[100:101],v239 offset:40960\nds_read_b64_tr_b16 v[102:103],v239 offset:41472\nv_smfmac_f32_16x16x64_bf16 v[216:219],v[220:223],v[88:95],v182\nds_read_b128 v[184:187],v237 offset:20480\nds_read_b128 v[188:191],v237 offset:16384\ns_waitcnt lgkmcnt(7)\nM_ 168,88,200\n"
"ds_read_b128 v[192:195],v237 offset:18432\nds_read_b128 v[196:199],v237 offset:22528\nM_ 172,92,200\ns_add_u32 m0,s77,98304\ns_nop 0\nbuffer_load_dwordx4 v238,s[20:23],s80 offen lds\ns_waitcnt lgkmcnt(8)\nM_ 152,88,204\n"
"s_add_u32 m0,s77,36864\ns_nop 0\nbuffer_load_dwordx4 v241,s[8:11],s81 offen lds\ns_add_u32 s84,s84,1\ns_cmp_eq_u32 s84,9\ns_cselect_b32 s3,0xfffffb80,0\ns_add_u32 s81,s81,128\ns_add_u32 s81,s81,s3\n"
"M_ 156,92,204\ns_waitcnt lgkmcnt(3)\nM_ 144,96,184\nM_ 148,100,184\ns_waitcnt vmcnt(9)\ns_barrier\ns_waitcnt lgkmcnt(2)\nM_ 160,96,188\n"
"ds_read_b64_tr_b16 v[104:105],v239 offset:49152\nds_read_b64_tr_b16 v[106:107],v239 offset:49664\nM_ 164,100,188\ns_add_u32 m0,s77,106496\ns_add_u32 s83,s80,294912\nbuffer_load_dwordx4 v238,s[16:19],s83 offen lds\nds_read_b64_tr_b16 v[108:109],v239 offset:57344\nds_read_b64_tr_b16 v[110:111],v239 offset:57856\n"
"v_smfmac_f32_16x16x64_bf16 v[216:219],v[220:223],v[96:103],v182\nds_read_b128 v[200:203],v226 offset:20480\nds_read_b128 v[204:207],v226 offset:16384\ns_waitcnt lgkmcnt(7)\nM_ 168,96,192\nds_read_b128 v[184:187],v226 offset:18432\nds_read_b128 v[188:191],v226 offset:22528\nM_ 172,100,192\n"
"s_add_u32 m0,s77,114688\ns_nop 0\nbuffer_load_dwordx4 v238,s[20:23],s83 offen lds\ns_add_u32 s82,s82,1\ns_cmp_eq_u32 s82,9\ns_cselect_b32 s3,0xffaf0000,0\ns_add_u32 s80,s80,589824\ns_add_u32 s80,s80,s3\n"
"s_waitcnt lgkmcnt(8)\nM_ 152,96,196\nM_ 156,100,196\ns_waitcnt lgkmcnt(3)\nM_ 144,104,200\nM_ 148,108,200\ns_waitcnt vmcnt(8)\ns_barrier\n"
"s_waitcnt lgkmcnt(2)\nM_ 160,104,204\ns_mul_i32 s83,s86,36864\ns_add_u32 s3,s78,36864\ns_xor_b32 s82,s3,s78\nds_read_b64_tr_b16 v[112:113],v240\nds_read_b64_tr_b16 v[114:115],v240 offset:512\nv_mov_b32 v238,v254\n"
"v_mov_b32 v241,v253\nM_ 164,108,204\ns_mov_b32 m0,s78\ns_nop 0\nbuffer_load_dwordx4 v227,s[72:75],s83 offen sc1 lds\nds_read_b64_tr_b16 v[116:117],v240 offset:8192\nds_read_b64_tr_b16 v[118:119],v240 offset:8704\nv_smfmac_f32_16x16x64_bf16 v[216:219],v[220:223],v[104:111],v182\n"
"ds_read_b128 v[192:195],v237 offset:28672\nds_read_b128 v[196:199],v237 offset:24576\ns_waitcnt lgkmcnt(7)\nM_ 168,104,184\nds_read_b128 v[200:203],v237 offset:26624\nds_read_b128 v[204:207],v237 offset:30720\nM_ 172,108,184\ns_mov_b32 m0,s78\n"
"s_nop 0\nbuffer_load_dwordx4 v228,s[72:75],s83 offen offset:1024 sc1 lds\ns_waitcnt lgkmcnt(8)\nM_ 152,104,188\ns_add_u32 m0,s78,18432\ns_add_u32 s81,s83,18432\nbuffer_load_dwordx4 v227,s[72:75],s81 offen sc1 lds\nM_ 156,108,188\n"
"s_waitcnt lgkmcnt(3)\nM_ 144,112,192\nM_ 148,116,192\ns_waitcnt vmcnt(8)\ns_barrier\ns_waitcnt lgkmcnt(2)\nM_ 160,112,196\nds_read_b64_tr_b16 v[120:121],v239\n"
"ds_read_b64_tr_b16 v[122:123],v239 offset:512\nM_ 164,116,196\ns_add_u32 m0,s78,18432\ns_nop 0\nbuffer_load_dwordx4 v228,s[72:75],s81 offen offset:1024 sc1 lds\nds_read_b64_tr_b16 v[124:125],v239 offset:8192\nds_read_b64_tr_b16 v[126:127],v239 offset:8704\nv_smfmac_f32_16x16x64_bf16 v[216:219],v[220:223],v[112:119],v182\n"
"v_cmp_eq_u32 s[4:5],s96,v129\nv_cmp_eq_u32 vcc,s97,v131\ns_nop 0\ns_and_b64 s[4:5],s[4:5],vcc\ns_cmp_eq_u64 s[4:5],-1\ns_cbranch_scc0 .sl2_%=\n.ok1_%=:\nds_read_b128 v[184:187],v226 offset:28672\n"
"ds_read_b128 v[188:191],v226 offset:24576\nv_mul_f32 v135,v130,v130\nv_xor_b32 v132,0x80000000,v128\nv_mul_f32 v133,0xc0135761,v130\ns_waitcnt lgkmcnt(7)\nM_ 168,112,200\nds_read_b128 v[192:195],v226 offset:26624\nds_read_b128 v[196:199],v226 offset:30720\n"
"v_mul_f32 v134,0xbdd2d3e8,v130\nv_mul_f32 v134,v134,v135\nM_ 172,116,200\ns_add_u32 m0,s78,s79\ns_mov_b32 exec_hi,0\nbuffer_load_dwordx4 v229,s[72:75],s83 offen sc1 lds\ns_mov_b32 exec_hi,-1\ns_xor_b32 s78,s78,s82\n"
"v_rcp_f32 v135,v135\nv_add_u32 v252,s77,v224\nv_add_u32 v252,155648,v252\nds_write_b128 v252,v[132:135]\ns_waitcnt lgkmcnt(9)\nM_ 152,112,204\ns_mul_i32 s83,s85,36864\nM_ 156,116,204\n"
"s_waitcnt lgkmcnt(4)\nM_ 144,120,184\nM_ 148,124,184\ns_waitcnt vmcnt(7) lgkmcnt(0)\ns_barrier\nM_ 160,120,188\nds_read_b64_tr_b16 v[128:129],v239 offset:16384\nds_read_b64_tr_b16 v[130:131],v239 offset:16896\n"
"M_ 164,124,188\nds_read_b64_tr_b16 v[132:133],v239 offset:24576\nds_read_b64_tr_b16 v[134:135],v239 offset:25088\nv_smfmac_f32_16x16x64_bf16 v[216:219],v[220:223],v[120:127],v182\nds_read_b128 v[200:203],v237 offset:4096\nds_read_b128 v[204:207],v237\nM_ 168,120,192\nds_read_b128 v[184:187],v237 offset:2048\n"
"ds_read_b128 v[188:191],v237 offset:6144\nM_ 172,124,192\nM_ 152,120,196\nM_ 156,124,196\ns_waitcnt lgkmcnt(3)\nM_ 144,128,200\nM_ 148,132,200\ns_waitcnt vmcnt(5)\n"
"s_barrier\nv_smfmac_f32_16x16x64_bf16 v[216:219],v[220:223],v[128:135],v182\nds_read_b64_tr_b16 v[136:137],v239 offset:32768\nds_read_b64_tr_b16 v[138:139],v239 offset:33280\ns_waitcnt lgkmcnt(4)\nM_ 160,128,204\nds_read_b64_tr_b16 v[140:141],v239 offset:40960\nds_read_b64_tr_b16 v[142:143],v239 offset:41472\n"
"M_ 164,132,204\nds_read_b128 v[192:195],v226 offset:4096\nds_read_b128 v[196:199],v226\ns_waitcnt lgkmcnt(7)\nM_ 168,128,184\nds_read_b128 v[200:203],v226 offset:2048\nds_read_b128 v[204:207],v226 offset:6144\nM_ 172,132,184\n"
"ds_read_b128 v[208:211],v231\ns_waitcnt lgkmcnt(9)\nM_ 152,128,188\nM_ 156,132,188\ns_waitcnt lgkmcnt(5)\nv_smfmac_f32_16x16x64_bf16 v[216:219],v[220:223],v[136:143],v182\ns_add_u32 m0,s77,73728\ns_nop 0\n"
"buffer_load_dwordx4 v238,s[24:27],s94 offen lds\ns_waitcnt lgkmcnt(4)\nM_ 144,136,192\nM_ 148,140,192\ns_waitcnt vmcnt(1) lgkmcnt(0)\ns_barrier\nv_xor_b32 v237,36864,v237\nv_xor_b32 v226,36864,v226\n"
"v_add_f32 v220,v216,v217\nv_add_f32 v221,v218,v219\nds_write2_b32 v181,v220,v221 offset1:16\nds_read_b128 v[176:179],v183\nds_read_b128 v[180:183],v183 offset:64\nM_ 160,136,196\nds_read_b128 v[212:215],v231 offset:256\ns_mov_b32 m0,s78\n"
"s_nop 0\nbuffer_load_dwordx4 v227,s[72:75],s83 offen sc1 lds\nds_read_b128 v[184:187],v237\nds_read_b128 v[188:191],v226\nM_ 164,140,196\nds_read_b128 v[192:195],v237 offset:2048\nds_read_b128 v[196:199],v226 offset:2048\ns_waitcnt lgkmcnt(6)\n"
"v_fmac_f32 v144,v208,v176\nv_fmac_f32 v145,v208,v177\nv_fmac_f32 v146,v208,v178\nv_fmac_f32 v147,v208,v179\nM_ 168,136,200\ns_mov_b32 m0,s78\ns_nop 0\nbuffer_load_dwordx4 v228,s[72:75],s83 offen offset:1024 sc1 lds\n"
"s_waitcnt lgkmcnt(5)\nv_fmac_f32 v148,v208,v180\nv_fmac_f32 v149,v208,v181\nv_fmac_f32 v150,v208,v182\nM_ 172,140,200\nds_read_b128 v[200:203],v237 offset:4096\nv_fmac_f32 v151,v208,v183\nv_mul_f32 v216,v144,v144\n"
"v_mul_f32 v217,v145,v145\nM_ 152,136,204\ns_add_u32 m0,s77,81792\ns_nop 0\nbuffer_load_dwordx4 v238,s[24:27],s94 offen offset:128 lds\nv_mul_f32 v218,v146,v146\nv_mul_f32 v219,v147,v147\nv_fma_f32 v216,v216,v210,v209\n"
"M_ 156,140,204\ns_add_u32 m0,s77,89856\ns_add_u32 s52,s52,24576\nbuffer_load_dwordx4 v238,s[24:27],s94 offen offset:256 lds\ns_add_u32 s51,s51,1\ns_cmp_eq_u32 s51,9\ns_cselect_b32 s3,0xfffaf000,0\ns_add_u32 s94,s94,36864\n"
"s_add_u32 s94,s94,s3\nv_fma_f32 v217,v217,v210,v209\nv_fma_f32 v218,v218,v210,v209\nv_fma_f32 v219,v219,v210,v209\nv_mul_f32 v216,v216,v144\nv_mul_f32 v217,v217,v145\nv_mul_f32 v218,v218,v146\nv_mul_f32 v219,v219,v147\n"
"v_exp_f32 v216,v216\nv_exp_f32 v217,v217\nv_exp_f32 v218,v218\nv_exp_f32 v219,v219\nv_mul_f32 v148,v144,v148\nv_mul_f32 v149,v145,v149\nv_mul_f32 v150,v146,v150\nv_mul_f32 v151,v147,v151\n"
"v_fma_f32 v216,v216,v211,v211\nv_fma_f32 v217,v217,v211,v211\nv_fma_f32 v218,v218,v211,v211\nv_fma_f32 v219,v219,v211,v211\nv_rcp_f32 v216,v216\nv_rcp_f32 v217,v217\nv_rcp_f32 v218,v218\nv_rcp_f32 v219,v219\n"
"v_mul_f32 v148,v148,v216\nv_mul_f32 v149,v149,v217\nv_mul_f32 v150,v150,v218\nv_mul_f32 v151,v151,v219\nv_cvt_pk_bf16_f32 v220,v148,v149\nv_cvt_pk_bf16_f32 v221,v150,v151\ns_waitcnt lgkmcnt(5)\ns_bitcmp1_b32 s76,2\n"
"s_cbranch_scc1 .xs4_%=\ns_setprio 2\n.xs4_%=:\ns_nop 7\n.p1_%=:\ns_waitcnt lgkmcnt(3)\nZ_ 144,0,184\nds_read_b128 v[204:207],v226 offset:4096\n"
"Z_ 148,4,184\nds_read_b128 v[184:187],v237 offset:6144\nv_fmac_f32 v152,v212,v176\nv_fmac_f32 v153,v212,v177\nM_ 144,8,188\ns_add_u32 m0,s78,18432\ns_add_u32 s81,s83,18432\nv_fmac_f32 v154,v212,v178\n"
"M_ 148,12,188\nbuffer_load_dwordx4 v227,s[72:75],s81 offen sc1 lds\nds_read_b128 v[188:191],v226 offset:6144\nv_fmac_f32 v155,v212,v179\ns_waitcnt lgkmcnt(4)\nM_ 144,16,192\nv_fmac_f32 v156,v212,v180\nv_fmac_f32 v157,v212,v181\n"
"M_ 148,20,192\nds_read_b128 v[192:195],v237 offset:8192\nv_fmac_f32 v158,v212,v182\nv_fmac_f32 v159,v212,v183\nM_ 144,24,196\nbuffer_load_dwordx4 v228,s[72:75],s81 offen offset:1024 sc1 lds\nv_mul_f32 v216,v152,v152\nM_ 148,28,196\n"
"ds_read_b128 v[196:199],v226 offset:8192\nv_mul_f32 v217,v153,v153\nv_mul_f32 v218,v154,v154\ns_waitcnt lgkmcnt(4)\nM_ 144,32,200\ns_add_u32 m0,s78,s79\ns_xor_b32 s78,s78,s82\nv_add_u32 v231,512,v231\n"
"ds_read_b128 v[208:211],v231\nM_ 148,36,200\ns_mov_b32 exec_hi,0\nbuffer_load_dwordx4 v229,s[72:75],s83 offen sc1 lds\ns_mov_b32 exec_hi,-1\nds_read_b128 v[200:203],v237 offset:10240\nM_ 144,40,204\ns_cmp_eq_u32 s59,2\n"
"s_cbranch_scc1 .ra5_%=\n.rb6_%=:\nv_mul_f32 v219,v155,v155\nv_fma_f32 v216,v216,v214,v213\nM_ 148,44,204\ns_mov_b32 s89,s88\ns_mov_b32 s88,s87\nds_read_b128 v[204:207],v226 offset:10240\n"
"s_waitcnt lgkmcnt(5)\nM_ 144,48,184\ns_mov_b32 s87,s86\ns_mov_b32 s86,s85\nv_fma_f32 v217,v217,v214,v213\nv_fma_f32 v218,v218,v214,v213\nM_ 148,52,184\ns_and_b32 s85,s64,63\n"
"s_lshr_b64 s[64:65],s[64:65],6\nds_read_b128 v[184:187],v237 offset:12288\nv_fma_f32 v219,v219,v214,v213\nM_ 144,56,188\ns_cmp_eq_u64 s[64:65],1\ns_cselect_b64 s[64:65],s[48:49],s[64:65]\nv_mul_f32 v216,v216,v152\nv_mul_f32 v217,v217,v153\n"
"M_ 148,60,188\ns_mul_i32 s83,s85,s90\ns_add_u32 s83,s83,s91\nds_read_b128 v[188:191],v226 offset:12288\ns_waitcnt lgkmcnt(5)\nM_ 144,64,192\ns_cmp_eq_u32 s92,0\ns_cbranch_scc0 .so7_%=\n"
".sb8_%=:\nv_mul_f32 v218,v218,v154\nv_mul_f32 v219,v219,v155\nM_ 148,68,192\nds_read_b128 v[192:195],v237 offset:14336\nv_exp_f32 v216,v216\nM_ 144,72,196\ns_mul_i32 s2,s89,18432\n"
"s_add_u32 s2,s2,s50\nv_exp_f32 v217,v217\nM_ 148,76,196\nds_read_b128 v[196:199],v226 offset:14336\nv_exp_f32 v218,v218\ns_waitcnt lgkmcnt(4)\nM_ 144,80,200\ns_waitcnt vmcnt(5)\n"
"s_mov_b64 exec,15\nbuffer_store_dwordx4 v[234:237],v224,s[12:15],s2 offen sc1\ns_mov_b64 exec,-1\nM_ 148,84,200\nds_read_b128 v[200:203],v237 offset:16384\nv_exp_f32 v219,v219\nv_mul_f32 v156,v152,v156\nM_ 144,88,204\n"
"s_min_u32 m0,s52,s53\ns_add_u32 s2,s94,s55\nv_mul_f32 v157,v153,v157\nM_ 148,92,204\nds_read_b128 v[204:207],v226 offset:16384\nv_mul_f32 v158,v154,v158\nv_mul_f32 v159,v155,v159\ns_waitcnt lgkmcnt(4)\n"
"M_ 144,96,184\nbuffer_load_dwordx4 v238,s[24:27],s2 offen lds\nv_fma_f32 v216,v216,v215,v215\nM_ 148,100,184\ns_add_u32 s52,s52,8192\ns_add_u32 s55,s55,128\nds_read_b128 v[184:187],v237 offset:18432\nv_fma_f32 v217,v217,v215,v215\n"
"M_ 144,104,188\ns_cmp_eq_u32 s55,384\ns_cbranch_scc1 .ra9_%=\n.rb10_%=:\nv_fma_f32 v218,v218,v215,v215\nv_fma_f32 v219,v219,v215,v215\nM_ 148,108,188\nds_read_b128 v[188:191],v226 offset:18432\n"
"v_rcp_f32 v216,v216\ns_waitcnt lgkmcnt(4)\nM_ 144,112,192\ns_sub_u32 s59,s59,1\nM_ 148,116,192\nds_read_b128 v[192:195],v237 offset:20480\nv_rcp_f32 v217,v217\nM_ 144,120,196\n"
"v_rcp_f32 v218,v218\nM_ 148,124,196\nds_read_b128 v[196:199],v226 offset:20480\nv_rcp_f32 v219,v219\ns_waitcnt lgkmcnt(4)\nM_ 144,128,200\nv_mul_f32 v156,v156,v216\nM_ 148,132,200\n"
"ds_read_b128 v[200:203],v237 offset:22528\nv_mul_f32 v157,v157,v217\nv_mul_f32 v158,v158,v218\nM_ 144,136,204\nv_mul_f32 v159,v159,v219\nM_ 148,140,204\nv_cvt_pk_bf16_f32 v222,v156,v157\nv_cvt_pk_bf16_f32 v223,v158,v159\n"
"s_nop 0\nv_permlane16_swap_b32 v220,v222\nv_permlane16_swap_b32 v221,v223\ns_mul_i32 s7,s88,294912\nbuffer_store_dwordx4 v[220:223],v230,s[12:15],s7 offen sc1\ns_cmp_eq_u32 s59,0\ns_cbranch_scc1 .p1x_%=\ns_waitcnt lgkmcnt(3)\n"
"Z_ 152,0,184\nds_read_b128 v[204:207],v226 offset:22528\nZ_ 156,4,184\nds_read_b128 v[184:187],v237 offset:24576\nv_fmac_f32 v144,v208,v176\nv_fmac_f32 v145,v208,v177\nM_ 152,8,188\nv_fmac_f32 v146,v208,v178\n"
"M_ 156,12,188\nds_read_b128 v[188:191],v226 offset:24576\nv_fmac_f32 v147,v208,v179\nv_fmac_f32 v148,v208,v180\ns_waitcnt lgkmcnt(4)\nM_ 152,16,192\nv_fmac_f32 v149,v208,v181\nM_ 156,20,192\n"
"ds_read_b128 v[192:195],v237 offset:26624\nv_fmac_f32 v150,v208,v182\nv_fmac_f32 v151,v208,v183\nM_ 152,24,196\nv_mul_f32 v216,v144,v144\nM_ 156,28,196\nds_read_b128 v[196:199],v226 offset:26624\nv_mul_f32 v217,v145,v145\n"
"v_mul_f32 v218,v146,v146\ns_waitcnt lgkmcnt(4)\nM_ 152,32,200\nds_read_b128 v[212:215],v231 offset:256\nv_mul_f32 v219,v147,v147\nM_ 156,36,200\nds_read_b128 v[200:203],v237 offset:28672\nv_fma_f32 v216,v216,v210,v209\n"
"v_fma_f32 v217,v217,v210,v209\nM_ 152,40,204\nv_fma_f32 v218,v218,v210,v209\nM_ 156,44,204\nds_read_b128 v[204:207],v226 offset:28672\nv_fma_f32 v219,v219,v210,v209\nv_mul_f32 v216,v216,v144\ns_waitcnt lgkmcnt(5)\n"
"M_ 152,48,184\nv_mul_f32 v217,v217,v145\nM_ 156,52,184\nds_read_b128 v[184:187],v237 offset:30720\nv_mul_f32 v218,v218,v146\nv_mul_f32 v219,v219,v147\nM_ 152,56,188\nM_ 156,60,188\n"
"ds_read_b128 v[188:191],v226 offset:30720\nv_exp_f32 v216,v216\ns_waitcnt lgkmcnt(5)\nM_ 152,64,192\ns_cmp_eq_u32 s92,0\ns_cbranch_scc0 .so11_%=\n.sb12_%=:\nv_exp_f32 v217,v217\n"
"M_ 156,68,192\nds_read_b128 v[192:195],v237 offset:32768\nv_exp_f32 v218,v218\nM_ 152,72,196\nM_ 156,76,196\nds_read_b128 v[196:199],v226 offset:32768\nv_exp_f32 v219,v219\ns_waitcnt lgkmcnt(4)\n"
"M_ 152,80,200\nv_mul_f32 v148,v144,v148\nv_mul_f32 v149,v145,v149\nM_ 156,84,200\nds_read_b128 v[200:203],v237 offset:34816\nv_mul_f32 v150,v146,v150\nv_mul_f32 v151,v147,v151\nM_ 152,88,204\n"
"v_fma_f32 v216,v216,v211,v211\nM_ 156,92,204\nds_read_b128 v[204:207],v226 offset:34816\nv_fma_f32 v217,v217,v211,v211\nv_fma_f32 v218,v218,v211,v211\ns_waitcnt lgkmcnt(4)\nM_ 152,96,184\nv_fma_f32 v219,v219,v211,v211\n"
"M_ 156,100,184\nv_rcp_f32 v216,v216\nM_ 152,104,188\nM_ 156,108,188\nv_xor_b32 v237,36864,v237\nv_xor_b32 v226,36864,v226\ns_waitcnt lgkmcnt(2)\nM_ 152,112,192\n"
"v_rcp_f32 v217,v217\nM_ 156,116,192\ns_waitcnt vmcnt(3) lgkmcnt(0)\ns_barrier\nv_rcp_f32 v218,v218\nM_ 152,120,196\ns_mov_b32 m0,s78\nds_read_b128 v[184:187],v237\n"
"ds_read_b128 v[188:191],v226\nM_ 156,124,196\nbuffer_load_dwordx4 v227,s[72:75],s83 offen sc1 lds\nds_read_b128 v[192:195],v237 offset:2048\nds_read_b128 v[196:199],v226 offset:2048\nM_ 152,128,200\nv_rcp_f32 v219,v219\nv_mul_f32 v148,v148,v216\n"
"M_ 156,132,200\nds_read_b128 v[200:203],v237 offset:4096\nv_mul_f32 v149,v149,v217\nv_mul_f32 v150,v150,v218\nM_ 152,136,204\nbuffer_load_dwordx4 v228,s[72:75],s83 offen offset:1024 sc1 lds\nv_mul_f32 v151,v151,v219\nM_ 156,140,204\n"
"v_cvt_pk_bf16_f32 v220,v148,v149\nv_cvt_pk_bf16_f32 v221,v150,v151\ns_branch .p1_%=\n.p1x_%=:\ns_cmp_eq_u32 s56,1\ns_cbranch_scc1 .spec_%=\ns_waitcnt lgkmcnt(4)\nZ_ 152,0,184\n"
"ds_read_b128 v[204:207],v226 offset:22528\nv_fmac_f32 v144,v208,v176\nv_fmac_f32 v145,v208,v177\nZ_ 156,4,184\ns_bfe_u32 s2,s70,262144\ns_add_u32 s51,s2,3\ns_sub_u32 s7,s51,9\ns_cselect_b32 s51,s51,s7\n"
"s_mul_i32 s2,s51,36864\ns_add_u32 s94,s61,s2\ns_mov_b32 s62,s87\nds_read_b128 v[184:187],v237 offset:24576\nv_fmac_f32 v146,v208,v178\nv_fmac_f32 v147,v208,v179\ns_waitcnt lgkmcnt(5)\nM_ 152,8,188\n"
"ds_read_b128 v[212:215],v231 offset:256\nv_fmac_f32 v148,v208,v180\nv_fmac_f32 v149,v208,v181\nM_ 156,12,188\nds_read_b64_tr_b16 v[0:1],v242\nds_read_b64_tr_b16 v[2:3],v242 offset:512\nds_read_b128 v[188:191],v226 offset:24576\nv_fmac_f32 v150,v208,v182\n"
"v_fmac_f32 v151,v208,v183\ns_waitcnt lgkmcnt(8)\nM_ 152,16,192\nds_read_b64_tr_b16 v[4:5],v243\nds_read_b64_tr_b16 v[6:7],v243 offset:512\nv_mul_f32 v216,v144,v144\nv_mul_f32 v217,v145,v145\nM_ 156,20,192\n"
"ds_read_b64_tr_b16 v[8:9],v241\nds_read_b64_tr_b16 v[10:11],v241 offset:512\nds_read_b128 v[192:195],v237 offset:26624\nv_mul_f32 v218,v146,v146\nv_mul_f32 v219,v147,v147\ns_waitcnt lgkmcnt(12)\nM_ 152,24,196\nds_read_b64_tr_b16 v[12:13],v242 offset:24576\n"
"ds_read_b64_tr_b16 v[14:15],v242 offset:25088\nv_fma_f32 v216,v216,v210,v209\nv_fma_f32 v217,v217,v210,v209\nM_ 156,28,196\nds_read_b64_tr_b16 v[16:17],v243 offset:24576\nds_read_b64_tr_b16 v[18:19],v243 offset:25088\nds_read_b128 v[196:199],v226 offset:26624\nv_fma_f32 v218,v218,v210,v209\n"
"v_fma_f32 v219,v219,v210,v209\ns_waitcnt lgkmcnt(15)\nM_ 152,32,200\nds_read_b64_tr_b16 v[20:21],v241 offset:24576\nds_read_b64_tr_b16 v[22:23],v241 offset:25088\nv_mul_f32 v216,v216,v144\nv_mul_f32 v217,v217,v145\nM_ 156,36,200\n"
"ds_read_b128 v[200:203],v237 offset:28672\nv_mul_f32 v218,v218,v146\nv_mul_f32 v219,v219,v147\nM_ 152,40,204\ns_waitcnt vmcnt(3)\nv_cmp_ne_u64 vcc,v[232:233],v[234:235]\ns_nop 0\ns_and_b64 s[4:5],vcc,s[92:93]\n"
"s_cbranch_scc1 .sl17_%=\n.ok16_%=:\nv_exp_f32 v216,v216\nM_ 156,44,204\nds_read_b128 v[204:207],v226 offset:28672\nv_exp_f32 v217,v217\ns_waitcnt lgkmcnt(15)\nM_ 152,48,184\n"
"v_exp_f32 v218,v218\nM_ 156,52,184\ns_waitcnt lgkmcnt(0)\ns_barrier\nds_read_b128 v[184:187],v237 offset:30720\nv_exp_f32 v219,v219\nM_ 152,56,188\ns_mov_b32 m0,s78\n"
"s_nop 0\nbuffer_load_dwordx4 v227,s[72:75],s83 offen sc1 lds\nv_mul_f32 v148,v144,v148\nv_mul_f32 v149,v145,v149\nM_ 156,60,188\nds_read_b64_tr_b16 v[24:25],v242 offset:49152\nds_read_b128 v[188:191],v226 offset:30720\nv_mul_f32 v150,v146,v150\n"
"v_mul_f32 v151,v147,v151\nM_ 152,64,192\nds_read_b64_tr_b16 v[26:27],v242 offset:49664\nv_fma_f32 v216,v216,v211,v211\nv_fma_f32 v217,v217,v211,v211\nM_ 156,68,192\nds_read_b64_tr_b16 v[28:29],v243 offset:49152\ns_mov_b32 m0,s78\n"
"s_nop 0\nbuffer_load_dwordx4 v228,s[72:75],s83 offen offset:1024 sc1 lds\nds_read_b128 v[192:195],v237 offset:32768\nv_fma_f32 v218,v218,v211,v211\nv_fma_f32 v219,v219,v211,v211\nM_ 152,72,196\nds_read_b64_tr_b16 v[30:31],v243 offset:49664\nv_rcp_f32 v216,v216\n"
"M_ 156,76,196\nds_read_b64_tr_b16 v[32:33],v241 offset:49152\nds_read_b128 v[196:199],v226 offset:32768\nv_rcp_f32 v217,v217\nM_ 152,80,200\nds_read_b64_tr_b16 v[34:35],v241 offset:49664\ns_add_u32 m0,s77,73728\ns_nop 0\n"
"buffer_load_dwordx4 v238,s[24:27],s94 offen lds\nv_rcp_f32 v218,v218\nM_ 156,84,200\nds_read_b128 v[200:203],v237 offset:34816\nv_rcp_f32 v219,v219\nM_ 152,88,204\nv_mul_f32 v148,v148,v216\nv_mul_f32 v149,v149,v217\n"
"M_ 156,92,204\ns_add_u32 m0,s77,81792\ns_nop 0\nbuffer_load_dwordx4 v238,s[24:27],s94 offen offset:128 lds\nds_read_b128 v[204:207],v226 offset:34816\nv_mul_f32 v150,v150,v218\nv_mul_f32 v151,v151,v219\ns_waitcnt lgkmcnt(11)\n"
"M_ 152,96,184\nv_cvt_pk_bf16_f32 v220,v148,v149\nv_cvt_pk_bf16_f32 v221,v150,v151\nM_ 156,100,184\nv_and_b32 v226,36864,v237\ns_lshr_b32 s2,s76,2\ns_lshl_b32 s2,s2,10\nv_add3_u32 v226,v226,v224,s2\n"
"v_xor_b32 v237,36864,v226\ns_waitcnt lgkmcnt(9)\nM_ 152,104,188\ns_add_u32 m0,s77,89856\ns_nop 0\nbuffer_load_dwordx4 v238,s[24:27],s94 offen offset:256 lds\ns_add_u32 s51,s51,1\ns_cmp_eq_u32 s51,9\n"
"s_cselect_b32 s3,0xfffaf000,0\ns_add_u32 s94,s94,36864\ns_add_u32 s94,s94,s3\nM_ 156,108,188\ns_waitcnt vmcnt(5)\ns_waitcnt lgkmcnt(0)\ns_barrier\nM_ 152,112,192\n"
"M_ 156,116,192\ns_add_u32 m0,s78,18432\ns_add_u32 s81,s83,18432\nbuffer_load_dwordx4 v227,s[72:75],s81 offen sc1 lds\nds_read_b128 v[192:195],v237\nM_ 152,120,196\nM_ 156,124,196\nds_read_b128 v[196:199],v237 offset:18432\n"
"M_ 152,128,200\ns_add_u32 m0,s78,18432\ns_nop 0\nbuffer_load_dwordx4 v228,s[72:75],s81 offen offset:1024 sc1 lds\nM_ 156,132,200\nds_read_b128 v[200:203],v237 offset:2048\nM_ 152,136,204\nM_ 156,140,204\n"
"s_add_u32 m0,s78,s79\ns_mov_b32 exec_hi,0\nbuffer_load_dwordx4 v229,s[72:75],s83 offen sc1 lds\ns_mov_b32 exec_hi,-1\ns_xor_b32 s78,s78,s82\nds_read_b128 v[204:207],v237 offset:20480\nds_read_b128 v[184:187],v237 offset:4096\ns_waitcnt lgkmcnt(4)\n"
"Z_ 108,192,0\nds_read_b128 v[188:191],v237 offset:22528\nZ_ 112,192,4\ns_mul_i32 s7,s88,18432\ns_add_u32 s7,s7,s50\ns_mov_b64 exec,15\nbuffer_store_dwordx4 v[234:237],v224,s[12:15],s7 offen sc1\ns_mov_b64 exec,-1\n"
"v_fmac_f32 v152,v212,v176\nv_fmac_f32 v153,v212,v177\nv_fmac_f32 v154,v212,v178\nZ_ 116,192,8\ns_mov_b32 s89,s88\ns_mov_b32 s88,s87\ns_mov_b32 s87,s86\ns_mov_b32 s86,s85\n"
"s_and_b32 s85,s64,63\ns_lshr_b64 s[64:65],s[64:65],6\ns_cmp_eq_u64 s[64:65],1\ns_cselect_b64 s[64:65],s[48:49],s[64:65]\ns_mul_i32 s83,s85,s90\ns_add_u32 s83,s83,s91\nds_read_b128 v[192:195],v237 offset:6144\nv_fmac_f32 v155,v212,v179\n"
"v_fmac_f32 v156,v212,v180\ns_waitcnt lgkmcnt(5)\nZ_ 120,196,0\ns_mul_i32 s7,s85,18432\nbuffer_load_dwordx2 v[232:233],v236,s[12:15],s7 offen sc1\nv_fmac_f32 v157,v212,v181\nv_fmac_f32 v158,v212,v182\nZ_ 124,196,4\n"
"v_fmac_f32 v159,v212,v183\nv_mul_f32 v216,v152,v152\nZ_ 128,196,8\ns_add_u32 m0,s77,98304\ns_nop 0\nbuffer_load_dwordx4 v238,s[24:27],s94 offen lds\nds_read_b128 v[196:199],v237 offset:24576\nv_mul_f32 v217,v153,v153\n"
"v_mul_f32 v218,v154,v154\ns_waitcnt lgkmcnt(5)\nM_ 108,200,12\nv_mul_f32 v219,v155,v155\nv_fma_f32 v216,v216,v214,v213\nM_ 112,200,16\nv_fma_f32 v217,v217,v214,v213\nv_fma_f32 v218,v218,v214,v213\n"
"M_ 116,200,20\ns_add_u32 m0,s77,106368\ns_nop 0\nbuffer_load_dwordx4 v238,s[24:27],s94 offen offset:128 lds\nv_fma_f32 v219,v219,v214,v213\nv_mul_f32 v216,v216,v152\ns_waitcnt lgkmcnt(4)\nM_ 120,204,12\n"
"v_mul_f32 v217,v217,v153\nv_mul_f32 v218,v218,v154\nM_ 124,204,16\nv_mul_f32 v219,v219,v155\nM_ 128,204,20\ns_waitcnt vmcnt(7)\ns_waitcnt lgkmcnt(0)\ns_barrier\n"
"v_exp_f32 v216,v216\nM_ 108,184,24\ns_add_u32 m0,s77,114432\ns_nop 0\nbuffer_load_dwordx4 v238,s[24:27],s94 offen offset:256 lds\ns_add_u32 s51,s51,1\ns_cmp_eq_u32 s51,9\ns_cselect_b32 s3,0xfffaf000,0\n"
"s_add_u32 s94,s94,36864\ns_add_u32 s94,s94,s3\nds_read_b64_tr_b16 v[36:37],v242\nds_read_b64_tr_b16 v[38:39],v242 offset:512\nds_read_b128 v[200:203],v226\nds_read_b128 v[204:207],v226 offset:2048\nv_exp_f32 v217,v217\nM_ 112,184,28\n"
"ds_read_b64_tr_b16 v[40:41],v243\nds_read_b64_tr_b16 v[42:43],v243 offset:512\nv_exp_f32 v218,v218\nM_ 116,184,32\nds_read_b64_tr_b16 v[44:45],v241\nds_read_b64_tr_b16 v[46:47],v241 offset:512\nds_read_b128 v[184:187],v237 offset:8192\nv_exp_f32 v219,v219\n"
"v_mul_f32 v156,v152,v156\nM_ 120,188,24\ns_add_u32 m0,s77,122880\ns_nop 0\nbuffer_load_dwordx4 v238,s[24:27],s94 offen lds\nv_mul_f32 v157,v153,v157\nv_mul_f32 v158,v154,v158\nM_ 124,188,28\n"
"v_mul_f32 v159,v155,v159\nv_fma_f32 v216,v216,v215,v215\nM_ 128,188,32\nds_read_b128 v[188:191],v237 offset:26624\nv_fma_f32 v217,v217,v215,v215\nv_fma_f32 v218,v218,v215,v215\ns_waitcnt lgkmcnt(8)\nM_ 108,192,36\n"
"s_add_u32 m0,s77,130944\ns_nop 0\nbuffer_load_dwordx4 v238,s[24:27],s94 offen offset:128 lds\nv_fma_f32 v219,v219,v215,v215\ns_waitcnt lgkmcnt(4)\nM_ 112,192,40\nv_rcp_f32 v216,v216\ns_waitcnt lgkmcnt(2)\n"
"M_ 116,192,44\nv_rcp_f32 v217,v217\nM_ 120,196,36\ns_add_u32 m0,s77,139008\ns_nop 0\nbuffer_load_dwordx4 v238,s[24:27],s94 offen offset:256 lds\ns_add_u32 s51,s51,1\ns_cmp_eq_u32 s51,9\n"
"s_cselect_b32 s3,0xfffaf000,0\ns_add_u32 s94,s94,36864\ns_add_u32 s94,s94,s3\nv_rcp_f32 v218,v218\nM_ 124,196,40\nv_rcp_f32 v219,v219\nv_mul_f32 v156,v156,v216\nM_ 128,196,44\n"
"v_mul_f32 v157,v157,v217\nv_mul_f32 v158,v158,v218\nZ_ 132,200,0\ns_waitcnt vmcnt(3)\ns_waitcnt lgkmcnt(0)\ns_barrier\nv_mul_f32 v159,v159,v219\nv_cvt_pk_bf16_f32 v222,v156,v157\n"
"Z_ 136,200,4\nds_read_b64_tr_b16 v[48:49],v242 offset:24576\nds_read_b64_tr_b16 v[50:51],v242 offset:25088\nds_read_b128 v[192:195],v226 offset:18432\nds_read_b128 v[196:199],v226 offset:20480\nv_cvt_pk_bf16_f32 v223,v158,v159\nv_permlane16_swap_b32 v220,v222\nZ_ 140,200,8\n"
"ds_read_b64_tr_b16 v[52:53],v243 offset:24576\nds_read_b64_tr_b16 v[54:55],v243 offset:25088\ns_add_u32 m0,s77,73728\ns_nop 0\nbuffer_load_dwordx4 v238,s[24:27],s94 offen lds\nds_read_b128 v[244:247],v237 offset:16384\nds_read_b128 v[248:251],v237 offset:34816\nv_permlane16_swap_b32 v221,v223\n"
"s_mul_i32 s7,s62,294912\nbuffer_store_dwordx4 v[220:223],v230,s[12:15],s7 offen sc1\nv_and_b32 v231,255,v224\nM_ 132,204,12\nds_read_b64_tr_b16 v[56:57],v241 offset:24576\nds_read_b64_tr_b16 v[58:59],v241 offset:25088\nds_read_b128 v[200:203],v237 offset:10240\nv_add_u32 v231,155648,v231\n"
"ds_read_b128 v[208:211],v231\nds_read_b128 v[212:215],v231 offset:256\ns_and_b32 s60,s60,255\nM_ 136,204,16\nM_ 140,204,20\nds_read_b128 v[204:207],v237 offset:28672\ns_waitcnt lgkmcnt(2)\nv_fmac_f32 v160,v208,v176\n"
"M_ 108,184,48\ns_add_u32 m0,s77,81792\ns_nop 0\nbuffer_load_dwordx4 v238,s[24:27],s94 offen offset:128 lds\nv_fmac_f32 v161,v208,v177\nv_fmac_f32 v162,v208,v178\nM_ 112,184,52\nv_fmac_f32 v163,v208,v179\n"
"v_fmac_f32 v164,v208,v180\nM_ 116,184,56\nds_read_b128 v[184:187],v226 offset:4096\nv_fmac_f32 v165,v208,v181\nv_fmac_f32 v166,v208,v182\nM_ 120,188,48\nv_fmac_f32 v167,v208,v183\nv_mul_f32 v216,v160,v160\n"
"M_ 124,188,52\ns_add_u32 m0,s77,89856\ns_nop 0\nbuffer_load_dwordx4 v238,s[24:27],s94 offen offset:256 lds\ns_add_u32 s51,s51,1\ns_cmp_eq_u32 s51,9\ns_cselect_b32 s3,0xfffaf000,0\ns_add_u32 s94,s94,36864\n"
"s_add_u32 s94,s94,s3\nv_mul_f32 v217,v161,v161\nv_mul_f32 v218,v162,v162\nM_ 128,188,56\nds_read_b128 v[188:191],v226 offset:22528\nv_mul_f32 v219,v163,v163\nv_fma_f32 v216,v216,v210,v209\nZ_ 144,192,0\n"
"v_fma_f32 v217,v217,v210,v209\nv_fma_f32 v218,v218,v210,v209\nZ_ 148,192,4\ns_waitcnt vmcnt(4)\ns_waitcnt lgkmcnt(0)\ns_barrier\nv_fma_f32 v219,v219,v210,v209\nv_mul_f32 v216,v216,v160\n"
"Z_ 152,192,8\nds_read_b64_tr_b16 v[60:61],v242 offset:49152\nds_read_b64_tr_b16 v[62:63],v242 offset:49664\nds_read_b128 v[192:195],v226 offset:6144\nv_mul_f32 v217,v217,v161\nv_mul_f32 v218,v218,v162\nM_ 144,196,12\nds_read_b64_tr_b16 v[64:65],v243 offset:49152\n"
"ds_read_b64_tr_b16 v[66:67],v243 offset:49664\ns_add_u32 m0,s77,98304\ns_nop 0\nbuffer_load_dwordx4 v238,s[24:27],s94 offen lds\nv_mul_f32 v219,v219,v163\nM_ 148,196,16\nds_read_b64_tr_b16 v[68:69],v241 offset:49152\nds_read_b64_tr_b16 v[70:71],v241 offset:49664\n"
"v_exp_f32 v216,v216\nM_ 152,196,20\nds_read_b128 v[196:199],v226 offset:24576\nv_exp_f32 v217,v217\ns_waitcnt lgkmcnt(6)\nM_ 108,200,60\nv_exp_f32 v218,v218\ns_waitcnt lgkmcnt(3)\n"
"M_ 112,200,64\ns_add_u32 m0,s77,106368\ns_nop 0\nbuffer_load_dwordx4 v238,s[24:27],s94 offen offset:128 lds\nv_exp_f32 v219,v219\nv_mul_f32 v164,v160,v164\ns_waitcnt lgkmcnt(1)\nM_ 116,200,68\n"
"ds_read_b128 v[200:203],v237 offset:12288\nv_mul_f32 v165,v161,v165\nv_mul_f32 v166,v162,v166\nM_ 120,204,60\nv_mul_f32 v167,v163,v167\nv_fma_f32 v216,v216,v211,v211\nM_ 124,204,64\nv_fma_f32 v217,v217,v211,v211\n"
"v_fma_f32 v218,v218,v211,v211\nM_ 128,204,68\ns_add_u32 m0,s77,114432\ns_nop 0\nbuffer_load_dwordx4 v238,s[24:27],s94 offen offset:256 lds\ns_add_u32 s51,s51,1\ns_cmp_eq_u32 s51,9\ns_cselect_b32 s3,0xfffaf000,0\n"
"s_add_u32 s94,s94,36864\ns_add_u32 s94,s94,s3\nds_read_b128 v[204:207],v237 offset:30720\nv_fma_f32 v219,v219,v211,v211\nM_ 132,184,24\nv_rcp_f32 v216,v216\nM_ 136,184,28\nv_rcp_f32 v217,v217\n"
"M_ 140,184,32\ns_waitcnt vmcnt(3)\ns_waitcnt lgkmcnt(0)\ns_barrier\nds_read_b128 v[184:187],v226 offset:8192\nv_rcp_f32 v218,v218\nM_ 144,188,24\nds_read_b64_tr_b16 v[72:73],v242\n"
"ds_read_b64_tr_b16 v[74:75],v242 offset:512\nv_rcp_f32 v219,v219\nv_mul_f32 v164,v164,v216\nM_ 148,188,28\nds_read_b64_tr_b16 v[76:77],v243\nds_read_b64_tr_b16 v[78:79],v243 offset:512\ns_add_u32 m0,s77,122880\ns_nop 0\n"
"buffer_load_dwordx4 v238,s[24:27],s94 offen lds\nv_mul_f32 v165,v165,v217\nv_mul_f32 v166,v166,v218\nM_ 152,188,32\nds_read_b64_tr_b16 v[80:81],v241\nds_read_b64_tr_b16 v[82:83],v241 offset:512\nds_read_b128 v[188:191],v226 offset:26624\nv_mul_f32 v167,v167,v219\n"
"v_cvt_pk_bf16_f32 v220,v164,v165\nM_ 132,192,36\ns_mul_i32 s7,s62,18432\ns_add_u32 s7,s7,s50\ns_mov_b64 exec,15\nbuffer_store_dwordx4 v[234:237],v224,s[12:15],s7 offen sc1\ns_mov_b64 exec,-1\nv_cvt_pk_bf16_f32 v221,v166,v167\n"
"v_fmac_f32 v168,v212,v176\nM_ 136,192,40\nv_fmac_f32 v169,v212,v177\nv_fmac_f32 v170,v212,v178\nM_ 140,192,44\ns_add_u32 m0,s77,130944\ns_nop 0\nbuffer_load_dwordx4 v238,s[24:27],s94 offen offset:128 lds\n"
"ds_read_b128 v[192:195],v237 offset:14336\nv_fmac_f32 v171,v212,v179\nv_fmac_f32 v172,v212,v180\nM_ 144,196,36\nv_fmac_f32 v173,v212,v181\nv_fmac_f32 v174,v212,v182\nM_ 148,196,40\nv_fmac_f32 v175,v212,v183\n"
"v_mul_f32 v216,v168,v168\nM_ 152,196,44\nds_read_b128 v[196:199],v237 offset:32768\nv_mul_f32 v217,v169,v169\nv_mul_f32 v218,v170,v170\ns_waitcnt lgkmcnt(7)\nM_ 108,200,72\ns_add_u32 m0,s77,139008\n"
"s_nop 0\nbuffer_load_dwordx4 v238,s[24:27],s94 offen offset:256 lds\ns_add_u32 s51,s51,1\ns_cmp_eq_u32 s51,9\ns_cselect_b32 s3,0xfffaf000,0\ns_add_u32 s94,s94,36864\ns_add_u32 s94,s94,s3\nv_cmp_ne_u64 vcc,v[232:233],v[234:235]\n"
"s_nop 0\ns_and_b64 s[4:5],vcc,s[92:93]\ns_cbranch_scc1 .sl20_%=\n.ok19_%=:\nv_mul_f32 v219,v171,v171\nv_fma_f32 v216,v216,v214,v213\ns_waitcnt lgkmcnt(5)\nM_ 112,200,76\n"
"v_fma_f32 v217,v217,v214,v213\nv_fma_f32 v218,v218,v214,v213\ns_waitcnt lgkmcnt(3)\nM_ 116,200,80\nds_read_b128 v[200:203],v226 offset:10240\nv_fma_f32 v219,v219,v214,v213\nv_mul_f32 v216,v216,v168\nM_ 120,204,72\n"
"s_waitcnt vmcnt(4)\ns_waitcnt lgkmcnt(0)\ns_barrier\nv_mul_f32 v217,v217,v169\nv_mul_f32 v218,v218,v170\nM_ 124,204,76\nds_read_b64_tr_b16 v[84:85],v242 offset:24576\nds_read_b64_tr_b16 v[86:87],v242 offset:25088\n"
"s_mov_b32 m0,s78\ns_nop 0\nbuffer_load_dwordx4 v227,s[72:75],s83 offen sc1 lds\nv_mul_f32 v219,v219,v171\nM_ 128,204,80\nds_read_b64_tr_b16 v[88:89],v243 offset:24576\nds_read_b64_tr_b16 v[90:91],v243 offset:25088\nds_read_b128 v[204:207],v226 offset:12288\n"
"v_exp_f32 v216,v216\nM_ 132,184,48\nds_read_b64_tr_b16 v[92:93],v241 offset:24576\nds_read_b64_tr_b16 v[94:95],v241 offset:25088\nv_exp_f32 v217,v217\nM_ 136,184,52\ns_mov_b32 m0,s78\ns_nop 0\n"
"buffer_load_dwordx4 v228,s[72:75],s83 offen offset:1024 sc1 lds\nv_exp_f32 v218,v218\nM_ 140,184,56\nds_read_b128 v[184:187],v226 offset:14336\nv_exp_f32 v219,v219\nv_mul_f32 v172,v168,v172\nM_ 144,188,48\nv_mul_f32 v173,v169,v173\n"
"v_mul_f32 v174,v170,v174\nM_ 148,188,52\ns_add_u32 m0,s78,18432\ns_add_u32 s81,s83,18432\nbuffer_load_dwordx4 v227,s[72:75],s81 offen sc1 lds\nv_mul_f32 v175,v171,v175\nv_fma_f32 v216,v216,v215,v215\nM_ 152,188,56\n"
"ds_read_b128 v[188:191],v226 offset:16384\nv_fma_f32 v217,v217,v215,v215\nv_fma_f32 v218,v218,v215,v215\ns_waitcnt lgkmcnt(7)\nM_ 108,192,84\nv_fma_f32 v219,v219,v215,v215\ns_waitcnt lgkmcnt(5)\nM_ 112,192,88\n"
"s_add_u32 m0,s78,18432\ns_nop 0\nbuffer_load_dwordx4 v228,s[72:75],s81 offen offset:1024 sc1 lds\nv_rcp_f32 v216,v216\ns_waitcnt lgkmcnt(2)\nM_ 116,192,92\nds_read_b128 v[192:195],v226 offset:28672\nv_rcp_f32 v217,v217\n"
"M_ 120,196,84\nv_rcp_f32 v218,v218\nM_ 124,196,88\ns_waitcnt vmcnt(4)\ns_waitcnt lgkmcnt(0)\ns_barrier\nv_rcp_f32 v219,v219\nv_mul_f32 v172,v172,v216\n"
"M_ 128,196,92\nds_read_b64_tr_b16 v[96:97],v242 offset:49152\nds_read_b64_tr_b16 v[98:99],v242 offset:49664\ns_add_u32 m0,s78,s79\ns_mov_b32 exec_hi,0\nbuffer_load_dwordx4 v229,s[72:75],s83 offen sc1 lds\ns_mov_b32 exec_hi,-1\ns_xor_b32 s78,s78,s82\n"
"ds_read_b128 v[196:199],v226 offset:30720\nv_mul_f32 v173,v173,v217\nv_mul_f32 v174,v174,v218\nM_ 132,200,60\nds_read_b64_tr_b16 v[100:101],v243 offset:49152\nds_read_b64_tr_b16 v[102:103],v243 offset:49664\ns_mov_b32 s89,s88\ns_mov_b32 s88,s87\n"
"s_mov_b32 s87,s86\ns_mov_b32 s86,s85\ns_and_b32 s85,s64,63\ns_lshr_b64 s[64:65],s[64:65],6\ns_cmp_eq_u64 s[64:65],1\ns_cselect_b64 s[64:65],s[48:49],s[64:65]\ns_mul_i32 s83,s85,s90\ns_add_u32 s83,s83,s91\n"
"v_mul_f32 v175,v175,v219\nv_cvt_pk_bf16_f32 v222,v172,v173\nM_ 136,200,64\nds_read_b64_tr_b16 v[104:105],v241 offset:49152\nds_read_b64_tr_b16 v[106:107],v241 offset:49664\ns_mul_i32 s7,s85,18432\nbuffer_load_dwordx2 v[232:233],v236,s[12:15],s7 offen sc1\nv_cvt_pk_bf16_f32 v223,v174,v175\n"
"v_permlane16_swap_b32 v220,v222\nM_ 140,200,68\nds_read_b128 v[200:203],v226 offset:32768\nv_permlane16_swap_b32 v221,v223\ns_mul_i32 s7,s60,294912\nbuffer_store_dwordx4 v[220:223],v230,s[12:15],s7 offen sc1\nM_ 132,204,72\nM_ 136,204,76\n"
"s_mul_i32 s2,s76,3072\ns_add_u32 s2,s2,73728\nv_add_u32 v239,s2,v224\ns_xor_b32 s2,s76,4\ns_mul_i32 s2,s2,3072\ns_add_u32 s2,s2,73728\nv_add_u32 v240,s2,v224\ns_lshr_b32 s59,s70,24\n"
"s_mov_b32 s54,0\ns_mov_b32 s55,0\nv_lshrrev_b32 v242,1,v224\nv_lshrrev_b32 v243,5,v224\nM_ 140,204,80\nds_read_b128 v[204:207],v226 offset:34816\nv_lshlrev_b32 v243,2,v243\nv_mov_b32 v238,v243\n"
"s_waitcnt lgkmcnt(7)\nM_ 108,244,96\nv_mov_b32 v222,0x3f80\nv_mov_b32 v223,0\ns_waitcnt lgkmcnt(4)\nM_ 112,244,100\nv_lshlrev_b64 v[222:223],v224,v[222:223]\nv_mov_b64 v[168:169],0\n"
"s_waitcnt lgkmcnt(2)\nM_ 116,244,104\nv_mov_b64 v[170:171],0\nv_mov_b64 v[172:173],0\nM_ 120,248,96\nv_mov_b64 v[174:175],0\nM_ 124,248,100\nds_write_b128 v239,v[108:111]\n"
"M_ 128,248,104\nds_write_b128 v239,v[112:115] offset:1024\nM_ 132,184,84\nds_write_b128 v239,v[116:119] offset:2048\nv_xor_b32 v239,32768,v239\nM_ 136,184,88\ns_waitcnt lgkmcnt(0)\ns_barrier\n"
"M_ 140,184,92\nds_read_b128 v[108:111],v240\nds_read_b128 v[112:115],v240 offset:1024\nds_read_b128 v[116:119],v240 offset:2048\nv_xor_b32 v240,32768,v240\nM_ 132,188,96\nM_ 136,188,100\nM_ 140,188,104\n"
"s_mul_i32 s2,s88,442368\ns_add_u32 s2,s2,s100\ns_waitcnt lgkmcnt(2)\nv_add_f32 v120,v108,v120\nv_add_f32 v121,v109,v121\nv_add_f32 v122,v110,v122\nM_ 144,192,60\ns_waitcnt vmcnt(1)\n"
"v_cmp_ne_u64 vcc,v[232:233],v[234:235]\ns_nop 0\ns_and_b64 s[4:5],vcc,s[92:93]\ns_cbranch_scc1 .sl23_%=\n.ok22_%=:\nv_add_f32 v123,v111,v123\ns_waitcnt lgkmcnt(1)\nv_add_f32 v124,v112,v124\n"
"M_ 148,192,64\nds_write_b128 v239,v[132:135]\nds_write_b128 v239,v[136:139] offset:1024\nv_add_f32 v125,v113,v125\nv_add_f32 v126,v114,v126\nM_ 152,192,68\nds_write_b128 v239,v[140:143] offset:2048\nv_xor_b32 v239,32768,v239\n"
"v_add_f32 v127,v115,v127\nv_cvt_pk_bf16_f32 v160,v120,v121\nM_ 144,196,72\ns_waitcnt vmcnt(0)\ns_waitcnt lgkmcnt(0)\ns_barrier\nv_cvt_pk_bf16_f32 v161,v122,v123\nv_cvt_pk_bf16_f32 v162,v124,v125\n"
"M_ 148,196,76\nds_read_b128 v[184:187],v237\nds_read_b128 v[188:191],v237 offset:2048\nv_cvt_pk_bf16_f32 v163,v126,v127\nbuffer_store_dwordx4 v[160:163],v224,s[12:15],s2 offen sc1\nv_add3_u32 v166,v160,v161,v162\nM_ 152,196,80\ns_mov_b32 m0,s78\n"
"s_nop 0\nbuffer_load_dwordx4 v227,s[72:75],s83 offen sc1 lds\nds_read_b128 v[192:195],v237 offset:4096\nds_read_b128 v[196:199],v237 offset:6144\nv_add_f32 v128,v116,v128\nv_add_f32 v129,v117,v129\nM_ 144,200,84\ns_mul_i32 s7,s60,18432\n"
"s_add_u32 s7,s7,s50\ns_mov_b64 exec,15\nbuffer_store_dwordx4 v[234:237],v224,s[12:15],s7 offen sc1\ns_mov_b64 exec,-1\nv_add_f32 v130,v118,v130\nv_add_f32 v131,v119,v131\nM_ 148,200,88\nv_cvt_pk_bf16_f32 v164,v128,v129\n"
"v_cvt_pk_bf16_f32 v165,v130,v131\nM_ 152,200,92\ns_mov_b32 m0,s78\ns_nop 0\nbuffer_load_dwordx4 v228,s[72:75],s83 offen offset:1024 sc1 lds\nv_add3_u32 v166,v166,v163,v164\nv_add3_u32 v166,v166,v165,s96\nbuffer_store_dwordx2 v[164:165],v242,s[12:15],s2 offen offset:1024 sc1\n"
"M_ 144,204,96\nv_add_u32_dpp v166,v166,v166 quad_perm:[1,0,3,2] row_mask:0xf bank_mask:0xf\nbuffer_store_dword v166,v238,s[12:15],s2 offen offset:1536 sc1\nv_mov_b64 v[120:121],0\nM_ 148,204,100\nv_mov_b64 v[122:123],0\nv_mov_b64 v[124:125],0\nM_ 152,204,104\n"
"s_add_u32 m0,s78,18432\ns_add_u32 s81,s83,18432\nbuffer_load_dwordx4 v227,s[72:75],s81 offen sc1 lds\nv_mov_b64 v[126:127],0\nv_mov_b64 v[128:129],0\nv_mov_b64 v[130:131],0\nds_read_b128 v[108:111],v240\nds_read_b128 v[112:115],v240 offset:1024\n"
"ds_read_b128 v[116:119],v240 offset:2048\nv_xor_b32 v240,32768,v240\ns_nop 7\n.p2_%=:\ns_waitcnt lgkmcnt(5)\nZ_ 132,184,0\nds_read_b128 v[200:203],v237 offset:8192\nds_read_b128 v[204:207],v237 offset:10240\n"
"Z_ 136,184,4\ns_sub_u32 s59,s59,1\nZ_ 140,184,8\nbuffer_load_dwordx4 v228,s[72:75],s81 offen offset:1024 sc1 lds\nds_read_b128 v[184:187],v237 offset:12288\nM_ 132,188,12\ns_cmp_eq_u32 s59,0\ns_cselect_b64 s[92:93],0,s[92:93]\n"
"s_cselect_b32 s91,0x80000000,s91\nM_ 136,188,16\ns_add_u32 m0,s78,s79\ns_xor_b32 s78,s78,s82\ns_mul_i32 s2,s87,442368\ns_add_u32 s2,s2,s100\ns_waitcnt lgkmcnt(5)\nv_add_f32 v144,v108,v144\n"
"v_add_f32 v145,v109,v145\nM_ 140,188,20\ns_mov_b32 exec_hi,0\nbuffer_load_dwordx4 v229,s[72:75],s83 offen sc1 lds\ns_mov_b32 exec_hi,-1\nds_read_b128 v[188:191],v237 offset:14336\nM_ 132,192,24\ns_mov_b32 s89,s88\n"
"s_mov_b32 s88,s87\ns_mov_b32 s87,s86\ns_mov_b32 s86,s85\nM_ 136,192,28\ns_and_b32 s85,s64,63\ns_lshr_b64 s[64:65],s[64:65],6\nv_add_f32 v146,v110,v146\nv_add_f32 v147,v111,v147\n"
"M_ 140,192,32\ns_cmp_eq_u32 s92,0\ns_cbranch_scc0 .so25_%=\n.sb26_%=:\nds_read_b128 v[192:195],v237 offset:16384\ns_waitcnt lgkmcnt(6)\nv_add_f32 v148,v112,v148\nM_ 132,196,36\n"
"v_add_f32 v149,v113,v149\nv_add_f32 v150,v114,v150\nv_add_f32 v151,v115,v151\nv_cvt_pk_bf16_f32 v160,v144,v145\nM_ 136,196,40\nv_cvt_pk_bf16_f32 v161,v146,v147\nv_cvt_pk_bf16_f32 v162,v148,v149\nM_ 140,196,44\n"
"ds_read_b128 v[196:199],v237 offset:18432\nv_cvt_pk_bf16_f32 v163,v150,v151\nbuffer_store_dwordx4 v[160:163],v224,s[12:15],s2 offen sc1\nv_add3_u32 v166,v160,v161,v162\ns_waitcnt lgkmcnt(4)\nM_ 132,200,48\nv_add_f32 v152,v116,v152\nv_add_f32 v153,v117,v153\n"
"M_ 136,200,52\ns_cmp_eq_u64 s[64:65],1\ns_cselect_b64 s[64:65],s[48:49],s[64:65]\nv_add_f32 v154,v118,v154\nv_add_f32 v155,v119,v155\nM_ 140,200,56\ns_mul_i32 s83,s85,s90\ns_add_u32 s83,s83,s91\n"
"ds_read_b128 v[200:203],v237 offset:20480\nv_cvt_pk_bf16_f32 v164,v152,v153\nM_ 132,204,60\nv_cvt_pk_bf16_f32 v165,v154,v155\nv_add3_u32 v166,v166,v163,v164\nv_add3_u32 v166,v166,v165,s96\nbuffer_store_dwordx2 v[164:165],v242,s[12:15],s2 offen offset:1024 sc1\nM_ 136,204,64\n"
"v_add_u32_dpp v166,v166,v166 quad_perm:[1,0,3,2] row_mask:0xf bank_mask:0xf\nbuffer_store_dword v166,v238,s[12:15],s2 offen offset:1536 sc1\nM_ 140,204,68\nds_read_b128 v[204:207],v237 offset:22528\ns_waitcnt lgkmcnt(4)\nM_ 132,184,72\nM_ 136,184,76\nM_ 140,184,80\n"
"ds_read_b128 v[184:187],v237 offset:24576\nM_ 132,188,84\nM_ 136,188,88\nM_ 140,188,92\nds_read_b128 v[188:191],v237 offset:26624\ns_waitcnt lgkmcnt(4)\nM_ 132,192,96\ns_waitcnt vmcnt(8)\n"
"v_add3_u32 v175,v168,v169,v170\nv_add3_u32 v175,v175,v171,v172\nM_ 136,192,100\nv_add3_u32 v175,v175,v173,s54\nM_ 140,192,104\nds_read_b128 v[192:195],v237 offset:28672\nv_add_u32_dpp v175,v175,v175 quad_perm:[1,0,3,2] row_mask:0xf bank_mask:0xf\nv_cmp_ne_u32 vcc,v175,v174\n"
"Z_ 144,196,0\ns_cmp_lg_u64 vcc,0\ns_cselect_b32 s3,0,s55\ns_ashr_i64 s[68:69],s[68:69],s3\nZ_ 148,196,4\ns_cmp_eq_u32 s3,0\ns_cbranch_scc1 .sk27_%=\nv_mfma_f32_4x4x4_16b_bf16 v[120:123],v[222:223],v[168:169],v[120:123]\n"
"v_mfma_f32_4x4x4_16b_bf16 v[124:127],v[222:223],v[170:171],v[124:127]\nv_mfma_f32_4x4x4_16b_bf16 v[128:131],v[222:223],v[172:173],v[128:131]\n.sk27_%=:\nZ_ 152,196,8\nds_read_b128 v[196:199],v237 offset:30720\ns_waitcnt lgkmcnt(4)\nM_ 144,200,12\nds_write_b128 v239,v[132:135]\n"
"M_ 148,200,16\nM_ 152,200,20\nds_read_b128 v[200:203],v237 offset:32768\nM_ 144,204,24\nds_write_b128 v239,v[136:139] offset:1024\nM_ 148,204,28\nM_ 152,204,32\nds_read_b128 v[204:207],v237 offset:34816\n"
"s_waitcnt lgkmcnt(6)\nM_ 144,184,36\nds_write_b128 v239,v[140:143] offset:2048\nM_ 148,184,40\ns_bfe_u32 s53,s68,262144\nM_ 152,184,44\ns_cmp_gt_u32 s59,2\ns_cselect_b32 s53,15,s53\n"
"s_cselect_b32 s55,0,4\nv_xor_b32 v239,32768,v239\nM_ 144,188,48\ns_cmp_eq_u32 s53,15\ns_cselect_b32 s54,0,s96\ns_cselect_b32 s3,0x80000000,s71\ns_cmp_eq_u32 s92,0\ns_cbranch_scc0 .so28_%=\n"
".sb29_%=:\nM_ 148,188,52\ns_mul_i32 s53,s53,2048\ns_add_u32 s53,s53,s3\nM_ 152,188,56\nbuffer_load_dwordx4 v[168:171],v224,s[12:15],s53 offen sc1\ns_waitcnt lgkmcnt(5)\nM_ 144,192,60\n"
"M_ 148,192,64\nbuffer_load_dwordx2 v[172:173],v242,s[12:15],s53 offen offset:1024 sc1\nM_ 152,192,68\nbuffer_load_dword v174,v243,s[12:15],s53 offen offset:1536 sc1\nv_xor_b32 v237,36864,v237\nM_ 144,196,72\ns_waitcnt vmcnt(6) lgkmcnt(0)\ns_barrier\n"
"M_ 148,196,76\ns_mov_b32 m0,s78\nds_read_b128 v[184:187],v237\nds_read_b128 v[188:191],v237 offset:2048\nM_ 152,196,80\nbuffer_load_dwordx4 v227,s[72:75],s83 offen sc1 lds\nds_read_b128 v[192:195],v237 offset:4096\nds_read_b128 v[196:199],v237 offset:6144\n"
"M_ 144,200,84\nM_ 148,200,88\nM_ 152,200,92\nbuffer_load_dwordx4 v228,s[72:75],s83 offen offset:1024 sc1 lds\nM_ 144,204,96\nds_read_b128 v[108:111],v240\nM_ 148,204,100\ns_add_u32 m0,s78,18432\n"
"s_add_u32 s81,s83,18432\nds_read_b128 v[112:115],v240 offset:1024\nM_ 152,204,104\nbuffer_load_dwordx4 v227,s[72:75],s81 offen sc1 lds\nds_read_b128 v[116:119],v240 offset:2048\nv_xor_b32 v240,32768,v240\ns_cmp_lg_u32 s59,0\ns_cbranch_scc1 .p2_%=\n"
".p2x_%=:\ns_waitcnt lgkmcnt(5)\nZ_ 132,184,0\nds_read_b128 v[200:203],v237 offset:8192\nds_read_b128 v[204:207],v237 offset:10240\nZ_ 136,184,4\nZ_ 140,184,8\nds_read_b128 v[184:187],v237 offset:12288\n"
"s_mul_i32 s2,s87,442368\ns_add_u32 s2,s2,s100\ns_waitcnt lgkmcnt(5)\nv_add_f32 v144,v108,v144\nv_add_f32 v145,v109,v145\nv_add_f32 v146,v110,v146\nM_ 132,188,12\nv_add_f32 v147,v111,v147\n"
"s_waitcnt lgkmcnt(4)\nv_add_f32 v148,v112,v148\nM_ 136,188,16\nv_add_f32 v149,v113,v149\nv_add_f32 v150,v114,v150\nM_ 140,188,20\nds_read_b128 v[188:191],v237 offset:14336\nv_add_f32 v151,v115,v151\n"
"v_cvt_pk_bf16_f32 v160,v144,v145\nM_ 132,192,24\nv_cvt_pk_bf16_f32 v161,v146,v147\nv_cvt_pk_bf16_f32 v162,v148,v149\nM_ 136,192,28\nv_cvt_pk_bf16_f32 v163,v150,v151\nbuffer_store_dwordx4 v[160:163],v224,s[12:15],s2 offen sc1\nv_add3_u32 v166,v160,v161,v162\n"
"M_ 140,192,32\nds_read_b128 v[192:195],v237 offset:16384\ns_waitcnt lgkmcnt(5)\nv_add_f32 v152,v116,v152\nv_add_f32 v153,v117,v153\nM_ 132,196,36\nv_add_f32 v154,v118,v154\nv_add_f32 v155,v119,v155\n"
"M_ 136,196,40\nv_cvt_pk_bf16_f32 v164,v152,v153\nv_cvt_pk_bf16_f32 v165,v154,v155\nM_ 140,196,44\nds_read_b128 v[196:199],v237 offset:18432\nv_add3_u32 v166,v166,v163,v164\nv_add3_u32 v166,v166,v165,s96\nbuffer_store_dwordx2 v[164:165],v242,s[12:15],s2 offen offset:1024 sc1\n"
"s_waitcnt lgkmcnt(4)\nM_ 132,200,48\nv_add_u32_dpp v166,v166,v166 quad_perm:[1,0,3,2] row_mask:0xf bank_mask:0xf\nbuffer_store_dword v166,v238,s[12:15],s2 offen offset:1536 sc1\nM_ 136,200,52\nM_ 140,200,56\ns_waitcnt vmcnt(6)\nds_read_b128 v[200:203],v237 offset:20480\n"
"v_add3_u32 v175,v168,v169,v170\nv_add3_u32 v175,v175,v171,v172\nM_ 132,204,60\nv_add3_u32 v175,v175,v173,s54\nM_ 136,204,64\nM_ 140,204,68\nds_read_b128 v[204:207],v237 offset:22528\nv_add_u32_dpp v175,v175,v175 quad_perm:[1,0,3,2] row_mask:0xf bank_mask:0xf\n"
"v_cmp_ne_u32 vcc,v175,v174\ns_waitcnt lgkmcnt(4)\nM_ 132,184,72\ns_cmp_lg_u64 vcc,0\ns_cselect_b32 s3,0,s55\ns_ashr_i64 s[68:69],s[68:69],s3\nM_ 136,184,76\ns_cmp_eq_u32 s3,0\n"
"s_cbranch_scc1 .sk33_%=\nv_mfma_f32_4x4x4_16b_bf16 v[120:123],v[222:223],v[168:169],v[120:123]\nv_mfma_f32_4x4x4_16b_bf16 v[124:127],v[222:223],v[170:171],v[124:127]\nv_mfma_f32_4x4x4_16b_bf16 v[128:131],v[222:223],v[172:173],v[128:131]\n.sk33_%=:\nM_ 140,184,80\nds_read_b128 v[184:187],v237 offset:24576\nM_ 132,188,84\n"
"M_ 136,188,88\nM_ 140,188,92\nds_read_b128 v[188:191],v237 offset:26624\ns_waitcnt lgkmcnt(4)\nM_ 132,192,96\nM_ 136,192,100\nM_ 140,192,104\ns_bfe_u32 s16,s68,262144\n"
"s_cmp_eq_u32 s16,15\ns_cselect_b32 s23,0,s96\ns_cselect_b32 s7,0x80000000,s71\ns_mul_i32 s16,s16,2048\ns_add_u32 s16,s16,s7\nbuffer_load_dwordx4 v[244:247],v224,s[12:15],s16 offen sc1\nds_read_b128 v[192:195],v237 offset:28672\nZ_ 144,196,0\n"
"Z_ 148,196,4\nbuffer_load_dwordx2 v[248:249],v242,s[12:15],s16 offen offset:1024 sc1\nbuffer_load_dword v250,v243,s[12:15],s16 offen offset:1536 sc1\nZ_ 152,196,8\nds_read_b128 v[196:199],v237 offset:30720\ns_waitcnt lgkmcnt(4)\nM_ 144,200,12\nds_write_b128 v239,v[132:135]\n"
"s_bfe_u32 s17,s68,262148\ns_cmp_eq_u32 s17,15\ns_cselect_b32 s24,0,s96\ns_cselect_b32 s7,0x80000000,s71\ns_mul_i32 s17,s17,2048\ns_add_u32 s17,s17,s7\nbuffer_load_dwordx4 v[252:255],v224,s[12:15],s17 offen sc1\nM_ 148,200,16\n"
"M_ 152,200,20\nbuffer_load_dwordx2 v[156:157],v242,s[12:15],s17 offen offset:1024 sc1\nbuffer_load_dword v158,v243,s[12:15],s17 offen offset:1536 sc1\nds_read_b128 v[200:203],v237 offset:32768\nM_ 144,204,24\nds_write_b128 v239,v[136:139] offset:1024\nM_ 148,204,28\nM_ 152,204,32\n"
"ds_read_b128 v[204:207],v237 offset:34816\ns_waitcnt lgkmcnt(6)\nM_ 144,184,36\nds_write_b128 v239,v[140:143] offset:2048\nM_ 148,184,40\nM_ 152,184,44\nM_ 144,188,48\ns_waitcnt lgkmcnt(0)\n"
"s_barrier\ns_bfe_u32 s18,s68,262152\ns_cmp_eq_u32 s18,15\ns_cselect_b32 s25,0,s96\ns_cselect_b32 s7,0x80000000,s71\ns_mul_i32 s18,s18,2048\ns_add_u32 s18,s18,s7\nbuffer_load_dwordx4 v[208:211],v224,s[12:15],s18 offen sc1\n"
"M_ 148,188,52\nds_read_b128 v[108:111],v240\nbuffer_load_dwordx2 v[212:213],v242,s[12:15],s18 offen offset:1024 sc1\nbuffer_load_dword v214,v243,s[12:15],s18 offen offset:1536 sc1\nM_ 152,188,56\nds_read_b128 v[112:115],v240 offset:1024\ns_bfe_u32 s19,s68,262156\ns_cmp_eq_u32 s19,15\n"
"s_cselect_b32 s26,0,s96\ns_cselect_b32 s7,0x80000000,s71\ns_mul_i32 s19,s19,2048\ns_add_u32 s19,s19,s7\nbuffer_load_dwordx4 v[176:179],v224,s[12:15],s19 offen sc1\nM_ 144,192,60\nds_read_b128 v[116:119],v240 offset:2048\nbuffer_load_dwordx2 v[180:181],v242,s[12:15],s19 offen offset:1024 sc1\n"
"buffer_load_dword v182,v243,s[12:15],s19 offen offset:1536 sc1\nv_lshrrev_b32 v221,8,v224\nv_mul_u32_u24 v220,9216,v221\nv_bfe_u32 v221,v224,4,4\nM_ 148,192,64\ns_bfe_u32 s20,s68,262160\ns_cmp_eq_u32 s20,15\ns_cselect_b32 s27,0,s96\n"
"s_cselect_b32 s7,0x80000000,s71\ns_mul_i32 s20,s20,2048\ns_add_u32 s20,s20,s7\nbuffer_load_dwordx4 v[168:171],v224,s[12:15],s20 offen sc1\nv_lshl_add_u32 v220,v221,2,v220\nM_ 152,192,68\nbuffer_load_dwordx2 v[172:173],v242,s[12:15],s20 offen offset:1024 sc1\nbuffer_load_dword v174,v243,s[12:15],s20 offen offset:1536 sc1\n"
"M_ 144,196,72\nM_ 148,196,76\nM_ 152,196,80\nM_ 144,200,84\nM_ 148,200,88\nM_ 152,200,92\nM_ 144,204,96\nM_ 148,204,100\n"
"M_ 152,204,104\ns_bitcmp1_b32 s70,6\ns_cbranch_scc0 .noown_%=\ns_mul_i32 s2,s86,73728\ns_bfe_u32 s3,s70,131076\ns_mul_i32 s3,s3,768\ns_add_u32 s2,s2,s3\ns_lshr_b32 s3,s76,2\n"
"s_mul_i32 s3,s3,36864\ns_add_u32 s2,s2,s3\ns_and_b32 s3,s76,3\ns_mul_i32 s3,s3,192\ns_add_u32 s2,s2,s3\ns_add_u32 s3,s2,4608\ns_waitcnt lgkmcnt(2)\nv_pk_add_f32 v[144:145],v[108:109],v[144:145]\n"
"v_pk_add_f32 v[146:147],v[110:111],v[146:147]\ns_waitcnt lgkmcnt(1)\nv_pk_add_f32 v[148:149],v[112:113],v[148:149]\nv_pk_add_f32 v[150:151],v[114:115],v[150:151]\ns_waitcnt lgkmcnt(0)\nv_pk_add_f32 v[152:153],v[116:117],v[152:153]\nv_pk_add_f32 v[154:155],v[118:119],v[154:155]\ns_mov_b32 s6,200000\n"
".tl_%=:\ns_bitcmp1_b32 s16,31\ns_cbranch_scc1 .tle_%=\ns_waitcnt vmcnt(12)\n.tc34_%=:\nv_add3_u32 v251,v244,v245,v246\nv_add3_u32 v251,v251,v247,v248\nv_add3_u32 v251,v251,v249,s23\n"
"s_nop 1\nv_add_u32_dpp v251,v251,v251 quad_perm:[1,0,3,2] row_mask:0xf bank_mask:0xf\nv_cmp_ne_u32 vcc,v251,v250\ns_nop 0\ns_cmp_lg_u64 vcc,0\ns_cbranch_scc1 .ts35_%=\n.to36_%=:\nv_mfma_f32_4x4x4_16b_bf16 v[120:123],v[222:223],v[244:245],v[120:123]\n"
"v_mfma_f32_4x4x4_16b_bf16 v[124:127],v[222:223],v[246:247],v[124:127]\nv_mfma_f32_4x4x4_16b_bf16 v[128:131],v[222:223],v[248:249],v[128:131]\ns_bitcmp1_b32 s17,31\ns_cbranch_scc1 .tle_%=\ns_waitcnt vmcnt(9)\n.tc37_%=:\nv_add3_u32 v159,v252,v253,v254\nv_add3_u32 v159,v159,v255,v156\n"
"v_add3_u32 v159,v159,v157,s24\ns_nop 1\nv_add_u32_dpp v159,v159,v159 quad_perm:[1,0,3,2] row_mask:0xf bank_mask:0xf\nv_cmp_ne_u32 vcc,v159,v158\ns_nop 0\ns_cmp_lg_u64 vcc,0\ns_cbranch_scc1 .ts38_%=\n.to39_%=:\n"
"v_mfma_f32_4x4x4_16b_bf16 v[120:123],v[222:223],v[252:253],v[120:123]\nv_mfma_f32_4x4x4_16b_bf16 v[124:127],v[222:223],v[254:255],v[124:127]\nv_mfma_f32_4x4x4_16b_bf16 v[128:131],v[222:223],v[156:157],v[128:131]\ns_bitcmp1_b32 s18,31\ns_cbranch_scc1 .tle_%=\ns_waitcnt vmcnt(6)\n.tc40_%=:\nv_add3_u32 v215,v208,v209,v210\n"
"v_add3_u32 v215,v215,v211,v212\nv_add3_u32 v215,v215,v213,s25\ns_nop 1\nv_add_u32_dpp v215,v215,v215 quad_perm:[1,0,3,2] row_mask:0xf bank_mask:0xf\nv_cmp_ne_u32 vcc,v215,v214\ns_nop 0\ns_cmp_lg_u64 vcc,0\ns_cbranch_scc1 .ts41_%=\n"
".to42_%=:\nv_mfma_f32_4x4x4_16b_bf16 v[120:123],v[222:223],v[208:209],v[120:123]\nv_mfma_f32_4x4x4_16b_bf16 v[124:127],v[222:223],v[210:211],v[124:127]\nv_mfma_f32_4x4x4_16b_bf16 v[128:131],v[222:223],v[212:213],v[128:131]\ns_bitcmp1_b32 s19,31\ns_cbranch_scc1 .tle_%=\ns_waitcnt vmcnt(3)\n.tc43_%=:\n"
"v_add3_u32 v183,v176,v177,v178\nv_add3_u32 v183,v183,v179,v180\nv_add3_u32 v183,v183,v181,s26\ns_nop 1\nv_add_u32_dpp v183,v183,v183 quad_perm:[1,0,3,2] row_mask:0xf bank_mask:0xf\nv_cmp_ne_u32 vcc,v183,v182\ns_nop 0\ns_cmp_lg_u64 vcc,0\n"
"s_cbranch_scc1 .ts44_%=\n.to45_%=:\nv_mfma_f32_4x4x4_16b_bf16 v[120:123],v[222:223],v[176:177],v[120:123]\nv_mfma_f32_4x4x4_16b_bf16 v[124:127],v[222:223],v[178:179],v[124:127]\nv_mfma_f32_4x4x4_16b_bf16 v[128:131],v[222:223],v[180:181],v[128:131]\ns_bitcmp1_b32 s20,31\ns_cbranch_scc1 .tle_%=\ns_waitcnt vmcnt(0)\n"
".tc46_%=:\nv_add3_u32 v175,v168,v169,v170\nv_add3_u32 v175,v175,v171,v172\nv_add3_u32 v175,v175,v173,s27\ns_nop 1\nv_add_u32_dpp v175,v175,v175 quad_perm:[1,0,3,2] row_mask:0xf bank_mask:0xf\nv_cmp_ne_u32 vcc,v175,v174\ns_nop 0\n"
"s_cmp_lg_u64 vcc,0\ns_cbranch_scc1 .ts47_%=\n.to48_%=:\nv_mfma_f32_4x4x4_16b_bf16 v[120:123],v[222:223],v[168:169],v[120:123]\nv_mfma_f32_4x4x4_16b_bf16 v[124:127],v[222:223],v[170:171],v[124:127]\nv_mfma_f32_4x4x4_16b_bf16 v[128:131],v[222:223],v[172:173],v[128:131]\ns_ashr_i64 s[68:69],s[68:69],20\ns_cmp_lg_u64 s[68:69],-1\n"
"s_cbranch_scc1 .tl2_%=\n.tle_%=:\ns_nop 3\nv_pk_add_f32 v[120:121],v[120:121],v[144:145]\nv_pk_add_f32 v[122:123],v[122:123],v[146:147]\nv_pk_add_f32 v[124:125],v[124:125],v[148:149]\nv_pk_add_f32 v[126:127],v[126:127],v[150:151]\nv_pk_add_f32 v[128:129],v[128:129],v[152:153]\n"
"v_pk_add_f32 v[130:131],v[130:131],v[154:155]\nbuffer_store_dword v120,v220,s[28:31],s2 offen\nbuffer_store_dword v124,v220,s[28:31],s2 offen offset:64\nbuffer_store_dword v128,v220,s[28:31],s2 offen offset:128\nbuffer_store_dword v121,v220,s[28:31],s2 offen offset:2304\nbuffer_store_dword v125,v220,s[28:31],s2 offen offset:2368\nbuffer_store_dword v129,v220,s[28:31],s2 offen offset:2432\nbuffer_store_dword v122,v220,s[28:31],s3 offen\n"
"buffer_store_dword v126,v220,s[28:31],s3 offen offset:64\nbuffer_store_dword v130,v220,s[28:31],s3 offen offset:128\nbuffer_store_dword v123,v220,s[28:31],s3 offen offset:2304\nbuffer_store_dword v127,v220,s[28:31],s3 offen offset:2368\nbuffer_store_dword v131,v220,s[28:31],s3 offen offset:2432\ns_endpgm\n.noown_%=:\ns_mul_i32 s2,s86,442368\n"
"s_add_u32 s2,s2,s100\ns_waitcnt lgkmcnt(2)\ns_nop 0\nv_add_f32 v144,v108,v144\nv_add_f32 v145,v109,v145\nv_add_f32 v146,v110,v146\nv_add_f32 v147,v111,v147\ns_waitcnt lgkmcnt(1)\n"
"v_add_f32 v148,v112,v148\nv_add_f32 v149,v113,v149\nv_add_f32 v150,v114,v150\nv_add_f32 v151,v115,v151\nv_cvt_pk_bf16_f32 v160,v144,v145\nv_cvt_pk_bf16_f32 v161,v146,v147\nv_cvt_pk_bf16_f32 v162,v148,v149\nv_cvt_pk_bf16_f32 v163,v150,v151\n"
"buffer_store_dwordx4 v[160:163],v224,s[12:15],s2 offen sc1\nv_add3_u32 v166,v160,v161,v162\ns_waitcnt lgkmcnt(0)\nv_add_f32 v152,v116,v152\nv_add_f32 v153,v117,v153\nv_add_f32 v154,v118,v154\nv_add_f32 v155,v119,v155\nv_cvt_pk_bf16_f32 v164,v152,v153\n"
"v_cvt_pk_bf16_f32 v165,v154,v155\nv_add3_u32 v166,v166,v163,v164\nv_add3_u32 v166,v166,v165,s96\nbuffer_store_dwordx2 v[164:165],v242,s[12:15],s2 offen offset:1024 sc1\ns_nop 0\nv_add_u32_dpp v166,v166,v166 quad_perm:[1,0,3,2] row_mask:0xf bank_mask:0xf\nbuffer_store_dword v166,v238,s[12:15],s2 offen offset:1536 sc1\ns_endpgm\n"
".spec_%=:\nds_read_b128 v[212:215],v231 offset:256\nv_and_b32 v240,255,v224\nv_add_u32 v240,155648,v240\ns_and_b32 s60,s60,255\nds_read_b128 v[244:247],v240\nds_read_b128 v[248:251],v240 offset:256\ns_waitcnt lgkmcnt(7)\n"
"Z_ 152,0,184\nds_read_b128 v[204:207],v226 offset:22528\nv_fmac_f32 v144,v208,v176\nv_fmac_f32 v145,v208,v177\nv_fmac_f32 v146,v208,v178\nv_fmac_f32 v147,v208,v179\nZ_ 156,4,184\nds_read_b128 v[184:187],v237 offset:24576\n"
"v_fmac_f32 v148,v208,v180\nv_fmac_f32 v149,v208,v181\nv_fmac_f32 v150,v208,v182\nv_fmac_f32 v151,v208,v183\nv_mul_f32 v216,v144,v144\ns_waitcnt lgkmcnt(8)\nM_ 152,8,188\nv_mul_f32 v217,v145,v145\n"
"v_mul_f32 v218,v146,v146\nv_mul_f32 v219,v147,v147\nv_fma_f32 v216,v216,v210,v209\nM_ 156,12,188\nds_read_b128 v[188:191],v226 offset:24576\nv_fma_f32 v217,v217,v210,v209\nv_fma_f32 v218,v218,v210,v209\nv_fma_f32 v219,v219,v210,v209\n"
"v_mul_f32 v216,v216,v144\nv_mul_f32 v217,v217,v145\ns_waitcnt lgkmcnt(8)\nM_ 152,16,192\nv_mul_f32 v218,v218,v146\nv_mul_f32 v219,v219,v147\nv_exp_f32 v216,v216\nM_ 156,20,192\n"
"ds_read_b128 v[192:195],v237 offset:26624\nv_exp_f32 v217,v217\nv_exp_f32 v218,v218\ns_waitcnt lgkmcnt(8)\nM_ 152,24,196\nv_exp_f32 v219,v219\nv_mul_f32 v148,v144,v148\nv_mul_f32 v149,v145,v149\n"
"v_mul_f32 v150,v146,v150\nM_ 156,28,196\nds_read_b128 v[196:199],v226 offset:26624\nv_mul_f32 v151,v147,v151\nv_fma_f32 v216,v216,v211,v211\nv_fma_f32 v217,v217,v211,v211\nv_fma_f32 v218,v218,v211,v211\nv_fma_f32 v219,v219,v211,v211\n"
"s_waitcnt lgkmcnt(8)\nM_ 152,32,200\nv_rcp_f32 v216,v216\nv_rcp_f32 v217,v217\nM_ 156,36,200\nds_read_b128 v[200:203],v237 offset:28672\nv_rcp_f32 v218,v218\nv_rcp_f32 v219,v219\n"
"v_mul_f32 v148,v148,v216\ns_waitcnt lgkmcnt(5)\nM_ 152,40,204\nv_mul_f32 v149,v149,v217\nv_mul_f32 v150,v150,v218\nv_mul_f32 v151,v151,v219\nv_cvt_pk_bf16_f32 v220,v148,v149\nM_ 156,44,204\n"
"ds_read_b128 v[204:207],v226 offset:28672\nv_cvt_pk_bf16_f32 v221,v150,v151\nv_fmac_f32 v160,v244,v176\nv_fmac_f32 v161,v244,v177\nv_fmac_f32 v162,v244,v178\nv_fmac_f32 v163,v244,v179\ns_waitcnt lgkmcnt(5)\nM_ 152,48,184\n"
"v_fmac_f32 v164,v244,v180\nv_fmac_f32 v165,v244,v181\nv_fmac_f32 v166,v244,v182\nv_fmac_f32 v167,v244,v183\nM_ 156,52,184\nds_read_b128 v[184:187],v237 offset:30720\nv_mul_f32 v216,v160,v160\nv_mul_f32 v217,v161,v161\n"
"v_mul_f32 v218,v162,v162\nv_mul_f32 v219,v163,v163\nv_fma_f32 v216,v216,v246,v245\ns_waitcnt lgkmcnt(5)\nM_ 152,56,188\nv_fma_f32 v217,v217,v246,v245\nv_fma_f32 v218,v218,v246,v245\nv_fma_f32 v219,v219,v246,v245\n"
"v_mul_f32 v216,v216,v160\nM_ 156,60,188\nds_read_b128 v[188:191],v226 offset:30720\nv_mul_f32 v217,v217,v161\nv_mul_f32 v218,v218,v162\nv_mul_f32 v219,v219,v163\nv_exp_f32 v216,v216\ns_waitcnt lgkmcnt(5)\n"
"M_ 152,64,192\nv_exp_f32 v217,v217\nv_exp_f32 v218,v218\nM_ 156,68,192\nds_read_b128 v[192:195],v237 offset:32768\nv_exp_f32 v219,v219\nv_mul_f32 v164,v160,v164\nv_mul_f32 v165,v161,v165\n"
"v_mul_f32 v166,v162,v166\ns_waitcnt lgkmcnt(5)\nM_ 152,72,196\nv_mul_f32 v167,v163,v167\nv_fma_f32 v216,v216,v247,v247\nv_fma_f32 v217,v217,v247,v247\nv_fma_f32 v218,v218,v247,v247\nM_ 156,76,196\n"
"ds_read_b128 v[196:199],v226 offset:32768\nv_fma_f32 v219,v219,v247,v247\nv_rcp_f32 v216,v216\nv_rcp_f32 v217,v217\ns_waitcnt lgkmcnt(5)\nM_ 152,80,200\ns_waitcnt vmcnt(0)\ns_mul_i32 s7,s88,18432\n"
"s_add_u32 s7,s7,s50\ns_mov_b64 exec,15\nbuffer_store_dwordx4 v[234:237],v224,s[12:15],s7 offen sc1\ns_mov_b64 exec,-1\nv_rcp_f32 v218,v218\nv_rcp_f32 v219,v219\nM_ 156,84,200\nds_read_b128 v[200:203],v237 offset:34816\n"
"v_mul_f32 v164,v164,v216\nv_mul_f32 v165,v165,v217\nv_mul_f32 v166,v166,v218\nv_mul_f32 v167,v167,v219\nv_cvt_pk_bf16_f32 v252,v164,v165\ns_waitcnt lgkmcnt(5)\nM_ 152,88,204\nv_cvt_pk_bf16_f32 v253,v166,v167\n"
"v_fmac_f32 v168,v248,v176\nv_fmac_f32 v169,v248,v177\nv_fmac_f32 v170,v248,v178\nM_ 156,92,204\nds_read_b128 v[204:207],v226 offset:34816\nv_fmac_f32 v171,v248,v179\nv_fmac_f32 v172,v248,v180\nv_fmac_f32 v173,v248,v181\n"
"v_fmac_f32 v174,v248,v182\nv_fmac_f32 v175,v248,v183\ns_waitcnt lgkmcnt(5)\nM_ 152,96,184\nv_mul_f32 v216,v168,v168\nv_mul_f32 v217,v169,v169\nv_mul_f32 v218,v170,v170\nv_mul_f32 v219,v171,v171\n"
"M_ 156,100,184\nv_fma_f32 v216,v216,v250,v249\nv_fma_f32 v217,v217,v250,v249\nv_fma_f32 v218,v218,v250,v249\nv_fma_f32 v219,v219,v250,v249\nv_mul_f32 v216,v216,v168\ns_waitcnt lgkmcnt(4)\nM_ 152,104,188\n"
"v_mul_f32 v217,v217,v169\nv_mul_f32 v218,v218,v170\nv_mul_f32 v219,v219,v171\nM_ 156,108,188\nv_exp_f32 v216,v216\nv_exp_f32 v217,v217\ns_waitcnt lgkmcnt(3)\nM_ 152,112,192\n"
"v_exp_f32 v218,v218\nv_exp_f32 v219,v219\nv_mul_f32 v172,v168,v172\nM_ 156,116,192\nv_mul_f32 v173,v169,v173\nv_mul_f32 v174,v170,v174\nv_mul_f32 v175,v171,v175\nv_fma_f32 v216,v216,v251,v251\n"
"v_fma_f32 v217,v217,v251,v251\ns_waitcnt lgkmcnt(2)\nM_ 152,120,196\nv_fma_f32 v218,v218,v251,v251\nv_fma_f32 v219,v219,v251,v251\nv_rcp_f32 v216,v216\nM_ 156,124,196\nv_rcp_f32 v217,v217\n"
"v_rcp_f32 v218,v218\ns_waitcnt lgkmcnt(1)\nM_ 152,128,200\nv_rcp_f32 v219,v219\nv_mul_f32 v172,v172,v216\nv_mul_f32 v173,v173,v217\nv_mul_f32 v174,v174,v218\nM_ 156,132,200\n"
"v_mul_f32 v175,v175,v219\nv_cvt_pk_bf16_f32 v254,v172,v173\nv_cvt_pk_bf16_f32 v255,v174,v175\ns_nop 0\nv_permlane16_swap_b32 v252,v254\nv_permlane16_swap_b32 v253,v255\ns_mul_i32 s7,s60,294912\nbuffer_store_dwordx4 v[252:255],v230,s[12:15],s7 offen sc1\n"
"s_waitcnt lgkmcnt(0)\nM_ 152,136,204\nM_ 156,140,204\ns_nop 6\nv_fmac_f32 v152,v212,v176\nv_fmac_f32 v153,v212,v177\nv_fmac_f32 v154,v212,v178\nv_fmac_f32 v155,v212,v179\n"
"v_fmac_f32 v156,v212,v180\nv_fmac_f32 v157,v212,v181\nv_fmac_f32 v158,v212,v182\nv_fmac_f32 v159,v212,v183\nv_mul_f32 v216,v152,v152\nv_mul_f32 v217,v153,v153\nv_mul_f32 v218,v154,v154\nv_mul_f32 v219,v155,v155\n"
"v_fma_f32 v216,v216,v214,v213\nv_fma_f32 v217,v217,v214,v213\nv_fma_f32 v218,v218,v214,v213\nv_fma_f32 v219,v219,v214,v213\nv_mul_f32 v216,v216,v152\nv_mul_f32 v217,v217,v153\nv_mul_f32 v218,v218,v154\nv_mul_f32 v219,v219,v155\n"
"v_exp_f32 v216,v216\nv_exp_f32 v217,v217\nv_exp_f32 v218,v218\nv_exp_f32 v219,v219\nv_mul_f32 v156,v152,v156\nv_mul_f32 v157,v153,v157\nv_mul_f32 v158,v154,v158\nv_mul_f32 v159,v155,v159\n"
"v_fma_f32 v216,v216,v215,v215\nv_fma_f32 v217,v217,v215,v215\nv_fma_f32 v218,v218,v215,v215\nv_fma_f32 v219,v219,v215,v215\nv_rcp_f32 v216,v216\nv_rcp_f32 v217,v217\nv_rcp_f32 v218,v218\nv_rcp_f32 v219,v219\n"
"v_mul_f32 v156,v156,v216\nv_mul_f32 v157,v157,v217\nv_mul_f32 v158,v158,v218\nv_mul_f32 v159,v159,v219\nv_cvt_pk_bf16_f32 v222,v156,v157\nv_cvt_pk_bf16_f32 v223,v158,v159\ns_nop 0\nv_permlane16_swap_b32 v220,v222\n"
"v_permlane16_swap_b32 v221,v223\ns_mul_i32 s7,s87,294912\nbuffer_store_dwordx4 v[220:223],v230,s[12:15],s7 offen sc1\ns_waitcnt vmcnt(1)\ns_mul_i32 s7,s60,18432\ns_add_u32 s7,s7,s50\ns_mov_b64 exec,15\nbuffer_store_dwordx4 v[234:237],v224,s[12:15],s7 offen sc1\n"
"s_mov_b64 exec,-1\ns_waitcnt vmcnt(1)\ns_mul_i32 s7,s87,18432\ns_add_u32 s7,s7,s50\ns_mov_b64 exec,15\nbuffer_store_dwordx4 v[234:237],v224,s[12:15],s7 offen sc1\ns_mov_b64 exec,-1\ns_bitcmp1_b32 s70,6\n"
"s_cbranch_scc0 .nojob_%=\ns_mul_i32 s2,s76,18432\ns_add_u32 s69,s69,s2\nv_lshrrev_b32 v242,1,v224\nv_lshrrev_b32 v243,5,v224\nv_lshlrev_b32 v243,2,v243\ns_barrier\ns_nop 7\n"
"s_add_u32 s7,s69,0\nbuffer_load_dwordx4 v[0:3],v224,s[12:15],s7 offen sc1\nbuffer_load_dwordx2 v[4:5],v242,s[12:15],s7 offen offset:1024 sc1\nbuffer_load_dword v6,v243,s[12:15],s7 offen offset:1536 sc1\ns_add_u32 s7,s69,2048\nbuffer_load_dwordx4 v[8:11],v224,s[12:15],s7 offen sc1\nbuffer_load_dwordx2 v[12:13],v242,s[12:15],s7 offen offset:1024 sc1\nbuffer_load_dword v14,v243,s[12:15],s7 offen offset:1536 sc1\n"
"s_add_u32 s7,s69,4096\nbuffer_load_dwordx4 v[16:19],v224,s[12:15],s7 offen sc1\nbuffer_load_dwordx2 v[20:21],v242,s[12:15],s7 offen offset:1024 sc1\nbuffer_load_dword v22,v243,s[12:15],s7 offen offset:1536 sc1\ns_add_u32 s7,s69,6144\nbuffer_load_dwordx4 v[24:27],v224,s[12:15],s7 offen sc1\nbuffer_load_dwordx2 v[28:29],v242,s[12:15],s7 offen offset:1024 sc1\nbuffer_load_dword v30,v243,s[12:15],s7 offen offset:1536 sc1\n"
"s_add_u32 s7,s69,8192\nbuffer_load_dwordx4 v[32:35],v224,s[12:15],s7 offen sc1\nbuffer_load_dwordx2 v[36:37],v242,s[12:15],s7 offen offset:1024 sc1\nbuffer_load_dword v38,v243,s[12:15],s7 offen offset:1536 sc1\ns_add_u32 s7,s69,10240\nbuffer_load_dwordx4 v[40:43],v224,s[12:15],s7 offen sc1\nbuffer_load_dwordx2 v[44:45],v242,s[12:15],s7 offen offset:1024 sc1\nbuffer_load_dword v46,v243,s[12:15],s7 offen offset:1536 sc1\n"
"s_add_u32 s7,s69,12288\nbuffer_load_dwordx4 v[48:51],v224,s[12:15],s7 offen sc1\nbuffer_load_dwordx2 v[52:53],v242,s[12:15],s7 offen offset:1024 sc1\nbuffer_load_dword v54,v243,s[12:15],s7 offen offset:1536 sc1\ns_add_u32 s7,s69,14336\nbuffer_load_dwordx4 v[56:59],v224,s[12:15],s7 offen sc1\nbuffer_load_dwordx2 v[60:61],v242,s[12:15],s7 offen offset:1024 sc1\nbuffer_load_dword v62,v243,s[12:15],s7 offen offset:1536 sc1\n"
"v_lshrrev_b32 v221,8,v224\nv_mul_u32_u24 v220,9216,v221\nv_bfe_u32 v221,v224,4,4\nv_lshl_add_u32 v220,v221,2,v220\nv_mov_b32 v222,0x3f80\nv_mov_b32 v223,0\nv_lshlrev_b64 v[222:223],v224,v[222:223]\nv_mov_b32 v120,0\n"
"v_mov_b32 v121,0\nv_mov_b32 v122,0\nv_mov_b32 v123,0\nv_mov_b32 v124,0\nv_mov_b32 v125,0\nv_mov_b32 v126,0\nv_mov_b32 v127,0\nv_mov_b32 v128,0\n"
"v_mov_b32 v129,0\nv_mov_b32 v130,0\nv_mov_b32 v131,0\ns_mul_i32 s2,s68,73728\ns_bfe_u32 s3,s70,131076\ns_mul_i32 s3,s3,768\ns_add_u32 s2,s2,s3\ns_lshr_b32 s3,s76,2\n"
"s_mul_i32 s3,s3,36864\ns_add_u32 s2,s2,s3\ns_and_b32 s3,s76,3\ns_mul_i32 s3,s3,192\ns_add_u32 s2,s2,s3\ns_add_u32 s3,s2,4608\ns_waitcnt vmcnt(23)\nv_add3_u32 v7,v0,v1,v2\n"
"s_waitcnt vmcnt(22)\nv_add3_u32 v7,v7,v3,v4\nv_add3_u32 v7,v7,v5,s96\ns_waitcnt vmcnt(20)\nv_add3_u32 v15,v8,v9,v10\ns_waitcnt vmcnt(19)\nv_add3_u32 v15,v15,v11,v12\nv_add3_u32 v15,v15,v13,s96\n"
"s_waitcnt vmcnt(17)\nv_add3_u32 v23,v16,v17,v18\ns_waitcnt vmcnt(16)\nv_add3_u32 v23,v23,v19,v20\nv_add3_u32 v23,v23,v21,s96\ns_waitcnt vmcnt(14)\nv_add3_u32 v31,v24,v25,v26\ns_waitcnt vmcnt(13)\n"
"v_add3_u32 v31,v31,v27,v28\nv_add3_u32 v31,v31,v29,s96\ns_waitcnt vmcnt(11)\nv_add3_u32 v39,v32,v33,v34\ns_waitcnt vmcnt(10)\nv_add3_u32 v39,v39,v35,v36\nv_add3_u32 v39,v39,v37,s96\ns_waitcnt vmcnt(8)\n"
"v_add3_u32 v47,v40,v41,v42\ns_waitcnt vmcnt(7)\nv_add3_u32 v47,v47,v43,v44\nv_add3_u32 v47,v47,v45,s96\ns_waitcnt vmcnt(5)\nv_add3_u32 v55,v48,v49,v50\ns_waitcnt vmcnt(4)\nv_add3_u32 v55,v55,v51,v52\n"
"v_add3_u32 v55,v55,v53,s96\ns_waitcnt vmcnt(2)\nv_add3_u32 v63,v56,v57,v58\ns_waitcnt vmcnt(1)\nv_add3_u32 v63,v63,v59,v60\nv_add3_u32 v63,v63,v61,s96\nv_add_u32_dpp v7,v7,v7 quad_perm:[1,0,3,2] row_mask:0xf bank_mask:0xf\nv_add_u32_dpp v15,v15,v15 quad_perm:[1,0,3,2] row_mask:0xf bank_mask:0xf\n"
"v_add_u32_dpp v23,v23,v23 quad_perm:[1,0,3,2] row_mask:0xf bank_mask:0xf\nv_add_u32_dpp v31,v31,v31 quad_perm:[1,0,3,2] row_mask:0xf bank_mask:0xf\nv_add_u32_dpp v39,v39,v39 quad_perm:[1,0,3,2] row_mask:0xf bank_mask:0xf\nv_add_u32_dpp v47,v47,v47 quad_perm:[1,0,3,2] row_mask:0xf bank_mask:0xf\nv_add_u32_dpp v55,v55,v55 quad_perm:[1,0,3,2] row_mask:0xf bank_mask:0xf\nv_add_u32_dpp v63,v63,v63 quad_perm:[1,0,3,2] row_mask:0xf bank_mask:0xf\nv_cmp_ne_u32 s[4:5],v7,v6\nv_cmp_ne_u32 vcc,v15,v14\n"
"s_nop 0\ns_or_b64 s[4:5],s[4:5],vcc\nv_cmp_ne_u32 vcc,v23,v22\ns_nop 0\ns_or_b64 s[4:5],s[4:5],vcc\nv_cmp_ne_u32 vcc,v31,v30\ns_nop 0\ns_or_b64 s[4:5],s[4:5],vcc\n"
"v_cmp_ne_u32 vcc,v39,v38\ns_nop 0\ns_or_b64 s[4:5],s[4:5],vcc\nv_cmp_ne_u32 vcc,v47,v46\ns_nop 0\ns_or_b64 s[4:5],s[4:5],vcc\nv_cmp_ne_u32 vcc,v55,v54\ns_nop 0\n"
"s_or_b64 s[4:5],s[4:5],vcc\ns_waitcnt vmcnt(0)\nv_cmp_ne_u32 vcc,v63,v62\ns_nop 0\ns_or_b64 s[4:5],s[4:5],vcc\ns_cbranch_scc1 .sl55_%=\n.ok54_%=:\ns_add_u32 s7,s69,16384\n"
"buffer_load_dwordx4 v[64:67],v224,s[12:15],s7 offen sc1\nbuffer_load_dwordx2 v[68:69],v242,s[12:15],s7 offen offset:1024 sc1\nbuffer_load_dword v70,v243,s[12:15],s7 offen offset:1536 sc1\nv_mfma_f32_4x4x4_16b_bf16 v[120:123],v[222:223],v[0:1],v[120:123]\nv_mfma_f32_4x4x4_16b_bf16 v[124:127],v[222:223],v[2:3],v[124:127]\nv_mfma_f32_4x4x4_16b_bf16 v[128:131],v[222:223],v[4:5],v[128:131]\nv_mfma_f32_4x4x4_16b_bf16 v[120:123],v[222:223],v[8:9],v[120:123]\nv_mfma_f32_4x4x4_16b_bf16 v[124:127],v[222:223],v[10:11],v[124:127]\n"
"v_mfma_f32_4x4x4_16b_bf16 v[128:131],v[222:223],v[12:13],v[128:131]\nv_mfma_f32_4x4x4_16b_bf16 v[120:123],v[222:223],v[16:17],v[120:123]\nv_mfma_f32_4x4x4_16b_bf16 v[124:127],v[222:223],v[18:19],v[124:127]\nv_mfma_f32_4x4x4_16b_bf16 v[128:131],v[222:223],v[20:21],v[128:131]\nv_mfma_f32_4x4x4_16b_bf16 v[120:123],v[222:223],v[24:25],v[120:123]\nv_mfma_f32_4x4x4_16b_bf16 v[124:127],v[222:223],v[26:27],v[124:127]\nv_mfma_f32_4x4x4_16b_bf16 v[128:131],v[222:223],v[28:29],v[128:131]\nv_mfma_f32_4x4x4_16b_bf16 v[120:123],v[222:223],v[32:33],v[120:123]\n"
"v_mfma_f32_4x4x4_16b_bf16 v[124:127],v[222:223],v[34:35],v[124:127]\nv_mfma_f32_4x4x4_16b_bf16 v[128:131],v[222:223],v[36:37],v[128:131]\nv_mfma_f32_4x4x4_16b_bf16 v[120:123],v[222:223],v[40:41],v[120:123]\nv_mfma_f32_4x4x4_16b_bf16 v[124:127],v[222:223],v[42:43],v[124:127]\nv_mfma_f32_4x4x4_16b_bf16 v[128:131],v[222:223],v[44:45],v[128:131]\nv_mfma_f32_4x4x4_16b_bf16 v[120:123],v[222:223],v[48:49],v[120:123]\nv_mfma_f32_4x4x4_16b_bf16 v[124:127],v[222:223],v[50:51],v[124:127]\nv_mfma_f32_4x4x4_16b_bf16 v[128:131],v[222:223],v[52:53],v[128:131]\n"
"v_mfma_f32_4x4x4_16b_bf16 v[120:123],v[222:223],v[56:57],v[120:123]\nv_mfma_f32_4x4x4_16b_bf16 v[124:127],v[222:223],v[58:59],v[124:127]\nv_mfma_f32_4x4x4_16b_bf16 v[128:131],v[222:223],v[60:61],v[128:131]\ns_waitcnt vmcnt(2)\nv_add3_u32 v71,v64,v65,v66\ns_waitcnt vmcnt(1)\nv_add3_u32 v71,v71,v67,v68\nv_add3_u32 v71,v71,v69,s96\n"
"s_nop 1\nv_add_u32_dpp v71,v71,v71 quad_perm:[1,0,3,2] row_mask:0xf bank_mask:0xf\ns_waitcnt vmcnt(0)\nv_cmp_ne_u32 vcc,v71,v70\ns_nop 0\ns_cmp_lg_u64 vcc,0\ns_cbranch_scc1 .sl66_%=\n.ok65_%=:\n"
"v_mfma_f32_4x4x4_16b_bf16 v[120:123],v[222:223],v[64:65],v[120:123]\nv_mfma_f32_4x4x4_16b_bf16 v[124:127],v[222:223],v[66:67],v[124:127]\nv_mfma_f32_4x4x4_16b_bf16 v[128:131],v[222:223],v[68:69],v[128:131]\ns_nop 2\ns_nop 2\nbuffer_store_dword v120,v220,s[28:31],s2 offen\nbuffer_store_dword v124,v220,s[28:31],s2 offen offset:64\nbuffer_store_dword v128,v220,s[28:31],s2 offen offset:128\n"
"buffer_store_dword v121,v220,s[28:31],s2 offen offset:2304\nbuffer_store_dword v125,v220,s[28:31],s2 offen offset:2368\nbuffer_store_dword v129,v220,s[28:31],s2 offen offset:2432\nbuffer_store_dword v122,v220,s[28:31],s3 offen\nbuffer_store_dword v126,v220,s[28:31],s3 offen offset:64\nbuffer_store_dword v130,v220,s[28:31],s3 offen offset:128\nbuffer_store_dword v123,v220,s[28:31],s3 offen offset:2304\nbuffer_store_dword v127,v220,s[28:31],s3 offen offset:2368\n"
"buffer_store_dword v131,v220,s[28:31],s3 offen offset:2432\n.nojob_%=:\ns_bitcmp1_b32 s70,7\ns_cbranch_scc0 .nobump_%=\ns_lshl_b32 s3,s77,2\ns_add_u32 s3,s3,4096\nv_lshlrev_b32 v230,0,v224\ns_nop 7\n"
"s_add_u32 s7,s3,0\nbuffer_load_dwordx4 v[0:3],v230,s[12:15],s7 offen sc1\ns_add_u32 s7,s3,1024\nbuffer_load_dwordx4 v[4:7],v230,s[12:15],s7 offen sc1\ns_add_u32 s7,s3,2048\nbuffer_load_dwordx4 v[8:11],v230,s[12:15],s7 offen sc1\ns_add_u32 s7,s3,3072\nbuffer_load_dwordx4 v[12:15],v230,s[12:15],s7 offen sc1\n"
"s_waitcnt vmcnt(3)\nv_cmp_eq_u32 s[0:1],s96,v1\nv_cmp_eq_u32 vcc,s97,v3\ns_nop 0\ns_and_b64 s[0:1],s[0:1],vcc\ns_waitcnt vmcnt(2)\nv_cmp_eq_u32 s[4:5],s96,v5\nv_cmp_eq_u32 vcc,s97,v7\n"
"s_nop 0\ns_and_b64 s[4:5],s[4:5],vcc\ns_and_b64 s[0:1],s[0:1],s[4:5]\ns_waitcnt vmcnt(1)\nv_cmp_eq_u32 s[4:5],s96,v9\nv_cmp_eq_u32 vcc,s97,v11\ns_nop 0\ns_and_b64 s[4:5],s[4:5],vcc\n"
"s_and_b64 s[0:1],s[0:1],s[4:5]\ns_waitcnt vmcnt(0)\nv_cmp_eq_u32 s[4:5],s96,v13\nv_cmp_eq_u32 vcc,s97,v15\ns_nop 0\ns_and_b64 s[4:5],s[4:5],vcc\ns_and_b64 s[0:1],s[0:1],s[4:5]\ns_cmp_eq_u64 s[0:1],-1\n"
"s_cbranch_scc0 .sl70_%=\n.ok69_%=:\ns_barrier\ns_mov_b64 exec,1\nbuffer_store_dword v234,v224,s[12:15],0 offen sc1\ns_mov_b64 exec,-1\n.nobump_%=:\ns_endpgm\n"
".sl2_%=:\ns_mov_b32 s6,200000\n.lp3_%=:\ns_sleep 1\ns_nop 3\nbuffer_load_dwordx4 v[128:131],v252,s[12:15],0 offen sc1\ns_waitcnt vmcnt(0)\nv_cmp_eq_u32 s[4:5],s96,v129\n"
"v_cmp_eq_u32 vcc,s97,v131\ns_nop 0\ns_and_b64 s[4:5],s[4:5],vcc\ns_cmp_eq_u64 s[4:5],-1\ns_cbranch_scc1 .ok1_%=\ns_sub_u32 s6,s6,1\ns_cmp_lg_u32 s6,0\ns_cbranch_scc1 .lp3_%=\n"
"s_branch .ok1_%=\n.ra5_%=:\ns_nop 1\ns_mov_b64 s[64:65],s[66:67]\ns_mov_b32 s90,294912\ns_mul_i32 s91,s101,36864\ns_add_u32 s91,s91,1048576\ns_lshl_b32 s4,s56,31\n"
"s_or_b32 s91,s91,s4\ns_xor_b32 s2,s76,0\ns_or_b32 s2,s2,s56\ns_cmp_eq_u32 s2,0\ns_cselect_b64 s[92:93],-1,0\ns_mov_b64 s[72:73],s[12:13]\ns_mov_b32 s74,s14\ns_bfe_u32 s2,s70,262144\n"
"s_add_u32 s3,s76,s2\ns_sub_u32 s7,s3,9\ns_cselect_b32 s3,s3,s7\ns_lshl_b32 s3,s3,11\nv_add_u32 v227,s3,v224\nv_mov_b32 v228,v227\ns_add_u32 s3,8,s2\ns_sub_u32 s7,s3,9\n"
"s_cselect_b32 s3,s3,s7\ns_lshl_b32 s3,s3,11\ns_add_u32 s3,s3,s79\ns_lshl_b32 s2,s76,11\ns_add_u32 s3,s3,s2\ns_sub_u32 s3,s3,16384\nv_add_u32 v229,s3,v224\ns_nop 1\n"
"s_branch .rb6_%=\n.so7_%=:\ns_mul_i32 s7,s85,18432\nbuffer_load_dwordx2 v[232:233],v236,s[12:15],s7 offen sc1\ns_nop 0\ns_branch .sb8_%=\n.ra9_%=:\ns_nop 1\n"
"s_mov_b32 s55,0\ns_add_u32 s51,s51,1\ns_cmp_eq_u32 s51,9\ns_cselect_b32 s3,0xfffaf000,0\ns_add_u32 s94,s94,36864\ns_add_u32 s94,s94,s3\ns_nop 1\ns_branch .rb10_%=\n"
".sl14_%=:\ns_mov_b32 s6,200000\n.lp15_%=:\ns_sleep 1\ns_nop 3\ns_mul_i32 s7,s85,18432\nbuffer_load_dwordx2 v[232:233],v236,s[12:15],s7 offen sc1\ns_waitcnt vmcnt(0)\n"
"v_cmp_ne_u64 vcc,v[232:233],v[234:235]\ns_nop 0\ns_and_b64 s[4:5],vcc,s[92:93]\ns_cbranch_scc0 .ok13_%=\ns_sub_u32 s6,s6,1\ns_cmp_lg_u32 s6,0\ns_cbranch_scc1 .lp15_%=\ns_branch .ok13_%=\n"
".so11_%=:\ns_waitcnt vmcnt(3)\nv_cmp_ne_u64 vcc,v[232:233],v[234:235]\ns_nop 0\ns_and_b64 s[4:5],vcc,s[92:93]\ns_cbranch_scc1 .sl14_%=\n.ok13_%=:\ns_nop 0\n"
"s_branch .sb12_%=\n.sl17_%=:\ns_mov_b32 s6,200000\n.lp18_%=:\ns_sleep 1\ns_nop 3\ns_mul_i32 s7,s85,18432\nbuffer_load_dwordx2 v[232:233],v236,s[12:15],s7 offen sc1\n"
"s_waitcnt vmcnt(0)\nv_cmp_ne_u64 vcc,v[232:233],v[234:235]\ns_nop 0\ns_and_b64 s[4:5],vcc,s[92:93]\ns_cbranch_scc0 .ok16_%=\ns_sub_u32 s6,s6,1\ns_cmp_lg_u32 s6,0\ns_cbranch_scc1 .lp18_%=\n"
"s_branch .ok16_%=\n.sl20_%=:\ns_mov_b32 s6,200000\n.lp21_%=:\ns_sleep 1\ns_nop 3\ns_mul_i32 s7,s85,18432\nbuffer_load_dwordx2 v[232:233],v236,s[12:15],s7 offen sc1\n"
"s_waitcnt vmcnt(0)\nv_cmp_ne_u64 vcc,v[232:233],v[234:235]\ns_nop 0\ns_and_b64 s[4:5],vcc,s[92:93]\ns_cbranch_scc0 .ok19_%=\ns_sub_u32 s6,s6,1\ns_cmp_lg_u32 s6,0\ns_cbranch_scc1 .lp21_%=\n"
"s_branch .ok19_%=\n.sl23_%=:\ns_mov_b32 s6,200000\n.lp24_%=:\ns_sleep 1\ns_nop 3\ns_mul_i32 s7,s85,18432\nbuffer_load_dwordx2 v[232:233],v236,s[12:15],s7 offen sc1\n"
"s_waitcnt vmcnt(0)\nv_cmp_ne_u64 vcc,v[232:233],v[234:235]\ns_nop 0\ns_and_b64 s[4:5],vcc,s[92:93]\ns_cbranch_scc0 .ok22_%=\ns_sub_u32 s6,s6,1\ns_cmp_lg_u32 s6,0\ns_cbranch_scc1 .lp24_%=\n"
"s_branch .ok22_%=\n.so25_%=:\ns_mul_i32 s7,s85,18432\nbuffer_load_dwordx2 v[232:233],v236,s[12:15],s7 offen sc1\ns_nop 0\ns_branch .sb26_%=\n.sl31_%=:\ns_mov_b32 s6,200000\n"
".lp32_%=:\ns_sleep 1\ns_nop 3\ns_mul_i32 s7,s85,18432\nbuffer_load_dwordx2 v[232:233],v236,s[12:15],s7 offen sc1\ns_waitcnt vmcnt(0)\nv_cmp_ne_u64 vcc,v[232:233],v[234:235]\ns_nop 0\n"
"s_and_b64 s[4:5],vcc,s[92:93]\ns_cbranch_scc0 .ok30_%=\ns_sub_u32 s6,s6,1\ns_cmp_lg_u32 s6,0\ns_cbranch_scc1 .lp32_%=\ns_branch .ok30_%=\n.so28_%=:\ns_waitcnt vmcnt(3)\n"
"v_cmp_ne_u64 vcc,v[232:233],v[234:235]\ns_nop 0\ns_and_b64 s[4:5],vcc,s[92:93]\ns_cbranch_scc1 .sl31_%=\n.ok30_%=:\ns_nop 0\ns_branch .sb29_%=\n.ts35_%=:\n"
"s_cmp_eq_u32 s6,0\ns_cbranch_scc1 .to36_%=\ns_sub_u32 s6,s6,1\ns_call_b64 s[0:1],.trt_%=\ns_branch .tc34_%=\n.ts38_%=:\ns_cmp_eq_u32 s6,0\ns_cbranch_scc1 .to39_%=\n"
"s_sub_u32 s6,s6,1\ns_call_b64 s[0:1],.trt_%=\ns_branch .tc37_%=\n.ts41_%=:\ns_cmp_eq_u32 s6,0\ns_cbranch_scc1 .to42_%=\ns_sub_u32 s6,s6,1\ns_call_b64 s[0:1],.trt_%=\n"
"s_branch .tc40_%=\n.ts44_%=:\ns_cmp_eq_u32 s6,0\ns_cbranch_scc1 .to45_%=\ns_sub_u32 s6,s6,1\ns_call_b64 s[0:1],.trt_%=\ns_branch .tc43_%=\n.ts47_%=:\n"
"s_cmp_eq_u32 s6,0\ns_cbranch_scc1 .to48_%=\ns_sub_u32 s6,s6,1\ns_call_b64 s[0:1],.trt_%=\ns_branch .tc46_%=\n.tl2_%=:\ns_nop 3\ns_bfe_u32 s16,s68,262144\n"
"s_cmp_eq_u32 s16,15\ns_cselect_b32 s23,0,s96\ns_cselect_b32 s7,0x80000000,s71\ns_mul_i32 s16,s16,2048\ns_add_u32 s16,s16,s7\nbuffer_load_dwordx4 v[244:247],v224,s[12:15],s16 offen sc1\nbuffer_load_dwordx2 v[248:249],v242,s[12:15],s16 offen offset:1024 sc1\nbuffer_load_dword v250,v243,s[12:15],s16 offen offset:1536 sc1\n"
"s_bfe_u32 s17,s68,262148\ns_cmp_eq_u32 s17,15\ns_cselect_b32 s24,0,s96\ns_cselect_b32 s7,0x80000000,s71\ns_mul_i32 s17,s17,2048\ns_add_u32 s17,s17,s7\nbuffer_load_dwordx4 v[252:255],v224,s[12:15],s17 offen sc1\nbuffer_load_dwordx2 v[156:157],v242,s[12:15],s17 offen offset:1024 sc1\n"
"buffer_load_dword v158,v243,s[12:15],s17 offen offset:1536 sc1\ns_bfe_u32 s18,s68,262152\ns_cmp_eq_u32 s18,15\ns_cselect_b32 s25,0,s96\ns_cselect_b32 s7,0x80000000,s71\ns_mul_i32 s18,s18,2048\ns_add_u32 s18,s18,s7\nbuffer_load_dwordx4 v[208:211],v224,s[12:15],s18 offen sc1\n"
"buffer_load_dwordx2 v[212:213],v242,s[12:15],s18 offen offset:1024 sc1\nbuffer_load_dword v214,v243,s[12:15],s18 offen offset:1536 sc1\ns_bfe_u32 s19,s68,262156\ns_cmp_eq_u32 s19,15\ns_cselect_b32 s26,0,s96\ns_cselect_b32 s7,0x80000000,s71\ns_mul_i32 s19,s19,2048\ns_add_u32 s19,s19,s7\n"
"buffer_load_dwordx4 v[176:179],v224,s[12:15],s19 offen sc1\nbuffer_load_dwordx2 v[180:181],v242,s[12:15],s19 offen offset:1024 sc1\nbuffer_load_dword v182,v243,s[12:15],s19 offen offset:1536 sc1\ns_bfe_u32 s20,s68,262160\ns_cmp_eq_u32 s20,15\ns_cselect_b32 s27,0,s96\ns_cselect_b32 s7,0x80000000,s71\ns_mul_i32 s20,s20,2048\n"
"s_add_u32 s20,s20,s7\nbuffer_load_dwordx4 v[168:171],v224,s[12:15],s20 offen sc1\nbuffer_load_dwordx2 v[172:173],v242,s[12:15],s20 offen offset:1024 sc1\nbuffer_load_dword v174,v243,s[12:15],s20 offen offset:1536 sc1\ns_waitcnt vmcnt(0)\ns_branch .tl_%=\n.trt_%=:\ns_sleep 2\n"
"s_waitcnt vmcnt(0)\ns_nop 3\nv_add3_u32 v251,v244,v245,v246\nv_add3_u32 v251,v251,v247,v248\nv_add3_u32 v251,v251,v249,s23\ns_nop 1\nv_add_u32_dpp v251,v251,v251 quad_perm:[1,0,3,2] row_mask:0xf bank_mask:0xf\nv_cmp_ne_u32 vcc,v251,v250\n"
"s_nop 0\ns_cmp_lg_u64 vcc,0\ns_cbranch_scc0 .tr49_%=\nbuffer_load_dwordx4 v[244:247],v224,s[12:15],s16 offen sc1\nbuffer_load_dwordx2 v[248:249],v242,s[12:15],s16 offen offset:1024 sc1\nbuffer_load_dword v250,v243,s[12:15],s16 offen offset:1536 sc1\n.tr49_%=:\nv_add3_u32 v159,v252,v253,v254\n"
"v_add3_u32 v159,v159,v255,v156\nv_add3_u32 v159,v159,v157,s24\ns_nop 1\nv_add_u32_dpp v159,v159,v159 quad_perm:[1,0,3,2] row_mask:0xf bank_mask:0xf\nv_cmp_ne_u32 vcc,v159,v158\ns_nop 0\ns_cmp_lg_u64 vcc,0\ns_cbranch_scc0 .tr50_%=\n"
"buffer_load_dwordx4 v[252:255],v224,s[12:15],s17 offen sc1\nbuffer_load_dwordx2 v[156:157],v242,s[12:15],s17 offen offset:1024 sc1\nbuffer_load_dword v158,v243,s[12:15],s17 offen offset:1536 sc1\n.tr50_%=:\nv_add3_u32 v215,v208,v209,v210\nv_add3_u32 v215,v215,v211,v212\nv_add3_u32 v215,v215,v213,s25\ns_nop 1\n"
"v_add_u32_dpp v215,v215,v215 quad_perm:[1,0,3,2] row_mask:0xf bank_mask:0xf\nv_cmp_ne_u32 vcc,v215,v214\ns_nop 0\ns_cmp_lg_u64 vcc,0\ns_cbranch_scc0 .tr51_%=\nbuffer_load_dwordx4 v[208:211],v224,s[12:15],s18 offen sc1\nbuffer_load_dwordx2 v[212:213],v242,s[12:15],s18 offen offset:1024 sc1\nbuffer_load_dword v214,v243,s[12:15],s18 offen offset:1536 sc1\n"
".tr51_%=:\nv_add3_u32 v183,v176,v177,v178\nv_add3_u32 v183,v183,v179,v180\nv_add3_u32 v183,v183,v181,s26\ns_nop 1\nv_add_u32_dpp v183,v183,v183 quad_perm:[1,0,3,2] row_mask:0xf bank_mask:0xf\nv_cmp_ne_u32 vcc,v183,v182\ns_nop 0\n"
"s_cmp_lg_u64 vcc,0\ns_cbranch_scc0 .tr52_%=\nbuffer_load_dwordx4 v[176:179],v224,s[12:15],s19 offen sc1\nbuffer_load_dwordx2 v[180:181],v242,s[12:15],s19 offen offset:1024 sc1\nbuffer_load_dword v182,v243,s[12:15],s19 offen offset:1536 sc1\n.tr52_%=:\nv_add3_u32 v175,v168,v169,v170\nv_add3_u32 v175,v175,v171,v172\n"
"v_add3_u32 v175,v175,v173,s27\ns_nop 1\nv_add_u32_dpp v175,v175,v175 quad_perm:[1,0,3,2] row_mask:0xf bank_mask:0xf\nv_cmp_ne_u32 vcc,v175,v174\ns_nop 0\ns_cmp_lg_u64 vcc,0\ns_cbranch_scc0 .tr53_%=\nbuffer_load_dwordx4 v[168:171],v224,s[12:15],s20 offen sc1\n"
"buffer_load_dwordx2 v[172:173],v242,s[12:15],s20 offen offset:1024 sc1\nbuffer_load_dword v174,v243,s[12:15],s20 offen offset:1536 sc1\n.tr53_%=:\ns_waitcnt vmcnt(0)\ns_setpc_b64 s[0:1]\n.sl55_%=:\ns_mov_b32 s6,200000\n.lp56_%=:\n"
"s_sleep 8\ns_nop 3\nv_add3_u32 v7,v0,v1,v2\nv_add3_u32 v7,v7,v3,v4\nv_add3_u32 v7,v7,v5,s96\ns_nop 1\nv_add_u32_dpp v7,v7,v7 quad_perm:[1,0,3,2] row_mask:0xf bank_mask:0xf\nv_cmp_ne_u32 vcc,v7,v6\n"
"s_nop 0\ns_cmp_lg_u64 vcc,0\ns_cbranch_scc0 .tr57_%=\ns_add_u32 s7,s69,0\nbuffer_load_dwordx4 v[0:3],v224,s[12:15],s7 offen sc1\nbuffer_load_dwordx2 v[4:5],v242,s[12:15],s7 offen offset:1024 sc1\nbuffer_load_dword v6,v243,s[12:15],s7 offen offset:1536 sc1\n.tr57_%=:\n"
"v_add3_u32 v15,v8,v9,v10\nv_add3_u32 v15,v15,v11,v12\nv_add3_u32 v15,v15,v13,s96\ns_nop 1\nv_add_u32_dpp v15,v15,v15 quad_perm:[1,0,3,2] row_mask:0xf bank_mask:0xf\nv_cmp_ne_u32 vcc,v15,v14\ns_nop 0\ns_cmp_lg_u64 vcc,0\n"
"s_cbranch_scc0 .tr58_%=\ns_add_u32 s7,s69,2048\nbuffer_load_dwordx4 v[8:11],v224,s[12:15],s7 offen sc1\nbuffer_load_dwordx2 v[12:13],v242,s[12:15],s7 offen offset:1024 sc1\nbuffer_load_dword v14,v243,s[12:15],s7 offen offset:1536 sc1\n.tr58_%=:\nv_add3_u32 v23,v16,v17,v18\nv_add3_u32 v23,v23,v19,v20\n"
"v_add3_u32 v23,v23,v21,s96\ns_nop 1\nv_add_u32_dpp v23,v23,v23 quad_perm:[1,0,3,2] row_mask:0xf bank_mask:0xf\nv_cmp_ne_u32 vcc,v23,v22\ns_nop 0\ns_cmp_lg_u64 vcc,0\ns_cbranch_scc0 .tr59_%=\ns_add_u32 s7,s69,4096\n"
"buffer_load_dwordx4 v[16:19],v224,s[12:15],s7 offen sc1\nbuffer_load_dwordx2 v[20:21],v242,s[12:15],s7 offen offset:1024 sc1\nbuffer_load_dword v22,v243,s[12:15],s7 offen offset:1536 sc1\n.tr59_%=:\nv_add3_u32 v31,v24,v25,v26\nv_add3_u32 v31,v31,v27,v28\nv_add3_u32 v31,v31,v29,s96\ns_nop 1\n"
"v_add_u32_dpp v31,v31,v31 quad_perm:[1,0,3,2] row_mask:0xf bank_mask:0xf\nv_cmp_ne_u32 vcc,v31,v30\ns_nop 0\ns_cmp_lg_u64 vcc,0\ns_cbranch_scc0 .tr60_%=\ns_add_u32 s7,s69,6144\nbuffer_load_dwordx4 v[24:27],v224,s[12:15],s7 offen sc1\nbuffer_load_dwordx2 v[28:29],v242,s[12:15],s7 offen offset:1024 sc1\n"
"buffer_load_dword v30,v243,s[12:15],s7 offen offset:1536 sc1\n.tr60_%=:\nv_add3_u32 v39,v32,v33,v34\nv_add3_u32 v39,v39,v35,v36\nv_add3_u32 v39,v39,v37,s96\ns_nop 1\nv_add_u32_dpp v39,v39,v39 quad_perm:[1,0,3,2] row_mask:0xf bank_mask:0xf\nv_cmp_ne_u32 vcc,v39,v38\n"
"s_nop 0\ns_cmp_lg_u64 vcc,0\ns_cbranch_scc0 .tr61_%=\ns_add_u32 s7,s69,8192\nbuffer_load_dwordx4 v[32:35],v224,s[12:15],s7 offen sc1\nbuffer_load_dwordx2 v[36:37],v242,s[12:15],s7 offen offset:1024 sc1\nbuffer_load_dword v38,v243,s[12:15],s7 offen offset:1536 sc1\n.tr61_%=:\n"
"v_add3_u32 v47,v40,v41,v42\nv_add3_u32 v47,v47,v43,v44\nv_add3_u32 v47,v47,v45,s96\ns_nop 1\nv_add_u32_dpp v47,v47,v47 quad_perm:[1,0,3,2] row_mask:0xf bank_mask:0xf\nv_cmp_ne_u32 vcc,v47,v46\ns_nop 0\ns_cmp_lg_u64 vcc,0\n"
"s_cbranch_scc0 .tr62_%=\ns_add_u32 s7,s69,10240\nbuffer_load_dwordx4 v[40:43],v224,s[12:15],s7 offen sc1\nbuffer_load_dwordx2 v[44:45],v242,s[12:15],s7 offen offset:1024 sc1\nbuffer_load_dword v46,v243,s[12:15],s7 offen offset:1536 sc1\n.tr62_%=:\nv_add3_u32 v55,v48,v49,v50\nv_add3_u32 v55,v55,v51,v52\n"
"v_add3_u32 v55,v55,v53,s96\ns_nop 1\nv_add_u32_dpp v55,v55,v55 quad_perm:[1,0,3,2] row_mask:0xf bank_mask:0xf\nv_cmp_ne_u32 vcc,v55,v54\ns_nop 0\ns_cmp_lg_u64 vcc,0\ns_cbranch_scc0 .tr63_%=\ns_add_u32 s7,s69,12288\n"
"buffer_load_dwordx4 v[48:51],v224,s[12:15],s7 offen sc1\nbuffer_load_dwordx2 v[52:53],v242,s[12:15],s7 offen offset:1024 sc1\nbuffer_load_dword v54,v243,s[12:15],s7 offen offset:1536 sc1\n.tr63_%=:\nv_add3_u32 v63,v56,v57,v58\nv_add3_u32 v63,v63,v59,v60\nv_add3_u32 v63,v63,v61,s96\ns_nop 1\n"
"v_add_u32_dpp v63,v63,v63 quad_perm:[1,0,3,2] row_mask:0xf bank_mask:0xf\nv_cmp_ne_u32 vcc,v63,v62\ns_nop 0\ns_cmp_lg_u64 vcc,0\ns_cbranch_scc0 .tr64_%=\ns_add_u32 s7,s69,14336\nbuffer_load_dwordx4 v[56:59],v224,s[12:15],s7 offen sc1\nbuffer_load_dwordx2 v[60:61],v242,s[12:15],s7 offen offset:1024 sc1\n"
"buffer_load_dword v62,v243,s[12:15],s7 offen offset:1536 sc1\n.tr64_%=:\ns_waitcnt vmcnt(0)\nv_add3_u32 v7,v0,v1,v2\nv_add3_u32 v7,v7,v3,v4\nv_add3_u32 v7,v7,v5,s96\nv_add3_u32 v15,v8,v9,v10\nv_add3_u32 v15,v15,v11,v12\n"
"v_add3_u32 v15,v15,v13,s96\nv_add3_u32 v23,v16,v17,v18\nv_add3_u32 v23,v23,v19,v20\nv_add3_u32 v23,v23,v21,s96\nv_add3_u32 v31,v24,v25,v26\nv_add3_u32 v31,v31,v27,v28\nv_add3_u32 v31,v31,v29,s96\nv_add3_u32 v39,v32,v33,v34\n"
"v_add3_u32 v39,v39,v35,v36\nv_add3_u32 v39,v39,v37,s96\nv_add3_u32 v47,v40,v41,v42\nv_add3_u32 v47,v47,v43,v44\nv_add3_u32 v47,v47,v45,s96\nv_add3_u32 v55,v48,v49,v50\nv_add3_u32 v55,v55,v51,v52\nv_add3_u32 v55,v55,v53,s96\n"
"v_add3_u32 v63,v56,v57,v58\nv_add3_u32 v63,v63,v59,v60\nv_add3_u32 v63,v63,v61,s96\nv_add_u32_dpp v7,v7,v7 quad_perm:[1,0,3,2] row_mask:0xf bank_mask:0xf\nv_add_u32_dpp v15,v15,v15 quad_perm:[1,0,3,2] row_mask:0xf bank_mask:0xf\nv_add_u32_dpp v23,v23,v23 quad_perm:[1,0,3,2] row_mask:0xf bank_mask:0xf\nv_add_u32_dpp v31,v31,v31 quad_perm:[1,0,3,2] row_mask:0xf bank_mask:0xf\nv_add_u32_dpp v39,v39,v39 quad_perm:[1,0,3,2] row_mask:0xf bank_mask:0xf\n"
"v_add_u32_dpp v47,v47,v47 quad_perm:[1,0,3,2] row_mask:0xf bank_mask:0xf\nv_add_u32_dpp v55,v55,v55 quad_perm:[1,0,3,2] row_mask:0xf bank_mask:0xf\nv_add_u32_dpp v63,v63,v63 quad_perm:[1,0,3,2] row_mask:0xf bank_mask:0xf\nv_cmp_ne_u32 s[4:5],v7,v6\nv_cmp_ne_u32 vcc,v15,v14\ns_nop 0\ns_or_b64 s[4:5],s[4:5],vcc\nv_cmp_ne_u32 vcc,v23,v22\n"
"s_nop 0\ns_or_b64 s[4:5],s[4:5],vcc\nv_cmp_ne_u32 vcc,v31,v30\ns_nop 0\ns_or_b64 s[4:5],s[4:5],vcc\nv_cmp_ne_u32 vcc,v39,v38\ns_nop 0\ns_or_b64 s[4:5],s[4:5],vcc\n"
"v_cmp_ne_u32 vcc,v47,v46\ns_nop 0\ns_or_b64 s[4:5],s[4:5],vcc\nv_cmp_ne_u32 vcc,v55,v54\ns_nop 0\ns_or_b64 s[4:5],s[4:5],vcc\nv_cmp_ne_u32 vcc,v63,v62\ns_nop 0\n"
"s_or_b64 s[4:5],s[4:5],vcc\ns_cbranch_scc0 .ok54_%=\ns_sub_u32 s6,s6,1\ns_cmp_lg_u32 s6,0\ns_cbranch_scc1 .lp56_%=\ns_branch .ok54_%=\n.sl66_%=:\ns_mov_b32 s6,200000\n"
".lp67_%=:\ns_sleep 8\ns_nop 3\nv_add3_u32 v71,v64,v65,v66\nv_add3_u32 v71,v71,v67,v68\nv_add3_u32 v71,v71,v69,s96\ns_nop 1\nv_add_u32_dpp v71,v71,v71 quad_perm:[1,0,3,2] row_mask:0xf bank_mask:0xf\n"
"v_cmp_ne_u32 vcc,v71,v70\ns_nop 0\ns_cmp_lg_u64 vcc,0\ns_cbranch_scc0 .tr68_%=\ns_add_u32 s7,s69,16384\nbuffer_load_dwordx4 v[64:67],v224,s[12:15],s7 offen sc1\nbuffer_load_dwordx2 v[68:69],v242,s[12:15],s7 offen offset:1024 sc1\nbuffer_load_dword v70,v243,s[12:15],s7 offen offset:1536 sc1\n"
".tr68_%=:\ns_waitcnt vmcnt(0)\nv_add3_u32 v71,v64,v65,v66\nv_add3_u32 v71,v71,v67,v68\nv_add3_u32 v71,v71,v69,s96\ns_nop 1\nv_add_u32_dpp v71,v71,v71 quad_perm:[1,0,3,2] row_mask:0xf bank_mask:0xf\nv_cmp_ne_u32 vcc,v71,v70\n"
"s_nop 0\ns_cmp_lg_u64 vcc,0\ns_cbranch_scc0 .ok65_%=\ns_sub_u32 s6,s6,1\ns_cmp_lg_u32 s6,0\ns_cbranch_scc1 .lp67_%=\ns_branch .ok65_%=\n.sl70_%=:\n"
"s_mov_b32 s6,200000\n.lp71_%=:\ns_sleep 1\ns_nop 3\ns_add_u32 s7,s3,0\nbuffer_load_dwordx4 v[0:3],v230,s[12:15],s7 offen sc1\ns_add_u32 s7,s3,1024\nbuffer_load_dwordx4 v[4:7],v230,s[12:15],s7 offen sc1\n"
"s_add_u32 s7,s3,2048\nbuffer_load_dwordx4 v[8:11],v230,s[12:15],s7 offen sc1\ns_add_u32 s7,s3,3072\nbuffer_load_dwordx4 v[12:15],v230,s[12:15],s7 offen sc1\ns_waitcnt vmcnt(0)\nv_cmp_eq_u32 s[0:1],s96,v1\nv_cmp_eq_u32 vcc,s97,v3\ns_nop 0\n"
"s_and_b64 s[0:1],s[0:1],vcc\nv_cmp_eq_u32 s[4:5],s96,v5\nv_cmp_eq_u32 vcc,s97,v7\ns_nop 0\ns_and_b64 s[4:5],s[4:5],vcc\ns_and_b64 s[0:1],s[0:1],s[4:5]\nv_cmp_eq_u32 s[4:5],s96,v9\nv_cmp_eq_u32 vcc,s97,v11\n"
"s_nop 0\ns_and_b64 s[4:5],s[4:5],vcc\ns_and_b64 s[0:1],s[0:1],s[4:5]\nv_cmp_eq_u32 s[4:5],s96,v13\nv_cmp_eq_u32 vcc,s97,v15\ns_nop 0\ns_and_b64 s[4:5],s[4:5],vcc\ns_and_b64 s[0:1],s[0:1],s[4:5]\n"
"s_cmp_eq_u64 s[0:1],-1\ns_cbranch_scc1 .ok69_%=\ns_sub_u32 s6,s6,1\ns_cmp_lg_u32 s6,0\ns_cbranch_scc1 .lp71_%=\ns_branch .ok69_%=\n.p2align 6\n.ssm_%=:\n"
"v_lshrrev_b32 v177,3,v224\ns_lshl_b32 s7,s95,3\ns_add_u32 s7,s7,s76\ns_mul_i32 s7,s7,1152\ns_waitcnt lgkmcnt(0)\ns_lshl_b32 s6,s95,6\ns_load_dwordx16 s[56:71],s[72:73],s6\ns_lshl_b32 s6,s95,3\n"
"s_add_u32 s6,s6,16384\ns_load_dwordx2 s[100:101],s[72:73],s6\ns_load_dwordx16 s[24:39],s[98:99],64\ns_load_dwordx16 s[40:55],s[98:99],128\nbuffer_load_dwordx4 v[168:171],v224,s[8:11],s7 offen\nbuffer_load_ushort v172,v177,s[8:11],s7 offen offset:1024\nbuffer_load_dword v176,off,s[12:15],0 sc1\ns_waitcnt lgkmcnt(0)\n"
"s_and_b32 s0,s76,3\ns_lshr_b32 s1,s76,2\ns_mul_i32 s4,s57,384\nv_and_b32 v33,4,v244\nv_lshlrev_b32 v33,2,v33\nv_and_b32 v31,1,v244\nv_lshl_add_u32 v33,v31,3,v33\nv_bfe_u32 v31,v244,1,1\n"
"v_add_u32 v33,v33,v31\nv_lshrrev_b32 v31,1,v244\nv_xor_b32 v31,v31,v245\nv_mul_u32_u24 v137,1152,v33\nv_lshl_add_u32 v137,v31,4,v137\ns_and_b32 s2,s76,1\ns_lshl_b32 s3,s2,6\nv_xor_b32 v137,s3,v137\n"
"s_mul_i32 s2,s2,31\ns_add_u32 s2,s2,s76\ns_lshl_b32 s3,s60,7\ns_add_u32 s2,s2,s3\ns_mul_i32 s69,s2,1152\nv_and_b32 v28,15,v0\nv_lshrrev_b32 v29,4,v0\ns_lshl_b32 s2,s0,5\n"
"s_add_u32 s2,s2,s4\nv_lshl_add_u32 v30,v28,1,s2\ns_mul_i32 s29,s57,221184\ns_mul_i32 s2,s76,9216\ns_add_u32 s29,s29,s2\ns_mul_i32 s28,s60,147456\ns_add_u32 s28,s28,s2\ns_add_u32 s28,s28,s4\n"
"s_lshl_b32 s2,s58,7\ns_add_u32 s69,s69,s2\ns_add_u32 s29,s29,s2\ns_add_u32 m0,s77,0\ns_add_u32 s7,s69,0\nbuffer_load_dwordx4 v137,s[8:11],s7 offen lds\ns_add_u32 m0,s77,8192\ns_add_u32 s7,s69,73728\n"
"buffer_load_dwordx4 v137,s[8:11],s7 offen lds\ns_add_u32 m0,s77,16384\ns_add_u32 s7,s29,0\nbuffer_load_dwordx4 v241,s[32:35],s7 offen lds\ns_add_u32 m0,s77,24576\ns_add_u32 s7,s29,0\nbuffer_load_dwordx4 v241,s[36:39],s7 offen lds\nv_lshlrev_b32 v34,3,v224\n"
"s_lshl_b32 s5,s59,3\ns_add_u32 s5,s5,s76\ns_mul_i32 s31,s5,4608\ns_mul_i32 s5,s57,221184\ns_add_u32 s31,s31,s5\ns_mov_b32 s62,0xffff0000\ns_mov_b32 s63,0x0\ns_mov_b32 s64,0x0\n"
"s_mov_b32 s65,0xffff\ns_mov_b32 s66,0x0\ns_mov_b32 s67,0xffff0000\ns_mov_b64 s[72:73],s[54:55]\ns_mov_b32 s74,1152\ns_mov_b32 s75,s11\ns_mul_i32 s2,s60,1769472\ns_mul_i32 s3,s76,18432\n"
"s_add_u32 s2,s2,s3\ns_mul_i32 s3,s57,147456\ns_add_u32 s2,s2,s3\ns_add_u32 s19,s2,19939328\ns_lshr_b32 s5,s76,1\ns_mul_i32 s6,s5,55296\ns_and_b32 s5,s76,1\ns_mul_i32 s5,s5,9216\n"
"s_add_u32 s6,s6,s5\ns_mul_i32 s5,s57,221184\ns_add_u32 s6,s6,s5\ns_mul_i32 s2,s60,294912\ns_add_u32 s2,s2,48381952\ns_add_u32 s3,s57,0\ns_sub_u32 s5,s3,3\ns_cselect_b32 s3,s3,s5\n"
"s_mul_i32 s5,s3,98304\ns_add_u32 s30,s5,s2\ns_add_u32 s2,s2,s77\ns_mul_i32 s5,s3,384\ns_add_u32 s16,s5,s6\ns_add_u32 s3,s57,1\ns_sub_u32 s5,s3,3\ns_cselect_b32 s3,s3,s5\n"
"s_mul_i32 s5,s3,98304\ns_add_u32 s20,s5,s2\ns_mul_i32 s5,s3,384\ns_add_u32 s17,s5,s6\ns_add_u32 s3,s57,2\ns_sub_u32 s5,s3,3\ns_cselect_b32 s3,s3,s5\ns_mul_i32 s5,s3,98304\n"
"s_add_u32 s21,s5,s2\ns_mul_i32 s5,s3,384\ns_add_u32 s18,s5,s6\nv_lshrrev_b32 v32,1,v28\nv_xor_b32 v32,v32,v29\nv_lshlrev_b32 v32,4,v32\nv_lshl_add_u32 v32,v28,7,v32\ns_lshl_b32 s2,s1,13\n"
"v_add_u32 v226,s2,v32\nv_xor_b32 v227,64,v226\ns_lshl_b32 s2,s0,11\nv_add_u32 v228,s2,v32\nv_xor_b32 v229,64,v228\nv_add_u32 v230,65536,v228\nv_add_u32 v231,65536,v229\nv_add_u32 v232,65536,v230\n"
"v_add_u32 v233,65536,v231\nv_add_u32 v124,65536,v226\nv_add_u32 v125,65536,v227\nv_add_u32 v142,131072,v226\nv_add_u32 v143,131072,v227\ns_lshl_b32 s2,s60,11\ns_lshl_b32 s3,s1,10\ns_add_u32 s2,s2,s3\n"
"s_add_u32 s2,s2,4096\nv_lshrrev_b32 v31,1,v28\nv_lshl_add_u32 v31,v29,4,v31\nv_and_b32 v32,1,v28\nv_lshl_add_u32 v31,v32,3,v31\nv_lshl_add_u32 v234,v31,4,s2\ns_lshl_b32 s2,s60,1\ns_add_u32 s2,s2,s1\n"
"s_lshl_b32 s3,s57,16\ns_lshl_b32 s5,s0,9\ns_add_u32 s3,s3,s5\ns_add_u32 s3,s3,186624\ns_lshl_b32 s5,s2,11\ns_add_u32 s5,s5,s3\ns_sub_u32 s2,s2,32\nv_lshl_add_u32 v235,v28,4,s5\n"
"v_lshl_add_u32 v31,v29,3,s2\nv_lshlrev_b32 v239,11,v31\nv_lshl_add_u32 v239,v28,4,v239\nv_add_u32 v239,s3,v239\nv_cmp_le_i32 s[78:79],0,v31\nv_cmp_le_i32 s[80:81],-1,v31\nv_cmp_le_i32 s[82:83],-2,v31\nv_cmp_le_i32 s[84:85],-3,v31\n"
"v_cmp_le_i32 s[86:87],-4,v31\nv_cmp_le_i32 s[88:89],-5,v31\nv_cmp_le_i32 s[90:91],-6,v31\nv_cmp_le_i32 s[92:93],-7,v31\ns_and_b32 s5,s0,2\ns_add_u32 s5,s5,s1\ns_lshl_b32 s5,s5,1\ns_and_b32 s6,s0,1\n"
"s_add_u32 s5,s5,s6\ns_lshl_b32 s5,s5,11\nv_add_u32 v238,s5,v224\nv_lshrrev_b32 v31,1,v29\nv_lshlrev_b32 v31,11,v31\nv_and_b32 v32,1,v29\nv_lshl_add_u32 v31,v32,7,v31\nv_lshrrev_b32 v32,2,v28\n"
"v_lshl_add_u32 v31,v32,4,v31\nv_and_b32 v32,1,v28\nv_lshl_add_u32 v31,v32,3,v31\nv_bfe_u32 v32,v28,1,1\nv_mul_u32_u24 v32,1088,v32\ns_lshl_b32 s5,s1,8\nv_add3_u32 v236,v31,v32,s5\nv_xor_b32 v237,64,v236\n"
"s_waitcnt vmcnt(4)\nv_readfirstlane_b32 s96,v176\nv_lshlrev_b32 v192,16,v172\nv_lshlrev_b32 v184,16,v168\nv_readfirstlane_b32 s97,v192\nv_and_b32 v185,0xffff0000,v168\nv_lshlrev_b32 v186,16,v169\nv_and_b32 v187,0xffff0000,v169\n"
"v_lshlrev_b32 v188,16,v170\nv_and_b32 v189,0xffff0000,v170\nv_lshlrev_b32 v190,16,v171\nv_and_b32 v191,0xffff0000,v171\nv_subrev_f32 v184,s97,v184\nv_subrev_f32 v185,s97,v185\nv_subrev_f32 v186,s97,v186\nv_subrev_f32 v187,s97,v187\n"
"v_subrev_f32 v188,s97,v188\nv_subrev_f32 v189,s97,v189\nv_subrev_f32 v190,s97,v190\nv_subrev_f32 v191,s97,v191\nv_subrev_f32 v192,s97,v192\ns_add_u32 m0,s77,32768\ns_add_u32 s7,s29,73728\nbuffer_load_dwordx4 v241,s[32:35],s7 offen lds\n"
"v_add_f32 v180,v184,v185\nv_mul_f32 v182,v184,v184\nv_add_f32 v181,v186,v187\nv_mul_f32 v183,v185,v185\nv_fmac_f32 v182,v186,v186\nv_fmac_f32 v183,v187,v187\nv_add_f32 v180,v180,v188\nv_fmac_f32 v182,v188,v188\n"
"v_add_f32 v181,v181,v189\nv_fmac_f32 v183,v189,v189\nv_add_f32 v180,v180,v190\nv_fmac_f32 v182,v190,v190\nv_add_f32 v181,v181,v191\nv_fmac_f32 v183,v191,v191\nv_add_f32 v180,v180,v192\nv_fmac_f32 v182,v192,v192\n"
"v_add_f32 v180,v180,v181\nv_add_f32 v182,v182,v183\nv_mov_b32 v169,s96\nv_add_f32_dpp v180,v180,v180 quad_perm:[1,0,3,2] row_mask:0xf bank_mask:0xf\ns_add_u32 m0,s77,40960\ns_add_u32 s7,s29,73728\nbuffer_load_dwordx4 v241,s[36:39],s7 offen lds\nv_add_f32_dpp v182,v182,v182 quad_perm:[1,0,3,2] row_mask:0xf bank_mask:0xf\n"
"v_add_u32 v169,1,v169\nv_add_f32_dpp v180,v180,v180 quad_perm:[2,3,0,1] row_mask:0xf bank_mask:0xf\nv_add_f32_dpp v182,v182,v182 quad_perm:[2,3,0,1] row_mask:0xf bank_mask:0xf\nv_add_u32 v171,0x10000,v169\nv_add_f32_dpp v180,v180,v180 row_half_mirror row_mask:0xf bank_mask:0xf\nv_add_f32_dpp v182,v182,v182 row_half_mirror row_mask:0xf bank_mask:0xf\ns_nop 0\nv_add_f32_dpp v180,v180,v180 row_mirror row_mask:0xf bank_mask:0xf\n"
"v_add_f32_dpp v182,v182,v182 row_mirror row_mask:0xf bank_mask:0xf\ns_nop 0\nv_add_f32_dpp v180,v180,v180 row_bcast:15 row_mask:0xa bank_mask:0xf\nv_add_f32_dpp v182,v182,v182 row_bcast:15 row_mask:0xa bank_mask:0xf\ns_nop 0\nv_add_f32_dpp v180,v180,v180 row_bcast:31 row_mask:0xc bank_mask:0xf\nv_add_f32_dpp v182,v182,v182 row_bcast:31 row_mask:0xc bank_mask:0xf\nv_mul_f32 v180,0x3ae38e39,v180\n"
"v_mul_f32 v182,0x3ae38e39,v182\nv_fma_f32 v182,-v180,v180,v182\nv_add_f32 v180,s97,v180\nv_max_f32 v182,0,v182\ns_add_u32 s97,s96,65537\ns_add_u32 s96,s96,1\nv_cvt_pk_bf16_f32 v168,v180,v180\ns_add_u32 m0,s77,49152\n"
"s_add_u32 s7,s29,147456\nbuffer_load_dwordx4 v241,s[32:35],s7 offen lds\nv_lshlrev_b32 v168,16,v168\nv_cvt_pk_bf16_f32 v182,v182,v182\nv_lshlrev_b32 v182,16,v182\nv_add_f32 v182,0x3727c5ac,v182\nv_cvt_pk_bf16_f32 v182,v182,v182\nv_lshlrev_b32 v182,16,v182\n"
"v_sqrt_f32 v182,v182\ns_nop 0\nv_cvt_pk_bf16_f32 v182,v182,v182\nv_lshlrev_b32 v182,16,v182\nv_rcp_f32 v170,v182\ns_lshl_b32 s7,s95,3\ns_add_u32 s7,s7,s76\ns_lshl_b32 s7,s7,4\n"
"s_add_u32 s7,s7,3088\ns_bfm_b64 exec,1,63\nbuffer_store_dwordx4 v[168:171],v224,s[12:15],s7 offen sc1\ns_mov_b64 exec,-1\ns_add_u32 m0,s77,57344\ns_add_u32 s7,s29,147456\nbuffer_load_dwordx4 v241,s[36:39],s7 offen lds\ns_add_u32 s58,s58,1\n"
"s_cmp_eq_u32 s58,9\ns_cselect_b32 s7,-8,1\ns_lshl_b32 s7,s7,7\ns_add_u32 s69,s69,s7\ns_add_u32 s29,s29,s7\ns_add_u32 m0,s77,65536\ns_add_u32 s7,s69,0\nbuffer_load_dwordx4 v137,s[8:11],s7 offen lds\n"
"s_add_u32 m0,s77,73728\ns_add_u32 s7,s69,73728\nbuffer_load_dwordx4 v137,s[8:11],s7 offen lds\ns_add_u32 m0,s77,81920\ns_add_u32 s7,s29,0\nbuffer_load_dwordx4 v241,s[32:35],s7 offen lds\ns_add_u32 m0,s77,90112\ns_add_u32 s7,s29,0\n"
"buffer_load_dwordx4 v241,s[36:39],s7 offen lds\nglobal_load_ushort v252,v30,s[48:49]\nglobal_load_ushort v253,v30,s[50:51]\nglobal_load_ushort v254,v30,s[52:53]\nglobal_load_ushort v246,v30,s[48:49] offset:128\nglobal_load_ushort v247,v30,s[50:51] offset:128\nglobal_load_ushort v248,v30,s[52:53] offset:128\nglobal_load_ushort v249,v30,s[48:49] offset:256\n"
"global_load_ushort v250,v30,s[50:51] offset:256\nglobal_load_ushort v251,v30,s[52:53] offset:256\ns_waitcnt vmcnt(13)\ns_barrier\nds_read_b128 v[200:203],v228 offset:16384\nds_read_b128 v[168:171],v226\nds_read_b128 v[172:175],v226 offset:2048\nds_read_b128 v[176:179],v226 offset:4096\n"
"ds_read_b128 v[180:183],v226 offset:6144\nds_read_b128 v[204:207],v228 offset:24576\nds_read_b128 v[184:187],v227\nds_read_b128 v[188:191],v227 offset:2048\nds_read_b128 v[192:195],v227 offset:4096\nds_read_b128 v[196:199],v227 offset:6144\nds_read_b128 v[208:211],v229 offset:16384\nds_read_b128 v[212:215],v229 offset:24576\n"
"ds_read_b128 v[216:219],v228 offset:32768\nds_read_b128 v[220:223],v228 offset:40960\ns_waitcnt lgkmcnt(12)\nZ_ 0,168,200\ns_add_u32 m0,s77,98304\ns_add_u32 s7,s29,73728\nbuffer_load_dwordx4 v241,s[32:35],s7 offen lds\ns_waitcnt lgkmcnt(11)\n"
"Z_ 4,172,200\ns_waitcnt lgkmcnt(10)\nZ_ 8,176,200\ns_waitcnt lgkmcnt(9)\nZ_ 12,180,200\nds_read_b128 v[200:203],v229 offset:32768\ns_waitcnt lgkmcnt(9)\nZ_ 16,168,204\n"
"v_lshrrev_b32 v120,8,v224\nv_mul_u32_u24 v121,896,v120\nv_add_u32 v121,v121,v224\nv_and_b32 v122,255,v224\nv_lshrrev_b32 v122,1,v122\nv_sub_u32 v122,v121,v122\nv_lshlrev_b32 v120,3,v120\ns_mul_i32 s2,s57,192\n"
"s_mul_i32 s3,s60,12\ns_add_u32 s2,s2,s3\ns_lshr_b32 s3,s76,1\ns_lshl_b32 s3,s3,2\ns_add_u32 s2,s2,s3\ns_mul_i32 s22,s2,1152\ns_lshl_b32 s23,s2,3\ns_bitcmp1_b32 s76,0\n"
"s_cselect_b64 s[24:25],s[36:37],s[32:33]\ns_cselect_b32 s3,4608,0\ns_add_u32 s23,s23,s3\ns_add_u32 s23,s23,383232\ns_mov_b64 s[26:27],s[34:35]\ns_cmp_ge_u32 s76,6\ns_cselect_b32 s22,0x80000000,s22\ns_cselect_b32 s23,0x80000000,s23\n"
"buffer_load_dwordx4 v[144:147],v121,s[24:27],s22 offen\nZ_ 20,172,204\ns_add_u32 m0,s77,106496\ns_add_u32 s7,s29,73728\nbuffer_load_dwordx4 v241,s[36:39],s7 offen lds\nZ_ 24,176,204\nZ_ 28,180,204\nbuffer_load_dwordx4 v[148:151],v121,s[24:27],s22 offen offset:256\n"
"ds_read_b128 v[204:207],v229 offset:40960\ns_waitcnt lgkmcnt(5)\nM_ 0,184,208\nM_ 4,188,208\nM_ 8,192,208\nbuffer_load_dwordx4 v[152:155],v121,s[24:27],s22 offen offset:512\ns_add_u32 m0,s77,114688\ns_add_u32 s7,s29,147456\n"
"buffer_load_dwordx4 v241,s[32:35],s7 offen lds\nM_ 12,196,208\nds_read_b128 v[208:211],v228 offset:49152\ns_waitcnt lgkmcnt(5)\nM_ 16,184,212\nM_ 20,188,212\nbuffer_load_dwordx4 v[156:159],v121,s[24:27],s22 offen offset:768\nM_ 24,192,212\n"
"M_ 28,196,212\nds_read_b128 v[212:215],v228 offset:57344\ns_add_u32 m0,s77,122880\ns_add_u32 s7,s29,147456\nbuffer_load_dwordx4 v241,s[36:39],s7 offen lds\ns_add_u32 s58,s58,1\ns_cmp_eq_u32 s58,9\ns_cselect_b32 s7,-8,1\n"
"s_lshl_b32 s7,s7,7\ns_add_u32 s69,s69,s7\ns_add_u32 s29,s29,s7\ns_waitcnt lgkmcnt(5)\nZ_ 32,168,216\nbuffer_load_dwordx2 v[160:161],v122,s[24:27],s22 offen offset:1024\nZ_ 36,172,216\nZ_ 40,176,216\n"
"Z_ 44,180,216\nds_read_b128 v[216:219],v229 offset:49152\ns_waitcnt lgkmcnt(5)\nZ_ 48,168,220\ns_add_u32 m0,s77,131072\ns_add_u32 s7,s69,0\nbuffer_load_dwordx4 v137,s[8:11],s7 offen lds\nZ_ 52,172,220\n"
"Z_ 56,176,220\nZ_ 60,180,220\nds_read_b128 v[220:223],v229 offset:57344\ns_waitcnt lgkmcnt(5)\nM_ 32,184,200\nM_ 36,188,200\ns_add_u32 m0,s77,139264\ns_add_u32 s7,s69,73728\n"
"buffer_load_dwordx4 v137,s[8:11],s7 offen lds\nM_ 40,192,200\nM_ 44,196,200\ns_waitcnt lgkmcnt(4)\nM_ 48,184,204\nM_ 52,188,204\nM_ 56,192,204\ns_add_u32 m0,s77,147456\n"
"s_add_u32 s7,s29,0\nbuffer_load_dwordx4 v241,s[32:35],s7 offen lds\nM_ 60,196,204\ns_waitcnt lgkmcnt(3)\nZ_ 64,168,208\ns_waitcnt vmcnt(4) lgkmcnt(0)\ns_barrier\nZ_ 68,172,208\n"
"ds_read_b128 v[200:203],v230 offset:16384\nZ_ 72,176,208\nds_read_b128 v[204:207],v230 offset:24576\nZ_ 76,180,208\nds_read_b128 v[208:211],v231 offset:16384\ns_add_u32 m0,s77,155648\ns_add_u32 s7,s29,0\nbuffer_load_dwordx4 v241,s[36:39],s7 offen lds\n"
"Z_ 80,168,212\nds_read_b128 v[168:171],v124\nZ_ 84,172,212\nds_read_b128 v[172:175],v124 offset:2048\nZ_ 88,176,212\nds_read_b128 v[176:179],v124 offset:4096\nZ_ 92,180,212\nds_read_b128 v[212:215],v231 offset:24576\n"
"ds_read_b128 v[180:183],v124 offset:6144\nM_ 64,184,216\ns_add_u32 m0,s77,0\ns_add_u32 s7,s29,73728\nbuffer_load_dwordx4 v241,s[32:35],s7 offen lds\nM_ 68,188,216\nM_ 72,192,216\nM_ 76,196,216\n"
"ds_read_b128 v[216:219],v230 offset:32768\nM_ 80,184,220\nds_read_b128 v[184:187],v125\nv_lshlrev_b32 v162,16,v144\nv_and_b32 v163,0xffff0000,v144\nM_ 84,188,220\nds_read_b128 v[188:191],v125 offset:2048\ns_add_u32 m0,s77,8192\n"
"s_add_u32 s7,s29,73728\nbuffer_load_dwordx4 v241,s[36:39],s7 offen lds\nv_lshlrev_b32 v166,16,v145\nv_and_b32 v167,0xffff0000,v145\nM_ 88,192,220\nds_read_b128 v[192:195],v125 offset:4096\nv_add_f32 v162,v162,v166\nv_add_f32 v163,v163,v167\n"
"M_ 92,196,220\nds_read_b128 v[220:223],v230 offset:40960\nds_read_b128 v[196:199],v125 offset:6144\nv_lshlrev_b32 v164,16,v146\nv_and_b32 v165,0xffff0000,v146\ns_waitcnt lgkmcnt(10)\nM_ 0,168,200\nv_add_f32 v162,v162,v164\n"
"s_waitcnt lgkmcnt(9)\nM_ 4,172,200\nv_add_f32 v163,v163,v165\nv_lshlrev_b32 v166,16,v147\ns_waitcnt lgkmcnt(8)\nM_ 8,176,200\ns_add_u32 m0,s77,16384\ns_add_u32 s7,s29,147456\n"
"buffer_load_dwordx4 v241,s[32:35],s7 offen lds\nv_and_b32 v167,0xffff0000,v147\nv_add_f32 v162,v162,v166\ns_waitcnt lgkmcnt(6)\nM_ 12,180,200\nds_read_b128 v[200:203],v231 offset:32768\nv_add_f32 v163,v163,v167\nv_lshlrev_b32 v164,16,v148\n"
"M_ 16,168,204\nv_and_b32 v165,0xffff0000,v148\nM_ 20,172,204\nv_add_f32 v162,v162,v164\nv_add_f32 v163,v163,v165\nM_ 24,176,204\nv_lshlrev_b32 v166,16,v149\nv_and_b32 v167,0xffff0000,v149\n"
"M_ 28,180,204\nds_read_b128 v[204:207],v231 offset:40960\ns_add_u32 m0,s77,24576\ns_add_u32 s7,s29,147456\nbuffer_load_dwordx4 v241,s[36:39],s7 offen lds\ns_add_u32 s58,s58,1\ns_cmp_eq_u32 s58,9\ns_cselect_b32 s7,-8,1\n"
"s_lshl_b32 s7,s7,7\ns_add_u32 s69,s69,s7\ns_add_u32 s29,s29,s7\nv_add_f32 v162,v162,v166\nv_add_f32 v163,v163,v167\ns_waitcnt lgkmcnt(6)\nM_ 0,184,208\nv_lshlrev_b32 v164,16,v150\n"
"s_waitcnt lgkmcnt(5)\nM_ 4,188,208\nv_and_b32 v165,0xffff0000,v150\nv_add_f32 v162,v162,v164\ns_waitcnt lgkmcnt(4)\nM_ 8,192,208\nv_add_f32 v163,v163,v165\nv_lshlrev_b32 v166,16,v151\n"
"s_waitcnt lgkmcnt(2)\nM_ 12,196,208\nds_read_b128 v[208:211],v230 offset:49152\nv_and_b32 v167,0xffff0000,v151\nv_add_f32 v162,v162,v166\nM_ 16,184,212\ns_add_u32 m0,s77,32768\ns_add_u32 s7,s69,0\n"
"buffer_load_dwordx4 v137,s[8:11],s7 offen lds\nv_add_f32 v163,v163,v167\nM_ 20,188,212\nv_lshlrev_b32 v164,16,v152\nv_and_b32 v165,0xffff0000,v152\nM_ 24,192,212\nv_add_f32 v162,v162,v164\nv_add_f32 v163,v163,v165\n"
"M_ 28,196,212\nds_read_b128 v[212:215],v230 offset:57344\nv_lshlrev_b32 v166,16,v153\nv_and_b32 v167,0xffff0000,v153\nM_ 32,168,216\nv_add_f32 v162,v162,v166\nM_ 36,172,216\ns_add_u32 m0,s77,40960\n"
"s_add_u32 s7,s69,73728\nbuffer_load_dwordx4 v137,s[8:11],s7 offen lds\nv_add_f32 v163,v163,v167\nv_lshlrev_b32 v164,16,v154\nM_ 40,176,216\nv_and_b32 v165,0xffff0000,v154\nv_add_f32 v162,v162,v164\nM_ 44,180,216\n"
"ds_read_b128 v[216:219],v231 offset:49152\nv_add_f32 v163,v163,v165\nv_lshlrev_b32 v166,16,v155\nM_ 48,168,220\nv_and_b32 v167,0xffff0000,v155\nM_ 52,172,220\nv_add_f32 v162,v162,v166\nv_add_f32 v163,v163,v167\n"
"M_ 56,176,220\ns_add_u32 m0,s77,49152\ns_add_u32 s7,s29,0\nbuffer_load_dwordx4 v241,s[32:35],s7 offen lds\nv_lshlrev_b32 v164,16,v156\nv_and_b32 v165,0xffff0000,v156\nM_ 60,180,220\nds_read_b128 v[220:223],v231 offset:57344\n"
"v_add_f32 v162,v162,v164\nv_add_f32 v163,v163,v165\ns_waitcnt lgkmcnt(5)\nM_ 32,184,200\nv_lshlrev_b32 v166,16,v157\nM_ 36,188,200\nv_and_b32 v167,0xffff0000,v157\nv_add_f32 v162,v162,v166\n"
"M_ 40,192,200\nv_add_f32 v163,v163,v167\nv_lshlrev_b32 v164,16,v158\nM_ 44,196,200\ns_add_u32 m0,s77,57344\ns_add_u32 s7,s29,0\nbuffer_load_dwordx4 v241,s[36:39],s7 offen lds\nv_and_b32 v165,0xffff0000,v158\n"
"v_add_f32 v162,v162,v164\ns_waitcnt lgkmcnt(4)\nM_ 48,184,204\nv_add_f32 v163,v163,v165\nM_ 52,188,204\nv_lshlrev_b32 v166,16,v159\nv_and_b32 v167,0xffff0000,v159\nM_ 56,192,204\n"
"v_add_f32 v162,v162,v166\nv_add_f32 v163,v163,v167\nM_ 60,196,204\ns_waitcnt vmcnt(12)\nv_lshlrev_b32 v164,16,v160\nv_and_b32 v165,0xffff0000,v160\ns_waitcnt lgkmcnt(3)\nM_ 64,168,208\n"
"s_waitcnt vmcnt(4) lgkmcnt(0)\ns_barrier\nv_add_f32 v162,v162,v164\nM_ 68,172,208\nds_read_b128 v[200:203],v232 offset:16384\ns_add_u32 m0,s77,65536\ns_add_u32 s7,s29,73728\nbuffer_load_dwordx4 v241,s[32:35],s7 offen lds\n"
"v_add_f32 v163,v163,v165\nv_lshlrev_b32 v166,16,v161\nM_ 72,176,208\nds_read_b128 v[204:207],v232 offset:24576\nv_and_b32 v167,0xffff0000,v161\nv_add_f32 v162,v162,v166\nM_ 76,180,208\nds_read_b128 v[208:211],v233 offset:16384\n"
"v_add_f32 v163,v163,v167\nv_add_f32 v162,v162,v163\nM_ 80,168,212\nds_read_b128 v[168:171],v142\nv_mov_b32 v163,s96\nM_ 84,172,212\nds_read_b128 v[172:175],v142 offset:2048\nv_add_f32_dpp v162,v162,v162 quad_perm:[1,0,3,2] row_mask:0xf bank_mask:0xf\n"
"M_ 88,176,212\nds_read_b128 v[176:179],v142 offset:4096\ns_add_u32 m0,s77,73728\ns_add_u32 s7,s29,73728\nbuffer_load_dwordx4 v241,s[36:39],s7 offen lds\nv_add_f32_dpp v162,v162,v162 quad_perm:[2,3,0,1] row_mask:0xf bank_mask:0xf\nM_ 92,180,212\nds_read_b128 v[212:215],v233 offset:24576\n"
"ds_read_b128 v[180:183],v142 offset:6144\nv_add_f32_dpp v162,v162,v162 row_half_mirror row_mask:0xf bank_mask:0xf\nM_ 64,184,216\nM_ 68,188,216\nv_add_f32_dpp v162,v162,v162 row_mirror row_mask:0xf bank_mask:0xf\ns_mov_b32 exec_lo,0x10001\ns_mov_b32 exec_hi,0x10001\nbuffer_store_dwordx2 v[162:163],v120,s[12:15],s23 offen sc1\n"
"s_mov_b64 exec,-1\nv_bfe_u32 v121,v224,4,4\nM_ 72,192,216\nv_lshlrev_b32 v121,3,v121\ns_mul_i32 s22,s57,1536\ns_lshl_b32 s3,s0,7\ns_add_u32 s22,s22,s3\ns_add_u32 s22,s22,383232\n"
"s_add_u32 s23,s22,4608\nv_lshrrev_b32 v136,8,v224\nM_ 76,196,216\nds_read_b128 v[216:219],v228\ns_add_u32 m0,s77,81920\ns_add_u32 s7,s29,147456\nbuffer_load_dwordx4 v241,s[32:35],s7 offen lds\nv_sub_u32 v135,3,v136\n"
"v_lshlrev_b32 v135,9,v135\nM_ 80,184,220\nds_read_b128 v[184:187],v143\nv_lshlrev_b32 v136,4,v136\nM_ 84,188,220\nds_read_b128 v[188:191],v143 offset:2048\nv_cvt_f32_u32 v135,v135\nv_cvt_f32_u32 v136,v136\n"
"M_ 88,192,220\nds_read_b128 v[192:195],v143 offset:4096\nv_lshlrev_b32 v128,16,v252\nv_lshlrev_b32 v129,16,v253\nM_ 92,196,220\nds_read_b128 v[220:223],v228 offset:8192\nds_read_b128 v[196:199],v143 offset:6144\nv_lshlrev_b32 v130,16,v254\n"
"v_mul_f32 v130,0x3fb8aa3b,v130\ns_waitcnt lgkmcnt(10)\nM_ 0,168,200\ns_add_u32 m0,s77,90112\ns_add_u32 s7,s29,147456\nbuffer_load_dwordx4 v241,s[36:39],s7 offen lds\ns_add_u32 s58,s58,1\ns_cmp_eq_u32 s58,9\n"
"s_cselect_b32 s7,-8,1\ns_lshl_b32 s7,s7,7\ns_add_u32 s69,s69,s7\ns_add_u32 s29,s29,s7\ns_waitcnt lgkmcnt(9)\nM_ 4,172,200\nv_exp_f32 v130,v130\ns_waitcnt lgkmcnt(8)\n"
"M_ 8,176,200\nv_cvt_pk_bf16_f32 v130,v130,v130\nv_lshlrev_b32 v130,16,v130\ns_waitcnt lgkmcnt(6)\nM_ 12,180,200\nds_read_b128 v[200:203],v229\nv_mul_f32 v131,v130,v128\nv_mul_f32 v132,v130,v129\n"
"M_ 16,168,204\nv_mul_f32 v131,0x3fb8aa3b,v131\nv_mul_f32 v132,0x3e22f983,v132\nM_ 20,172,204\ns_add_u32 m0,s77,98304\ns_add_u32 s7,s69,0\nbuffer_load_dwordx4 v137,s[8:11],s7 offen lds\nv_mul_f32 v138,1.0,v131\n"
"M_ 24,176,204\nv_mul_f32 v139,1.0,v132\nM_ 28,180,204\nds_read_b128 v[204:207],v229 offset:8192\nv_exp_f32 v138,v138\ns_waitcnt lgkmcnt(6)\nM_ 0,184,208\nv_fract_f32 v139,v139\n"
"s_waitcnt lgkmcnt(5)\nM_ 4,188,208\nv_cos_f32 v96,v139\ns_waitcnt lgkmcnt(4)\nM_ 8,192,208\ns_add_u32 m0,s77,106496\ns_add_u32 s7,s69,73728\nbuffer_load_dwordx4 v137,s[8:11],s7 offen lds\n"
"v_sin_f32 v97,v139\ns_waitcnt lgkmcnt(2)\nM_ 12,196,208\nds_read_b128 v[208:211],v228 offset:16384\nv_min_f32 v138,0x7e967699,v138\nv_mul_f32 v96,v96,v138\nM_ 16,184,212\nv_mul_f32 v97,v97,v138\n"
"v_mul_f32 v140,0x41000000,v131\nM_ 20,188,212\nv_mul_f32 v141,0x41000000,v132\nM_ 24,192,212\nv_exp_f32 v140,v140\nM_ 28,196,212\nds_read_b128 v[212:215],v228 offset:24576\ns_add_u32 m0,s77,114688\n"
"s_add_u32 s7,s29,0\nbuffer_load_dwordx4 v241,s[32:35],s7 offen lds\nv_fract_f32 v141,v141\nM_ 32,168,216\nv_cos_f32 v98,v141\nM_ 36,172,216\nv_sin_f32 v99,v141\nM_ 40,176,216\n"
"v_min_f32 v140,0x7e967699,v140\nv_mul_f32 v98,v98,v140\nM_ 44,180,216\nds_read_b128 v[216:219],v229 offset:16384\nv_mul_f32 v99,v99,v140\nv_mul_f32 v138,v135,v131\nM_ 48,168,220\ns_add_u32 m0,s77,122880\n"
"s_add_u32 s7,s29,0\nbuffer_load_dwordx4 v241,s[36:39],s7 offen lds\nv_mul_f32 v139,v135,v132\nM_ 52,172,220\nv_exp_f32 v138,v138\nM_ 56,176,220\nv_fract_f32 v139,v139\nM_ 60,180,220\n"
"ds_read_b128 v[220:223],v229 offset:24576\nv_cos_f32 v100,v139\ns_waitcnt lgkmcnt(5)\nM_ 32,184,200\nv_sin_f32 v101,v139\nM_ 36,188,200\nv_min_f32 v138,0x7e967699,v138\nv_mul_f32 v100,v100,v138\n"
"M_ 40,192,200\nv_mul_f32 v101,v101,v138\nv_mul_f32 v140,v136,v131\nM_ 44,196,200\nv_mul_f32 v141,v136,v132\ns_waitcnt lgkmcnt(4)\nM_ 48,184,204\nv_exp_f32 v140,v140\n"
"M_ 52,188,204\nv_fract_f32 v141,v141\nM_ 56,192,204\nv_cos_f32 v102,v141\nM_ 60,196,204\nv_sin_f32 v103,v141\ns_waitcnt lgkmcnt(3)\nM_ 64,168,208\n"
"s_waitcnt vmcnt(4) lgkmcnt(0)\ns_barrier\nv_min_f32 v140,0x7e967699,v140\nv_mul_f32 v102,v102,v140\nM_ 68,172,208\nds_read_b128 v[200:203],v228 offset:49152\ns_add_u32 m0,s77,131072\ns_add_u32 s7,s29,73728\n"
"buffer_load_dwordx4 v241,s[32:35],s7 offen lds\nv_mul_f32 v103,v103,v140\nv_add_f32 v133,-1.0,v96\nM_ 72,176,208\nds_read_b128 v[204:207],v228 offset:57344\nv_mul_f32 v134,v128,v128\nM_ 76,180,208\nds_read_b128 v[208:211],v229 offset:49152\n"
"v_fmac_f32 v134,v129,v129\nv_mul_f32 v104,v133,v128\nM_ 80,168,212\nds_read_b128 v[168:171],v226 offset:32768\nv_rcp_f32 v134,v134\nM_ 84,172,212\nds_read_b128 v[172:175],v226 offset:34816\nv_mul_f32 v105,v97,v128\n"
"v_fmac_f32 v104,v97,v129\nM_ 88,176,212\nds_read_b128 v[176:179],v226 offset:36864\ns_add_u32 m0,s77,139264\ns_add_u32 s7,s29,73728\nbuffer_load_dwordx4 v241,s[36:39],s7 offen lds\nv_fma_f32 v105,-v133,v129,v105\nM_ 92,180,212\n"
"ds_read_b128 v[212:215],v229 offset:57344\nds_read_b128 v[180:183],v226 offset:38912\nv_add_f32 v134,v134,v134\nv_mul_f32 v104,v104,v134\nM_ 64,184,216\nv_mul_f32 v105,v105,v134\nv_lshlrev_b32 v128,16,v246\nM_ 68,188,216\n"
"v_lshlrev_b32 v129,16,v247\nv_lshlrev_b32 v130,16,v248\nM_ 72,192,216\nv_mul_f32 v130,0x3fb8aa3b,v130\nM_ 76,196,216\nds_read_b128 v[216:219],v230\ns_add_u32 m0,s77,147456\ns_add_u32 s7,s29,147456\n"
"buffer_load_dwordx4 v241,s[32:35],s7 offen lds\nv_exp_f32 v130,v130\nM_ 80,184,220\nds_read_b128 v[184:187],v227 offset:32768\nv_cvt_pk_bf16_f32 v130,v130,v130\nv_lshlrev_b32 v130,16,v130\nM_ 84,188,220\nds_read_b128 v[188:191],v227 offset:34816\n"
"v_mul_f32 v131,v130,v128\nv_mul_f32 v132,v130,v129\nM_ 88,192,220\nds_read_b128 v[192:195],v227 offset:36864\nv_mul_f32 v131,0x3fb8aa3b,v131\nM_ 92,196,220\nds_read_b128 v[220:223],v230 offset:8192\nds_read_b128 v[196:199],v227 offset:38912\n"
"v_mul_f32 v132,0x3e22f983,v132\nv_mul_f32 v138,1.0,v131\ns_waitcnt lgkmcnt(10)\nM_ 0,168,200\ns_add_u32 m0,s77,155648\ns_add_u32 s7,s29,147456\nbuffer_load_dwordx4 v241,s[36:39],s7 offen lds\ns_add_u32 s58,s58,1\n"
"s_cmp_eq_u32 s58,9\ns_cselect_b32 s7,-8,1\ns_lshl_b32 s7,s7,7\ns_add_u32 s69,s69,s7\ns_add_u32 s29,s29,s7\nv_mul_f32 v139,1.0,v132\ns_waitcnt lgkmcnt(9)\nM_ 4,172,200\n"
"v_exp_f32 v138,v138\ns_waitcnt lgkmcnt(8)\nM_ 8,176,200\nv_fract_f32 v139,v139\ns_waitcnt lgkmcnt(6)\nM_ 12,180,200\nds_read_b128 v[200:203],v231\nv_cos_f32 v106,v139\n"
"M_ 16,168,204\nv_sin_f32 v107,v139\nM_ 20,172,204\ns_add_u32 m0,s77,0\ns_add_u32 s7,s69,0\nbuffer_load_dwordx4 v137,s[8:11],s7 offen lds\nv_min_f32 v138,0x7e967699,v138\nv_mul_f32 v106,v106,v138\n"
"M_ 24,176,204\nv_mul_f32 v107,v107,v138\nv_mul_f32 v140,0x41000000,v131\nM_ 28,180,204\nds_read_b128 v[204:207],v231 offset:8192\nv_mul_f32 v141,0x41000000,v132\ns_waitcnt lgkmcnt(6)\nM_ 0,184,208\n"
"v_exp_f32 v140,v140\ns_waitcnt lgkmcnt(5)\nM_ 4,188,208\nv_fract_f32 v141,v141\ns_waitcnt lgkmcnt(4)\nM_ 8,192,208\ns_add_u32 m0,s77,8192\ns_add_u32 s7,s69,73728\n"
"buffer_load_dwordx4 v137,s[8:11],s7 offen lds\nv_cos_f32 v108,v141\ns_waitcnt lgkmcnt(2)\nM_ 12,196,208\nds_read_b128 v[208:211],v230 offset:16384\nv_sin_f32 v109,v141\nM_ 16,184,212\nv_min_f32 v140,0x7e967699,v140\n"
"v_mul_f32 v108,v108,v140\nM_ 20,188,212\nv_mul_f32 v109,v109,v140\nv_mul_f32 v138,v135,v131\nM_ 24,192,212\nv_mul_f32 v139,v135,v132\nM_ 28,196,212\nds_read_b128 v[212:215],v230 offset:24576\n"
"s_add_u32 m0,s77,16384\ns_add_u32 s7,s29,0\nbuffer_load_dwordx4 v241,s[32:35],s7 offen lds\nv_exp_f32 v138,v138\nM_ 32,168,216\nv_fract_f32 v139,v139\nM_ 36,172,216\nv_cos_f32 v110,v139\n"
"M_ 40,176,216\nv_sin_f32 v111,v139\nM_ 44,180,216\nds_read_b128 v[216:219],v231 offset:16384\nv_min_f32 v138,0x7e967699,v138\nv_mul_f32 v110,v110,v138\nM_ 48,168,220\ns_add_u32 m0,s77,24576\n"
"s_add_u32 s7,s29,0\nbuffer_load_dwordx4 v241,s[36:39],s7 offen lds\nv_mul_f32 v111,v111,v138\nv_mul_f32 v140,v136,v131\nM_ 52,172,220\nv_mul_f32 v141,v136,v132\nM_ 56,176,220\nv_exp_f32 v140,v140\n"
"M_ 60,180,220\nds_read_b128 v[220:223],v231 offset:24576\nv_fract_f32 v141,v141\ns_waitcnt lgkmcnt(5)\nM_ 32,184,200\nv_cos_f32 v242,v141\nM_ 36,188,200\nv_sin_f32 v243,v141\n"
"M_ 40,192,200\nv_min_f32 v140,0x7e967699,v140\nv_mul_f32 v242,v242,v140\nM_ 44,196,200\nv_mul_f32 v243,v243,v140\nv_add_f32 v133,-1.0,v106\ns_waitcnt lgkmcnt(4)\nM_ 48,184,204\n"
"v_mul_f32 v134,v128,v128\nM_ 52,188,204\nv_fmac_f32 v134,v129,v129\nv_mul_f32 v244,v133,v128\nM_ 56,192,204\nv_rcp_f32 v134,v134\nM_ 60,196,204\nv_mul_f32 v245,v107,v128\n"
"v_fmac_f32 v244,v107,v129\ns_waitcnt lgkmcnt(3)\nM_ 64,168,208\ns_waitcnt vmcnt(4) lgkmcnt(0)\ns_barrier\nv_fma_f32 v245,-v133,v129,v245\nM_ 68,172,208\nds_read_b128 v[200:203],v230 offset:49152\n"
"s_add_u32 m0,s77,32768\ns_add_u32 s7,s29,73728\nbuffer_load_dwordx4 v241,s[32:35],s7 offen lds\nv_add_f32 v134,v134,v134\nv_mul_f32 v244,v244,v134\nM_ 72,176,208\nds_read_b128 v[204:207],v230 offset:57344\nv_mul_f32 v245,v245,v134\n"
"v_lshlrev_b32 v128,16,v249\nM_ 76,180,208\nds_read_b128 v[208:211],v231 offset:49152\nv_lshlrev_b32 v129,16,v250\nv_lshlrev_b32 v130,16,v251\nM_ 80,168,212\nds_read_b128 v[168:171],v124 offset:32768\nv_mul_f32 v130,0x3fb8aa3b,v130\n"
"M_ 84,172,212\nds_read_b128 v[172:175],v124 offset:34816\nv_exp_f32 v130,v130\nM_ 88,176,212\nds_read_b128 v[176:179],v124 offset:36864\ns_add_u32 m0,s77,40960\ns_add_u32 s7,s29,73728\nbuffer_load_dwordx4 v241,s[36:39],s7 offen lds\n"
"v_cvt_pk_bf16_f32 v130,v130,v130\nv_lshlrev_b32 v130,16,v130\nM_ 92,180,212\nds_read_b128 v[212:215],v231 offset:57344\nds_read_b128 v[180:183],v124 offset:38912\nv_mul_f32 v131,v130,v128\nv_mul_f32 v132,v130,v129\nM_ 64,184,216\n"
"v_mul_f32 v131,0x3fb8aa3b,v131\nM_ 68,188,216\nv_mul_f32 v132,0x3e22f983,v132\nv_mul_f32 v138,1.0,v131\nM_ 72,192,216\nv_mul_f32 v139,1.0,v132\nM_ 76,196,216\nds_read_b128 v[216:219],v232\n"
"s_add_u32 m0,s77,49152\ns_add_u32 s7,s29,147456\nbuffer_load_dwordx4 v241,s[32:35],s7 offen lds\nv_exp_f32 v138,v138\nM_ 80,184,220\nds_read_b128 v[184:187],v125 offset:32768\nv_fract_f32 v139,v139\nM_ 84,188,220\n"
"ds_read_b128 v[188:191],v125 offset:34816\nv_cos_f32 v246,v139\nM_ 88,192,220\nds_read_b128 v[192:195],v125 offset:36864\nv_sin_f32 v247,v139\nM_ 92,196,220\nds_read_b128 v[220:223],v232 offset:8192\nds_read_b128 v[196:199],v125 offset:38912\n"
"v_min_f32 v138,0x7e967699,v138\nv_mul_f32 v246,v246,v138\ns_waitcnt lgkmcnt(10)\nM_ 0,168,200\ns_add_u32 m0,s77,57344\ns_add_u32 s7,s29,147456\nbuffer_load_dwordx4 v241,s[36:39],s7 offen lds\ns_add_u32 s58,s58,1\n"
"s_cmp_eq_u32 s58,9\ns_cselect_b32 s7,-8,1\ns_lshl_b32 s7,s7,7\ns_add_u32 s69,s69,s7\ns_add_u32 s29,s29,s7\nv_mul_f32 v247,v247,v138\nv_mul_f32 v140,0x41000000,v131\ns_waitcnt lgkmcnt(9)\n"
"M_ 4,172,200\nv_mul_f32 v141,0x41000000,v132\ns_waitcnt lgkmcnt(8)\nM_ 8,176,200\nv_exp_f32 v140,v140\ns_waitcnt lgkmcnt(6)\nM_ 12,180,200\nds_read_b128 v[200:203],v233\n"
"v_fract_f32 v141,v141\nM_ 16,168,204\nv_cos_f32 v248,v141\nM_ 20,172,204\ns_add_u32 m0,s77,65536\ns_add_u32 s7,s69,0\nbuffer_load_dwordx4 v137,s[8:11],s7 offen lds\nv_sin_f32 v249,v141\n"
"M_ 24,176,204\nv_min_f32 v140,0x7e967699,v140\nv_mul_f32 v248,v248,v140\nM_ 28,180,204\nds_read_b128 v[204:207],v233 offset:8192\nv_mul_f32 v249,v249,v140\nv_mul_f32 v138,v135,v131\ns_waitcnt lgkmcnt(6)\n"
"M_ 0,184,208\nv_mul_f32 v139,v135,v132\ns_waitcnt lgkmcnt(5)\nM_ 4,188,208\nv_exp_f32 v138,v138\ns_waitcnt lgkmcnt(4)\nM_ 8,192,208\ns_add_u32 m0,s77,73728\n"
"s_add_u32 s7,s69,73728\nbuffer_load_dwordx4 v137,s[8:11],s7 offen lds\nv_fract_f32 v139,v139\ns_waitcnt lgkmcnt(2)\nM_ 12,196,208\nds_read_b128 v[208:211],v232 offset:16384\nv_cos_f32 v250,v139\nM_ 16,184,212\n"
"v_sin_f32 v251,v139\nM_ 20,188,212\nv_min_f32 v138,0x7e967699,v138\nv_mul_f32 v250,v250,v138\nM_ 24,192,212\nv_mul_f32 v251,v251,v138\nv_mul_f32 v140,v136,v131\nM_ 28,196,212\n"
"ds_read_b128 v[212:215],v232 offset:24576\ns_add_u32 m0,s77,81920\ns_add_u32 s7,s29,0\nbuffer_load_dwordx4 v241,s[32:35],s7 offen lds\nv_mul_f32 v141,v136,v132\nM_ 32,168,216\nv_exp_f32 v140,v140\nM_ 36,172,216\n"
"v_fract_f32 v141,v141\nM_ 40,176,216\nv_cos_f32 v252,v141\nM_ 44,180,216\nds_read_b128 v[216:219],v233 offset:16384\nv_sin_f32 v253,v141\nM_ 48,168,220\ns_add_u32 m0,s77,90112\n"
"s_add_u32 s7,s29,0\nbuffer_load_dwordx4 v241,s[36:39],s7 offen lds\nv_min_f32 v140,0x7e967699,v140\nv_mul_f32 v252,v252,v140\nM_ 52,172,220\nv_mul_f32 v253,v253,v140\nv_add_f32 v133,-1.0,v246\nM_ 56,176,220\n"
"v_mul_f32 v134,v128,v128\nM_ 60,180,220\nds_read_b128 v[220:223],v233 offset:24576\nv_fmac_f32 v134,v129,v129\nv_mul_f32 v254,v133,v128\ns_waitcnt lgkmcnt(5)\nM_ 32,184,200\nv_rcp_f32 v134,v134\n"
"M_ 36,188,200\nv_mul_f32 v255,v247,v128\nv_fmac_f32 v254,v247,v129\nM_ 40,192,200\nv_fma_f32 v255,-v133,v129,v255\nM_ 44,196,200\nv_add_f32 v134,v134,v134\nv_mul_f32 v254,v254,v134\n"
"s_waitcnt lgkmcnt(4)\nM_ 48,184,204\nv_mul_f32 v255,v255,v134\nM_ 52,188,204\nM_ 56,192,204\nM_ 60,196,204\ns_waitcnt lgkmcnt(3)\nM_ 64,168,208\n"
"s_waitcnt vmcnt(4) lgkmcnt(0)\ns_barrier\nM_ 68,172,208\nds_read_b128 v[200:203],v228 offset:16384\ns_add_u32 m0,s77,98304\ns_add_u32 s7,s29,73728\nbuffer_load_dwordx4 v241,s[32:35],s7 offen lds\nM_ 72,176,208\n"
"ds_read_b128 v[204:207],v228 offset:24576\nM_ 76,180,208\nds_read_b128 v[208:211],v229 offset:16384\nM_ 80,168,212\nds_read_b128 v[168:171],v226\nM_ 84,172,212\nds_read_b128 v[172:175],v226 offset:2048\nM_ 88,176,212\n"
"ds_read_b128 v[176:179],v226 offset:4096\ns_add_u32 m0,s77,106496\ns_add_u32 s7,s29,73728\nbuffer_load_dwordx4 v241,s[36:39],s7 offen lds\nM_ 92,180,212\nds_read_b128 v[212:215],v229 offset:24576\nds_read_b128 v[180:183],v226 offset:6144\nM_ 64,184,216\n"
"M_ 68,188,216\nM_ 72,192,216\nM_ 76,196,216\nds_read_b128 v[216:219],v228 offset:32768\ns_add_u32 m0,s77,114688\ns_add_u32 s7,s29,147456\nbuffer_load_dwordx4 v241,s[32:35],s7 offen lds\nM_ 80,184,220\n"
"ds_read_b128 v[184:187],v227\nM_ 84,188,220\nds_read_b128 v[188:191],v227 offset:2048\nM_ 88,192,220\nds_read_b128 v[192:195],v227 offset:4096\nM_ 92,196,220\nds_read_b128 v[220:223],v228 offset:40960\nds_read_b128 v[196:199],v227 offset:6144\n"
"s_waitcnt lgkmcnt(10)\nM_ 0,168,200\ns_add_u32 m0,s77,122880\ns_add_u32 s7,s29,147456\nbuffer_load_dwordx4 v241,s[36:39],s7 offen lds\ns_add_u32 s58,s58,1\ns_cmp_eq_u32 s58,9\ns_cselect_b32 s7,-8,1\n"
"s_lshl_b32 s7,s7,7\ns_add_u32 s69,s69,s7\ns_add_u32 s29,s29,s7\ns_waitcnt lgkmcnt(9)\nM_ 4,172,200\ns_waitcnt lgkmcnt(8)\nM_ 8,176,200\ns_waitcnt lgkmcnt(6)\n"
"M_ 12,180,200\nds_read_b128 v[200:203],v229 offset:32768\nM_ 16,168,204\nM_ 20,172,204\ns_add_u32 m0,s77,131072\ns_add_u32 s7,s69,0\nbuffer_load_dwordx4 v137,s[8:11],s7 offen lds\nM_ 24,176,204\n"
"M_ 28,180,204\nds_read_b128 v[204:207],v229 offset:40960\ns_waitcnt lgkmcnt(6)\nM_ 0,184,208\ns_waitcnt lgkmcnt(5)\nM_ 4,188,208\ns_waitcnt lgkmcnt(4)\nM_ 8,192,208\n"
"s_add_u32 m0,s77,139264\ns_add_u32 s7,s69,73728\nbuffer_load_dwordx4 v137,s[8:11],s7 offen lds\ns_waitcnt lgkmcnt(2)\nM_ 12,196,208\nds_read_b128 v[208:211],v228 offset:49152\nM_ 16,184,212\nM_ 20,188,212\n"
"M_ 24,192,212\nM_ 28,196,212\nds_read_b128 v[212:215],v228 offset:57344\ns_add_u32 m0,s77,147456\ns_add_u32 s7,s29,0\nbuffer_load_dwordx4 v241,s[32:35],s7 offen lds\nM_ 32,168,216\nM_ 36,172,216\n"
"M_ 40,176,216\nM_ 44,180,216\nds_read_b128 v[216:219],v229 offset:49152\nM_ 48,168,220\nbuffer_load_dwordx4 v[128:131],v234,s[12:15],0 offen sc1\ns_add_u32 m0,s77,155648\ns_add_u32 s7,s29,0\nbuffer_load_dwordx4 v241,s[36:39],s7 offen lds\n"
"M_ 52,172,220\nM_ 56,176,220\nM_ 60,180,220\nds_read_b128 v[220:223],v229 offset:57344\ns_waitcnt lgkmcnt(5)\nM_ 32,184,200\nM_ 36,188,200\nM_ 40,192,200\n"
"M_ 44,196,200\ns_waitcnt lgkmcnt(4)\nM_ 48,184,204\nM_ 52,188,204\nM_ 56,192,204\nM_ 60,196,204\ns_waitcnt lgkmcnt(3)\nM_ 64,168,208\n"
"s_waitcnt vmcnt(5) lgkmcnt(0)\ns_barrier\nM_ 68,172,208\nds_read_b128 v[200:203],v230 offset:16384\ns_add_u32 m0,s77,0\ns_add_u32 s7,s29,73728\nbuffer_load_dwordx4 v241,s[32:35],s7 offen lds\nM_ 72,176,208\n"
"ds_read_b128 v[204:207],v230 offset:24576\nM_ 76,180,208\nds_read_b128 v[208:211],v231 offset:16384\nM_ 80,168,212\nds_read_b128 v[168:171],v124\nM_ 84,172,212\nds_read_b128 v[172:175],v124 offset:2048\nM_ 88,176,212\n"
"ds_read_b128 v[176:179],v124 offset:4096\ns_add_u32 m0,s77,8192\ns_add_u32 s7,s29,73728\nbuffer_load_dwordx4 v241,s[36:39],s7 offen lds\nM_ 92,180,212\nds_read_b128 v[212:215],v231 offset:24576\nds_read_b128 v[180:183],v124 offset:6144\nM_ 64,184,216\n"
"M_ 68,188,216\nM_ 72,192,216\nM_ 76,196,216\nds_read_b128 v[216:219],v230 offset:32768\ns_add_u32 m0,s77,16384\ns_add_u32 s7,s29,147456\nbuffer_load_dwordx4 v241,s[32:35],s7 offen lds\nM_ 80,184,220\n"
"ds_read_b128 v[184:187],v125\nM_ 84,188,220\nds_read_b128 v[188:191],v125 offset:2048\nM_ 88,192,220\nds_read_b128 v[192:195],v125 offset:4096\nM_ 92,196,220\nds_read_b128 v[220:223],v230 offset:40960\nds_read_b128 v[196:199],v125 offset:6144\n"
"s_waitcnt lgkmcnt(10)\nM_ 0,168,200\ns_add_u32 m0,s77,24576\ns_add_u32 s7,s29,147456\nbuffer_load_dwordx4 v241,s[36:39],s7 offen lds\ns_add_u32 s58,s58,1\ns_cmp_eq_u32 s58,9\ns_cselect_b32 s7,-8,1\n"
"s_lshl_b32 s7,s7,7\ns_add_u32 s69,s69,s7\ns_add_u32 s29,s29,s7\ns_waitcnt lgkmcnt(9)\nM_ 4,172,200\ns_waitcnt lgkmcnt(8)\nM_ 8,176,200\ns_waitcnt lgkmcnt(6)\n"
"M_ 12,180,200\nds_read_b128 v[200:203],v231 offset:32768\nM_ 16,168,204\nM_ 20,172,204\ns_add_u32 m0,s77,32768\ns_add_u32 s7,s69,0\nbuffer_load_dwordx4 v137,s[8:11],s7 offen lds\nM_ 24,176,204\n"
"M_ 28,180,204\nds_read_b128 v[204:207],v231 offset:40960\ns_waitcnt lgkmcnt(6)\nM_ 0,184,208\ns_waitcnt lgkmcnt(5)\nM_ 4,188,208\ns_waitcnt lgkmcnt(4)\nM_ 8,192,208\n"
"s_add_u32 m0,s77,40960\ns_add_u32 s7,s69,73728\nbuffer_load_dwordx4 v137,s[8:11],s7 offen lds\ns_waitcnt lgkmcnt(2)\nM_ 12,196,208\nds_read_b128 v[208:211],v230 offset:49152\nM_ 16,184,212\nM_ 20,188,212\n"
"M_ 24,192,212\nM_ 28,196,212\nds_read_b128 v[212:215],v230 offset:57344\ns_add_u32 m0,s77,49152\ns_add_u32 s7,s29,0\nbuffer_load_dwordx4 v241,s[32:35],s7 offen lds\nM_ 32,168,216\nM_ 36,172,216\n"
"M_ 40,176,216\nM_ 44,180,216\nds_read_b128 v[216:219],v231 offset:49152\nM_ 48,168,220\ns_waitcnt vmcnt(8)\nv_cmp_eq_u32 s[4:5],s96,v129\nv_cmp_eq_u32 vcc,s97,v131\ns_nop 0\n"
"s_and_b64 s[4:5],s[4:5],vcc\ns_cmp_eq_u64 s[4:5],-1\ns_cbranch_scc0 .sl73_%=\n.ok72_%=:\nv_bfe_u32 v234,v241,5,2\nv_lshl_add_u32 v234,v234,6,v241\ns_add_u32 m0,s77,57344\ns_add_u32 s7,s29,0\n"
"buffer_load_dwordx4 v241,s[36:39],s7 offen lds\nM_ 52,172,220\nM_ 56,176,220\nM_ 60,180,220\nds_read_b128 v[220:223],v231 offset:57344\ns_waitcnt lgkmcnt(5)\nM_ 32,184,200\nM_ 36,188,200\n"
"M_ 40,192,200\nM_ 44,196,200\ns_waitcnt lgkmcnt(4)\nM_ 48,184,204\nM_ 52,188,204\nM_ 56,192,204\nM_ 60,196,204\ns_waitcnt lgkmcnt(3)\n"
"M_ 64,168,208\ns_waitcnt vmcnt(4) lgkmcnt(0)\ns_barrier\nM_ 68,172,208\nds_read_b128 v[200:203],v232 offset:16384\ns_add_u32 m0,s77,65536\ns_add_u32 s7,s29,73728\nbuffer_load_dwordx4 v241,s[32:35],s7 offen lds\n"
"M_ 72,176,208\nds_read_b128 v[204:207],v232 offset:24576\nM_ 76,180,208\nds_read_b128 v[208:211],v233 offset:16384\nM_ 80,168,212\nds_read_b128 v[168:171],v142\nM_ 84,172,212\nds_read_b128 v[172:175],v142 offset:2048\n"
"M_ 88,176,212\nds_read_b128 v[176:179],v142 offset:4096\ns_add_u32 m0,s77,73728\ns_add_u32 s7,s29,73728\nbuffer_load_dwordx4 v241,s[36:39],s7 offen lds\nM_ 92,180,212\nds_read_b128 v[212:215],v233 offset:24576\nds_read_b128 v[180:183],v142 offset:6144\n"
"M_ 64,184,216\nM_ 68,188,216\nM_ 72,192,216\nM_ 76,196,216\nds_read_b128 v[216:219],v228\ns_add_u32 m0,s77,81920\ns_add_u32 s7,s29,147456\nbuffer_load_dwordx4 v241,s[32:35],s7 offen lds\n"
"M_ 80,184,220\nds_read_b128 v[184:187],v143\nM_ 84,188,220\nds_read_b128 v[188:191],v143 offset:2048\nM_ 88,192,220\nds_read_b128 v[192:195],v143 offset:4096\nM_ 92,196,220\nds_read_b128 v[220:223],v228 offset:8192\n"
"ds_read_b128 v[196:199],v143 offset:6144\ns_waitcnt lgkmcnt(10)\nM_ 0,168,200\ns_add_u32 m0,s77,90112\ns_add_u32 s7,s29,147456\nbuffer_load_dwordx4 v241,s[36:39],s7 offen lds\ns_add_u32 s58,s58,1\ns_cmp_eq_u32 s58,9\n"
"s_cselect_b32 s7,-8,1\ns_lshl_b32 s7,s7,7\ns_add_u32 s69,s69,s7\ns_add_u32 s29,s29,s7\ns_waitcnt lgkmcnt(9)\nM_ 4,172,200\ns_waitcnt lgkmcnt(8)\nM_ 8,176,200\n"
"s_waitcnt lgkmcnt(6)\nM_ 12,180,200\nds_read_b128 v[200:203],v229\nM_ 16,168,204\nbuffer_load_dwordx2 v[144:145],v121,s[12:15],s22 offen sc1\nM_ 20,172,204\ns_add_u32 m0,s77,98304\ns_add_u32 s7,s16,0\n"
"buffer_load_dwordx4 v241,s[40:43],s7 offen lds\nM_ 24,176,204\nM_ 28,180,204\nds_read_b128 v[204:207],v229 offset:8192\ns_waitcnt lgkmcnt(6)\nM_ 0,184,208\ns_waitcnt lgkmcnt(5)\nM_ 4,188,208\n"
"buffer_load_dwordx2 v[146:147],v121,s[12:15],s23 offen sc1\ns_waitcnt lgkmcnt(4)\nM_ 8,192,208\ns_add_u32 m0,s77,106496\ns_add_u32 s7,s16,18432\nbuffer_load_dwordx4 v241,s[40:43],s7 offen lds\ns_waitcnt lgkmcnt(2)\nM_ 12,196,208\n"
"ds_read_b128 v[208:211],v228 offset:16384\nM_ 16,184,212\nM_ 20,188,212\nM_ 24,192,212\nbuffer_load_dwordx2 v[148:149],v121,s[12:15],s22 offen offset:512 sc1\nM_ 28,196,212\nds_read_b128 v[212:215],v228 offset:24576\ns_add_u32 m0,s77,114688\n"
"s_add_u32 s7,s16,36864\nbuffer_load_dwordx4 v241,s[40:43],s7 offen lds\nM_ 32,168,216\nM_ 36,172,216\nM_ 40,176,216\nM_ 44,180,216\nbuffer_load_dwordx2 v[150:151],v121,s[12:15],s23 offen offset:512 sc1\nds_read_b128 v[216:219],v229 offset:16384\n"
"M_ 48,168,220\ns_add_u32 m0,s77,122880\ns_add_u32 s7,s16,0\nbuffer_load_dwordx4 v241,s[44:47],s7 offen lds\nM_ 52,172,220\nM_ 56,176,220\nM_ 60,180,220\nds_read_b128 v[220:223],v229 offset:24576\n"
"s_waitcnt lgkmcnt(5)\nM_ 32,184,200\nbuffer_load_dwordx2 v[152:153],v121,s[12:15],s22 offen offset:1024 sc1\nM_ 36,188,200\nM_ 40,192,200\nM_ 44,196,200\ns_waitcnt lgkmcnt(4)\nM_ 48,184,204\n"
"M_ 52,188,204\nbuffer_load_dwordx2 v[154:155],v121,s[12:15],s23 offen offset:1024 sc1\nM_ 56,192,204\nM_ 60,196,204\ns_waitcnt lgkmcnt(3)\nM_ 64,168,208\ns_waitcnt vmcnt(10) lgkmcnt(0)\ns_barrier\n"
"M_ 68,172,208\nds_read_b128 v[200:203],v228 offset:49152\ns_add_u32 m0,s77,131072\ns_add_u32 s7,s16,18432\nbuffer_load_dwordx4 v241,s[44:47],s7 offen lds\nM_ 72,176,208\nv_xor_b32 v128,0x80000000,v128\nv_lshrrev_b32 v126,2,v224\n"
"v_and_b32 v127,0xfffff800,v238\nv_add_u32 v126,v127,v126\nds_write_b32 v126,v128 offset:256\nds_write_b32 v126,v130\nv_and_b32 v127,0xffffffc0,v126\nds_read_b128 v[204:207],v228 offset:57344\nM_ 76,180,208\nds_read_b128 v[208:211],v229 offset:49152\n"
"M_ 80,168,212\nds_read_b128 v[168:171],v226 offset:32768\nM_ 84,172,212\nds_read_b128 v[172:175],v226 offset:34816\nM_ 88,176,212\nds_read_b128 v[176:179],v226 offset:36864\ns_add_u32 m0,s77,139264\ns_add_u32 s7,s16,36864\n"
"buffer_load_dwordx4 v241,s[44:47],s7 offen lds\nM_ 92,180,212\nds_read_b128 v[212:215],v229 offset:57344\nds_read_b128 v[180:183],v226 offset:38912\nM_ 64,184,216\nM_ 68,188,216\nM_ 72,192,216\nM_ 76,196,216\n"
"ds_read_b128 v[216:219],v230\ns_add_u32 m0,s77,147456\ns_add_u32 s7,s28,0\nbuffer_load_dwordx4 v234,s[8:11],s7 offen lds\nM_ 80,184,220\nds_read_b128 v[184:187],v227 offset:32768\nM_ 84,188,220\nds_read_b128 v[188:191],v227 offset:34816\n"
"M_ 88,192,220\nds_read_b128 v[192:195],v227 offset:36864\nM_ 92,196,220\nds_read_b128 v[220:223],v230 offset:8192\nds_read_b128 v[196:199],v227 offset:38912\ns_waitcnt lgkmcnt(10)\nM_ 0,168,200\ns_add_u32 m0,s77,155648\n"
"s_add_u32 s7,s16,128\nbuffer_load_dwordx4 v241,s[40:43],s7 offen lds\ns_waitcnt lgkmcnt(9)\nM_ 4,172,200\ns_waitcnt lgkmcnt(8)\nM_ 8,176,200\ns_waitcnt lgkmcnt(6)\nM_ 12,180,200\n"
"ds_read_b128 v[200:203],v231\nM_ 16,168,204\nv_mov_b32 v126,0x80000000\nv_add_u32 v112,0,v239\nM_ 20,172,204\nv_cndmask_b32 v112,v126,v112,s[78:79]\nv_add_u32 v113,2048,v239\nM_ 24,176,204\n"
"v_cndmask_b32 v113,v126,v113,s[80:81]\nv_add_u32 v114,4096,v239\nM_ 28,180,204\nds_read_b128 v[204:207],v231 offset:8192\nv_cndmask_b32 v114,v126,v114,s[82:83]\nv_add_u32 v115,6144,v239\ns_waitcnt lgkmcnt(6)\nM_ 0,184,208\n"
"ds_read_b128 v[128:131],v127\nv_cndmask_b32 v115,v126,v115,s[84:85]\ns_waitcnt lgkmcnt(6)\nM_ 4,188,208\nds_read_b128 v[132:135],v127 offset:16\nv_add_u32 v116,8192,v239\nv_cndmask_b32 v116,v126,v116,s[86:87]\ns_waitcnt lgkmcnt(6)\n"
"M_ 8,192,208\nds_read_b128 v[136:139],v127 offset:32\nv_add_u32 v117,10240,v239\nv_cndmask_b32 v117,v126,v117,s[88:89]\ns_waitcnt lgkmcnt(5)\nM_ 12,196,208\nds_read_b128 v[208:211],v230 offset:16384\nds_read_b128 v[140:143],v127 offset:48\n"
"v_add_u32 v118,12288,v239\nv_cndmask_b32 v118,v126,v118,s[90:91]\nM_ 16,184,212\nv_add_u32 v119,14336,v239\nM_ 20,188,212\nv_cndmask_b32 v119,v126,v119,s[92:93]\nv_lshlrev_b32 v126,3,v224\ns_mov_b32 exec_hi,15\n"
"buffer_load_dword v240,v126,s[40:43],s31 offen\ns_mov_b32 exec_hi,-1\nM_ 24,192,212\nM_ 28,196,212\nds_read_b128 v[212:215],v230 offset:24576\ns_mov_b32 exec_hi,15\nbuffer_load_dword v240,v126,s[44:47],s31 offen\ns_mov_b32 exec_hi,-1\n"
"M_ 32,168,216\nM_ 36,172,216\nM_ 40,176,216\nM_ 44,180,216\nds_read_b128 v[216:219],v231 offset:16384\nv_bfe_u32 v124,v224,5,3\nv_lshrrev_b32 v125,9,v224\nM_ 48,168,220\n"
"v_xor_b32 v124,v124,v125\ns_lshl_b32 s2,s0,1\ns_lshl_b32 s3,s1,11\nv_xor_b32 v124,s2,v124\nM_ 52,172,220\nv_lshlrev_b32 v124,4,v124\nM_ 56,176,220\nv_bfe_u32 v125,v224,8,1\n"
"v_lshl_add_u32 v124,v125,3,v124\nM_ 60,180,220\nds_read_b128 v[220:223],v231 offset:24576\nv_bfe_u32 v125,v224,4,4\nv_lshl_add_u32 v124,v125,7,v124\ns_waitcnt lgkmcnt(9)\nM_ 32,184,200\nv_add_u32 v227,s3,v124\n"
"M_ 36,188,200\nM_ 40,192,200\nM_ 44,196,200\ns_waitcnt lgkmcnt(8)\nM_ 48,184,204\nM_ 52,188,204\nM_ 56,192,204\nM_ 60,196,204\n"
"s_waitcnt lgkmcnt(4)\nM_ 64,168,208\ns_waitcnt lgkmcnt(0)\ns_barrier\nM_ 68,172,208\nM_ 72,176,208\nM_ 76,180,208\nM_ 80,168,212\n"
"ds_read_b128 v[168:171],v127 offset:256\nM_ 84,172,212\nds_read_b128 v[172:175],v127 offset:272\nM_ 88,176,212\nds_read_b128 v[176:179],v127 offset:288\nM_ 92,180,212\nds_read_b128 v[180:183],v127 offset:304\nM_ 64,184,216\n"
"M_ 68,188,216\nM_ 72,192,216\nM_ 76,196,216\nM_ 80,184,220\nM_ 84,188,220\nM_ 88,192,220\nM_ 92,196,220\ns_waitcnt vmcnt(15)\n"
"v_cmp_ne_u32 s[4:5],s96,v145\ns_waitcnt vmcnt(13)\nv_cmp_ne_u32 vcc,s96,v147\ns_nop 0\ns_or_b64 s[4:5],s[4:5],vcc\ns_waitcnt vmcnt(11)\nv_cmp_ne_u32 vcc,s96,v149\ns_nop 0\n"
"s_or_b64 s[4:5],s[4:5],vcc\ns_waitcnt vmcnt(9)\nv_cmp_ne_u32 vcc,s96,v151\ns_nop 0\ns_or_b64 s[4:5],s[4:5],vcc\ns_waitcnt vmcnt(7)\nv_cmp_ne_u32 vcc,s96,v153\ns_nop 0\n"
"s_or_b64 s[4:5],s[4:5],vcc\ns_waitcnt vmcnt(6)\nv_cmp_ne_u32 vcc,s96,v155\ns_nop 0\ns_or_b64 s[4:5],s[4:5],vcc\ns_cbranch_scc1 .sl76_%=\n.ok75_%=:\ns_waitcnt lgkmcnt(3)\n"
"v_pk_fma_f32 v[0:1],v[168:169],v[144:145],v[0:1] op_sel_hi:[1,0,1]\nv_pk_fma_f32 v[2:3],v[170:171],v[144:145],v[2:3] op_sel_hi:[1,0,1]\ns_waitcnt lgkmcnt(2)\nv_pk_fma_f32 v[4:5],v[172:173],v[144:145],v[4:5] op_sel_hi:[1,0,1]\nv_pk_fma_f32 v[6:7],v[174:175],v[144:145],v[6:7] op_sel_hi:[1,0,1]\ns_waitcnt lgkmcnt(1)\nv_pk_fma_f32 v[8:9],v[176:177],v[144:145],v[8:9] op_sel_hi:[1,0,1]\nv_pk_fma_f32 v[10:11],v[178:179],v[144:145],v[10:11] op_sel_hi:[1,0,1]\n"
"s_waitcnt lgkmcnt(0)\nv_pk_fma_f32 v[12:13],v[180:181],v[144:145],v[12:13] op_sel_hi:[1,0,1]\nv_pk_fma_f32 v[14:15],v[182:183],v[144:145],v[14:15] op_sel_hi:[1,0,1]\nv_pk_mul_f32 v[0:1],v[0:1],v[128:129]\nv_pk_mul_f32 v[2:3],v[2:3],v[130:131]\nv_pk_mul_f32 v[4:5],v[4:5],v[132:133]\nv_pk_mul_f32 v[6:7],v[6:7],v[134:135]\nv_pk_mul_f32 v[8:9],v[8:9],v[136:137]\n"
"v_pk_mul_f32 v[10:11],v[10:11],v[138:139]\nv_pk_mul_f32 v[12:13],v[12:13],v[140:141]\nv_pk_mul_f32 v[14:15],v[14:15],v[142:143]\nv_pk_fma_f32 v[16:17],v[168:169],v[146:147],v[16:17] op_sel_hi:[1,0,1]\nv_pk_fma_f32 v[18:19],v[170:171],v[146:147],v[18:19] op_sel_hi:[1,0,1]\nv_pk_fma_f32 v[20:21],v[172:173],v[146:147],v[20:21] op_sel_hi:[1,0,1]\nv_pk_fma_f32 v[22:23],v[174:175],v[146:147],v[22:23] op_sel_hi:[1,0,1]\nv_pk_fma_f32 v[24:25],v[176:177],v[146:147],v[24:25] op_sel_hi:[1,0,1]\n"
"v_pk_fma_f32 v[26:27],v[178:179],v[146:147],v[26:27] op_sel_hi:[1,0,1]\nv_pk_fma_f32 v[28:29],v[180:181],v[146:147],v[28:29] op_sel_hi:[1,0,1]\nv_pk_fma_f32 v[30:31],v[182:183],v[146:147],v[30:31] op_sel_hi:[1,0,1]\nv_pk_mul_f32 v[16:17],v[16:17],v[128:129]\nv_pk_mul_f32 v[18:19],v[18:19],v[130:131]\nv_pk_mul_f32 v[20:21],v[20:21],v[132:133]\nv_pk_mul_f32 v[22:23],v[22:23],v[134:135]\nv_pk_mul_f32 v[24:25],v[24:25],v[136:137]\n"
"v_pk_mul_f32 v[26:27],v[26:27],v[138:139]\nv_pk_mul_f32 v[28:29],v[28:29],v[140:141]\nv_pk_mul_f32 v[30:31],v[30:31],v[142:143]\nv_pk_fma_f32 v[32:33],v[168:169],v[148:149],v[32:33] op_sel_hi:[1,0,1]\nv_pk_fma_f32 v[34:35],v[170:171],v[148:149],v[34:35] op_sel_hi:[1,0,1]\nv_pk_fma_f32 v[36:37],v[172:173],v[148:149],v[36:37] op_sel_hi:[1,0,1]\nv_pk_fma_f32 v[38:39],v[174:175],v[148:149],v[38:39] op_sel_hi:[1,0,1]\nv_pk_fma_f32 v[40:41],v[176:177],v[148:149],v[40:41] op_sel_hi:[1,0,1]\n"
"v_pk_fma_f32 v[42:43],v[178:179],v[148:149],v[42:43] op_sel_hi:[1,0,1]\nv_pk_fma_f32 v[44:45],v[180:181],v[148:149],v[44:45] op_sel_hi:[1,0,1]\nv_pk_fma_f32 v[46:47],v[182:183],v[148:149],v[46:47] op_sel_hi:[1,0,1]\nv_pk_mul_f32 v[32:33],v[32:33],v[128:129]\nv_pk_mul_f32 v[34:35],v[34:35],v[130:131]\nv_pk_mul_f32 v[36:37],v[36:37],v[132:133]\nv_pk_mul_f32 v[38:39],v[38:39],v[134:135]\nv_pk_mul_f32 v[40:41],v[40:41],v[136:137]\n"
"v_pk_mul_f32 v[42:43],v[42:43],v[138:139]\nv_pk_mul_f32 v[44:45],v[44:45],v[140:141]\nv_pk_mul_f32 v[46:47],v[46:47],v[142:143]\nv_pk_fma_f32 v[48:49],v[168:169],v[150:151],v[48:49] op_sel_hi:[1,0,1]\nv_pk_fma_f32 v[50:51],v[170:171],v[150:151],v[50:51] op_sel_hi:[1,0,1]\nv_pk_fma_f32 v[52:53],v[172:173],v[150:151],v[52:53] op_sel_hi:[1,0,1]\nv_pk_fma_f32 v[54:55],v[174:175],v[150:151],v[54:55] op_sel_hi:[1,0,1]\nv_pk_fma_f32 v[56:57],v[176:177],v[150:151],v[56:57] op_sel_hi:[1,0,1]\n"
"v_pk_fma_f32 v[58:59],v[178:179],v[150:151],v[58:59] op_sel_hi:[1,0,1]\nv_pk_fma_f32 v[60:61],v[180:181],v[150:151],v[60:61] op_sel_hi:[1,0,1]\nv_pk_fma_f32 v[62:63],v[182:183],v[150:151],v[62:63] op_sel_hi:[1,0,1]\nv_pk_mul_f32 v[48:49],v[48:49],v[128:129]\nv_pk_mul_f32 v[50:51],v[50:51],v[130:131]\nv_pk_mul_f32 v[52:53],v[52:53],v[132:133]\nv_pk_mul_f32 v[54:55],v[54:55],v[134:135]\nv_pk_mul_f32 v[56:57],v[56:57],v[136:137]\n"
"v_pk_mul_f32 v[58:59],v[58:59],v[138:139]\nv_pk_mul_f32 v[60:61],v[60:61],v[140:141]\nv_pk_mul_f32 v[62:63],v[62:63],v[142:143]\nv_pk_fma_f32 v[64:65],v[168:169],v[152:153],v[64:65] op_sel_hi:[1,0,1]\nv_pk_fma_f32 v[66:67],v[170:171],v[152:153],v[66:67] op_sel_hi:[1,0,1]\nv_pk_fma_f32 v[68:69],v[172:173],v[152:153],v[68:69] op_sel_hi:[1,0,1]\nv_pk_fma_f32 v[70:71],v[174:175],v[152:153],v[70:71] op_sel_hi:[1,0,1]\nv_pk_fma_f32 v[72:73],v[176:177],v[152:153],v[72:73] op_sel_hi:[1,0,1]\n"
"v_pk_fma_f32 v[74:75],v[178:179],v[152:153],v[74:75] op_sel_hi:[1,0,1]\nv_pk_fma_f32 v[76:77],v[180:181],v[152:153],v[76:77] op_sel_hi:[1,0,1]\nv_pk_fma_f32 v[78:79],v[182:183],v[152:153],v[78:79] op_sel_hi:[1,0,1]\nv_pk_mul_f32 v[64:65],v[64:65],v[128:129]\nv_pk_mul_f32 v[66:67],v[66:67],v[130:131]\nv_pk_mul_f32 v[68:69],v[68:69],v[132:133]\nv_pk_mul_f32 v[70:71],v[70:71],v[134:135]\nv_pk_mul_f32 v[72:73],v[72:73],v[136:137]\n"
"v_pk_mul_f32 v[74:75],v[74:75],v[138:139]\nv_pk_mul_f32 v[76:77],v[76:77],v[140:141]\nv_pk_mul_f32 v[78:79],v[78:79],v[142:143]\nv_pk_fma_f32 v[80:81],v[168:169],v[154:155],v[80:81] op_sel_hi:[1,0,1]\nv_pk_fma_f32 v[82:83],v[170:171],v[154:155],v[82:83] op_sel_hi:[1,0,1]\nv_pk_fma_f32 v[84:85],v[172:173],v[154:155],v[84:85] op_sel_hi:[1,0,1]\nv_pk_fma_f32 v[86:87],v[174:175],v[154:155],v[86:87] op_sel_hi:[1,0,1]\nv_pk_fma_f32 v[88:89],v[176:177],v[154:155],v[88:89] op_sel_hi:[1,0,1]\n"
"v_pk_fma_f32 v[90:91],v[178:179],v[154:155],v[90:91] op_sel_hi:[1,0,1]\nv_pk_fma_f32 v[92:93],v[180:181],v[154:155],v[92:93] op_sel_hi:[1,0,1]\nv_pk_fma_f32 v[94:95],v[182:183],v[154:155],v[94:95] op_sel_hi:[1,0,1]\nv_pk_mul_f32 v[80:81],v[80:81],v[128:129]\nv_pk_mul_f32 v[82:83],v[82:83],v[130:131]\nv_pk_mul_f32 v[84:85],v[84:85],v[132:133]\nv_pk_mul_f32 v[86:87],v[86:87],v[134:135]\nv_pk_mul_f32 v[88:89],v[88:89],v[136:137]\n"
"v_pk_mul_f32 v[90:91],v[90:91],v[138:139]\nv_pk_mul_f32 v[92:93],v[92:93],v[140:141]\nv_pk_mul_f32 v[94:95],v[94:95],v[142:143]\nv_pk_mul_f32 v[160:161],v[98:99],v[98:99] op_sel:[0,0] op_sel_hi:[0,1]\nv_pk_mul_f32 v[180:181],v[108:109],v[108:109] op_sel:[0,0] op_sel_hi:[0,1]\nv_pk_mul_f32 v[200:201],v[248:249],v[248:249] op_sel:[0,0] op_sel_hi:[0,1]\nv_pk_fma_f32 v[160:161],v[98:99],v[98:99],v[160:161] op_sel:[1,1,0] op_sel_hi:[1,0,1] neg_lo:[1,0,0]\nv_pk_fma_f32 v[180:181],v[108:109],v[108:109],v[180:181] op_sel:[1,1,0] op_sel_hi:[1,0,1] neg_lo:[1,0,0]\n"
"v_pk_fma_f32 v[200:201],v[248:249],v[248:249],v[200:201] op_sel:[1,1,0] op_sel_hi:[1,0,1] neg_lo:[1,0,0]\nv_mov_b32 v207,s96\nv_mov_b32 v211,s96\nv_pk_fma_f32 v[148:149],v[96:97],v[0:1],v[2:3] op_sel:[0,0,0] op_sel_hi:[0,1,1]\nv_pk_fma_f32 v[150:151],v[96:97],v[16:17],v[18:19] op_sel:[0,0,0] op_sel_hi:[0,1,1]\nv_pk_fma_f32 v[168:169],v[106:107],v[32:33],v[34:35] op_sel:[0,0,0] op_sel_hi:[0,1,1]\nv_pk_fma_f32 v[170:171],v[106:107],v[48:49],v[50:51] op_sel:[0,0,0] op_sel_hi:[0,1,1]\nv_pk_fma_f32 v[188:189],v[246:247],v[64:65],v[66:67] op_sel:[0,0,0] op_sel_hi:[0,1,1]\n"
"v_pk_fma_f32 v[190:191],v[246:247],v[80:81],v[82:83] op_sel:[0,0,0] op_sel_hi:[0,1,1]\nv_pk_fma_f32 v[148:149],v[96:97],v[16:17],v[148:149] op_sel:[1,0,0] op_sel_hi:[1,1,1] neg_lo:[1,0,0] neg_hi:[1,0,0]\nv_pk_fma_f32 v[150:151],v[96:97],v[0:1],v[150:151] op_sel:[1,0,0] op_sel_hi:[1,1,1]\nv_pk_fma_f32 v[168:169],v[106:107],v[48:49],v[168:169] op_sel:[1,0,0] op_sel_hi:[1,1,1] neg_lo:[1,0,0] neg_hi:[1,0,0]\nv_pk_fma_f32 v[170:171],v[106:107],v[32:33],v[170:171] op_sel:[1,0,0] op_sel_hi:[1,1,1]\nv_pk_fma_f32 v[188:189],v[246:247],v[80:81],v[188:189] op_sel:[1,0,0] op_sel_hi:[1,1,1] neg_lo:[1,0,0] neg_hi:[1,0,0]\nv_pk_fma_f32 v[190:191],v[246:247],v[64:65],v[190:191] op_sel:[1,0,0] op_sel_hi:[1,1,1]\nv_pk_fma_f32 v[144:145],v[96:97],v[148:149],v[4:5] op_sel:[0,0,0] op_sel_hi:[0,1,1]\n"
"v_pk_fma_f32 v[146:147],v[96:97],v[150:151],v[20:21] op_sel:[0,0,0] op_sel_hi:[0,1,1]\nv_pk_fma_f32 v[164:165],v[106:107],v[168:169],v[36:37] op_sel:[0,0,0] op_sel_hi:[0,1,1]\nv_pk_fma_f32 v[166:167],v[106:107],v[170:171],v[52:53] op_sel:[0,0,0] op_sel_hi:[0,1,1]\nv_pk_fma_f32 v[184:185],v[246:247],v[188:189],v[68:69] op_sel:[0,0,0] op_sel_hi:[0,1,1]\nv_pk_fma_f32 v[186:187],v[246:247],v[190:191],v[84:85] op_sel:[0,0,0] op_sel_hi:[0,1,1]\nv_pk_fma_f32 v[144:145],v[96:97],v[150:151],v[144:145] op_sel:[1,0,0] op_sel_hi:[1,1,1] neg_lo:[1,0,0] neg_hi:[1,0,0]\nv_pk_fma_f32 v[146:147],v[96:97],v[148:149],v[146:147] op_sel:[1,0,0] op_sel_hi:[1,1,1]\nv_pk_fma_f32 v[164:165],v[106:107],v[170:171],v[164:165] op_sel:[1,0,0] op_sel_hi:[1,1,1] neg_lo:[1,0,0] neg_hi:[1,0,0]\n"
"v_pk_fma_f32 v[166:167],v[106:107],v[168:169],v[166:167] op_sel:[1,0,0] op_sel_hi:[1,1,1]\nv_pk_fma_f32 v[184:185],v[246:247],v[190:191],v[184:185] op_sel:[1,0,0] op_sel_hi:[1,1,1] neg_lo:[1,0,0] neg_hi:[1,0,0]\nv_pk_fma_f32 v[186:187],v[246:247],v[188:189],v[186:187] op_sel:[1,0,0] op_sel_hi:[1,1,1]\nv_pk_fma_f32 v[148:149],v[96:97],v[144:145],v[6:7] op_sel:[0,0,0] op_sel_hi:[0,1,1]\nv_pk_fma_f32 v[150:151],v[96:97],v[146:147],v[22:23] op_sel:[0,0,0] op_sel_hi:[0,1,1]\nv_pk_fma_f32 v[168:169],v[106:107],v[164:165],v[38:39] op_sel:[0,0,0] op_sel_hi:[0,1,1]\nv_pk_fma_f32 v[170:171],v[106:107],v[166:167],v[54:55] op_sel:[0,0,0] op_sel_hi:[0,1,1]\nv_pk_fma_f32 v[188:189],v[246:247],v[184:185],v[70:71] op_sel:[0,0,0] op_sel_hi:[0,1,1]\n"
"v_pk_fma_f32 v[190:191],v[246:247],v[186:187],v[86:87] op_sel:[0,0,0] op_sel_hi:[0,1,1]\nv_pk_fma_f32 v[148:149],v[96:97],v[146:147],v[148:149] op_sel:[1,0,0] op_sel_hi:[1,1,1] neg_lo:[1,0,0] neg_hi:[1,0,0]\nv_pk_fma_f32 v[150:151],v[96:97],v[144:145],v[150:151] op_sel:[1,0,0] op_sel_hi:[1,1,1]\nv_pk_fma_f32 v[168:169],v[106:107],v[166:167],v[168:169] op_sel:[1,0,0] op_sel_hi:[1,1,1] neg_lo:[1,0,0] neg_hi:[1,0,0]\nv_pk_fma_f32 v[170:171],v[106:107],v[164:165],v[170:171] op_sel:[1,0,0] op_sel_hi:[1,1,1]\nv_pk_fma_f32 v[188:189],v[246:247],v[186:187],v[188:189] op_sel:[1,0,0] op_sel_hi:[1,1,1] neg_lo:[1,0,0] neg_hi:[1,0,0]\nv_pk_fma_f32 v[190:191],v[246:247],v[184:185],v[190:191] op_sel:[1,0,0] op_sel_hi:[1,1,1]\nv_pk_fma_f32 v[144:145],v[96:97],v[148:149],v[8:9] op_sel:[0,0,0] op_sel_hi:[0,1,1]\n"
"v_pk_fma_f32 v[146:147],v[96:97],v[150:151],v[24:25] op_sel:[0,0,0] op_sel_hi:[0,1,1]\nv_pk_fma_f32 v[164:165],v[106:107],v[168:169],v[40:41] op_sel:[0,0,0] op_sel_hi:[0,1,1]\nv_pk_fma_f32 v[166:167],v[106:107],v[170:171],v[56:57] op_sel:[0,0,0] op_sel_hi:[0,1,1]\nv_pk_fma_f32 v[184:185],v[246:247],v[188:189],v[72:73] op_sel:[0,0,0] op_sel_hi:[0,1,1]\nv_pk_fma_f32 v[186:187],v[246:247],v[190:191],v[88:89] op_sel:[0,0,0] op_sel_hi:[0,1,1]\nv_pk_fma_f32 v[144:145],v[96:97],v[150:151],v[144:145] op_sel:[1,0,0] op_sel_hi:[1,1,1] neg_lo:[1,0,0] neg_hi:[1,0,0]\nv_pk_fma_f32 v[146:147],v[96:97],v[148:149],v[146:147] op_sel:[1,0,0] op_sel_hi:[1,1,1]\nv_pk_fma_f32 v[164:165],v[106:107],v[170:171],v[164:165] op_sel:[1,0,0] op_sel_hi:[1,1,1] neg_lo:[1,0,0] neg_hi:[1,0,0]\n"
"v_pk_fma_f32 v[166:167],v[106:107],v[168:169],v[166:167] op_sel:[1,0,0] op_sel_hi:[1,1,1]\nv_pk_fma_f32 v[184:185],v[246:247],v[190:191],v[184:185] op_sel:[1,0,0] op_sel_hi:[1,1,1] neg_lo:[1,0,0] neg_hi:[1,0,0]\nv_pk_fma_f32 v[186:187],v[246:247],v[188:189],v[186:187] op_sel:[1,0,0] op_sel_hi:[1,1,1]\nv_pk_fma_f32 v[148:149],v[96:97],v[144:145],v[10:11] op_sel:[0,0,0] op_sel_hi:[0,1,1]\nv_pk_fma_f32 v[150:151],v[96:97],v[146:147],v[26:27] op_sel:[0,0,0] op_sel_hi:[0,1,1]\nv_pk_fma_f32 v[168:169],v[106:107],v[164:165],v[42:43] op_sel:[0,0,0] op_sel_hi:[0,1,1]\nv_pk_fma_f32 v[170:171],v[106:107],v[166:167],v[58:59] op_sel:[0,0,0] op_sel_hi:[0,1,1]\nv_pk_fma_f32 v[188:189],v[246:247],v[184:185],v[74:75] op_sel:[0,0,0] op_sel_hi:[0,1,1]\n"
"v_pk_fma_f32 v[190:191],v[246:247],v[186:187],v[90:91] op_sel:[0,0,0] op_sel_hi:[0,1,1]\nv_pk_fma_f32 v[148:149],v[96:97],v[146:147],v[148:149] op_sel:[1,0,0] op_sel_hi:[1,1,1] neg_lo:[1,0,0] neg_hi:[1,0,0]\nv_pk_fma_f32 v[150:151],v[96:97],v[144:145],v[150:151] op_sel:[1,0,0] op_sel_hi:[1,1,1]\nv_pk_fma_f32 v[168:169],v[106:107],v[166:167],v[168:169] op_sel:[1,0,0] op_sel_hi:[1,1,1] neg_lo:[1,0,0] neg_hi:[1,0,0]\nv_pk_fma_f32 v[170:171],v[106:107],v[164:165],v[170:171] op_sel:[1,0,0] op_sel_hi:[1,1,1]\nv_pk_fma_f32 v[188:189],v[246:247],v[186:187],v[188:189] op_sel:[1,0,0] op_sel_hi:[1,1,1] neg_lo:[1,0,0] neg_hi:[1,0,0]\nv_pk_fma_f32 v[190:191],v[246:247],v[184:185],v[190:191] op_sel:[1,0,0] op_sel_hi:[1,1,1]\nv_pk_fma_f32 v[144:145],v[96:97],v[148:149],v[12:13] op_sel:[0,0,0] op_sel_hi:[0,1,1]\n"
"v_pk_fma_f32 v[146:147],v[96:97],v[150:151],v[28:29] op_sel:[0,0,0] op_sel_hi:[0,1,1]\nv_pk_fma_f32 v[164:165],v[106:107],v[168:169],v[44:45] op_sel:[0,0,0] op_sel_hi:[0,1,1]\nv_pk_fma_f32 v[166:167],v[106:107],v[170:171],v[60:61] op_sel:[0,0,0] op_sel_hi:[0,1,1]\nv_pk_fma_f32 v[184:185],v[246:247],v[188:189],v[76:77] op_sel:[0,0,0] op_sel_hi:[0,1,1]\nv_pk_fma_f32 v[186:187],v[246:247],v[190:191],v[92:93] op_sel:[0,0,0] op_sel_hi:[0,1,1]\nv_pk_fma_f32 v[144:145],v[96:97],v[150:151],v[144:145] op_sel:[1,0,0] op_sel_hi:[1,1,1] neg_lo:[1,0,0] neg_hi:[1,0,0]\nv_pk_fma_f32 v[146:147],v[96:97],v[148:149],v[146:147] op_sel:[1,0,0] op_sel_hi:[1,1,1]\nv_pk_fma_f32 v[164:165],v[106:107],v[170:171],v[164:165] op_sel:[1,0,0] op_sel_hi:[1,1,1] neg_lo:[1,0,0] neg_hi:[1,0,0]\n"
"v_pk_fma_f32 v[166:167],v[106:107],v[168:169],v[166:167] op_sel:[1,0,0] op_sel_hi:[1,1,1]\nv_pk_fma_f32 v[184:185],v[246:247],v[190:191],v[184:185] op_sel:[1,0,0] op_sel_hi:[1,1,1] neg_lo:[1,0,0] neg_hi:[1,0,0]\nv_pk_fma_f32 v[186:187],v[246:247],v[188:189],v[186:187] op_sel:[1,0,0] op_sel_hi:[1,1,1]\nv_pk_fma_f32 v[148:149],v[96:97],v[144:145],v[14:15] op_sel:[0,0,0] op_sel_hi:[0,1,1]\nv_pk_fma_f32 v[150:151],v[96:97],v[146:147],v[30:31] op_sel:[0,0,0] op_sel_hi:[0,1,1]\nv_pk_fma_f32 v[168:169],v[106:107],v[164:165],v[46:47] op_sel:[0,0,0] op_sel_hi:[0,1,1]\nv_pk_fma_f32 v[170:171],v[106:107],v[166:167],v[62:63] op_sel:[0,0,0] op_sel_hi:[0,1,1]\nv_pk_fma_f32 v[188:189],v[246:247],v[184:185],v[78:79] op_sel:[0,0,0] op_sel_hi:[0,1,1]\n"
"v_pk_fma_f32 v[190:191],v[246:247],v[186:187],v[94:95] op_sel:[0,0,0] op_sel_hi:[0,1,1]\nv_pk_fma_f32 v[148:149],v[96:97],v[146:147],v[148:149] op_sel:[1,0,0] op_sel_hi:[1,1,1] neg_lo:[1,0,0] neg_hi:[1,0,0]\nv_pk_fma_f32 v[150:151],v[96:97],v[144:145],v[150:151] op_sel:[1,0,0] op_sel_hi:[1,1,1]\nv_pk_fma_f32 v[168:169],v[106:107],v[166:167],v[168:169] op_sel:[1,0,0] op_sel_hi:[1,1,1] neg_lo:[1,0,0] neg_hi:[1,0,0]\nv_pk_fma_f32 v[170:171],v[106:107],v[164:165],v[170:171] op_sel:[1,0,0] op_sel_hi:[1,1,1]\nv_pk_fma_f32 v[188:189],v[246:247],v[186:187],v[188:189] op_sel:[1,0,0] op_sel_hi:[1,1,1] neg_lo:[1,0,0] neg_hi:[1,0,0]\nv_pk_fma_f32 v[190:191],v[246:247],v[184:185],v[190:191] op_sel:[1,0,0] op_sel_hi:[1,1,1]\nv_swap_b32 v149,v150\n"
"v_swap_b32 v169,v170\nv_swap_b32 v189,v190\nv_pk_fma_f32 v[150:151],v[98:99],v[148:149],v[150:151] op_sel:[0,0,0] op_sel_hi:[0,1,1]\nv_pk_fma_f32 v[170:171],v[108:109],v[168:169],v[170:171] op_sel:[0,0,0] op_sel_hi:[0,1,1]\nv_pk_fma_f32 v[190:191],v[248:249],v[188:189],v[190:191] op_sel:[0,0,0] op_sel_hi:[0,1,1]\nv_pk_fma_f32 v[150:151],v[98:99],v[148:149],v[150:151] op_sel:[1,1,0] op_sel_hi:[1,0,1] neg_lo:[1,0,0]\nv_pk_fma_f32 v[170:171],v[108:109],v[168:169],v[170:171] op_sel:[1,1,0] op_sel_hi:[1,0,1] neg_lo:[1,0,0]\nv_pk_fma_f32 v[190:191],v[248:249],v[188:189],v[190:191] op_sel:[1,1,0] op_sel_hi:[1,0,1] neg_lo:[1,0,0]\n"
"v_mov_b64 v[152:153],v[150:151]\nv_mov_b64 v[156:157],v[150:151]\nv_mov_b64 v[172:173],v[170:171]\nv_mov_b64 v[176:177],v[170:171]\nv_mov_b64 v[192:193],v[190:191]\nv_mov_b64 v[196:197],v[190:191]\nv_permlane32_swap_b32 v152,v156\nv_permlane32_swap_b32 v153,v157\n"
"v_permlane32_swap_b32 v172,v176\nv_permlane32_swap_b32 v173,v177\nv_permlane32_swap_b32 v192,v196\nv_permlane32_swap_b32 v193,v197\nv_mov_b64 v[154:155],v[152:153]\nv_mov_b64 v[158:159],v[156:157]\nv_mov_b64 v[174:175],v[172:173]\nv_mov_b64 v[178:179],v[176:177]\n"
"v_mov_b64 v[194:195],v[192:193]\nv_mov_b64 v[198:199],v[196:197]\nv_permlane16_swap_b32 v152,v154\nv_permlane16_swap_b32 v156,v158\nv_permlane16_swap_b32 v153,v155\nv_permlane16_swap_b32 v157,v159\nv_permlane16_swap_b32 v172,v174\nv_permlane16_swap_b32 v176,v178\n"
"v_permlane16_swap_b32 v173,v175\nv_permlane16_swap_b32 v177,v179\nv_permlane16_swap_b32 v192,v194\nv_permlane16_swap_b32 v196,v198\nv_permlane16_swap_b32 v193,v195\nv_permlane16_swap_b32 v197,v199\nv_pk_fma_f32 v[154:155],v[160:161],v[152:153],v[154:155] op_sel:[0,0,0] op_sel_hi:[0,1,1]\nv_pk_fma_f32 v[174:175],v[180:181],v[172:173],v[174:175] op_sel:[0,0,0] op_sel_hi:[0,1,1]\n"
"v_pk_fma_f32 v[194:195],v[200:201],v[192:193],v[194:195] op_sel:[0,0,0] op_sel_hi:[0,1,1]\nv_pk_fma_f32 v[154:155],v[160:161],v[152:153],v[154:155] op_sel:[1,1,0] op_sel_hi:[1,0,1] neg_lo:[1,0,0]\nv_pk_fma_f32 v[174:175],v[180:181],v[172:173],v[174:175] op_sel:[1,1,0] op_sel_hi:[1,0,1] neg_lo:[1,0,0]\nv_pk_fma_f32 v[194:195],v[200:201],v[192:193],v[194:195] op_sel:[1,1,0] op_sel_hi:[1,0,1] neg_lo:[1,0,0]\nv_pk_fma_f32 v[156:157],v[160:161],v[154:155],v[156:157] op_sel:[0,0,0] op_sel_hi:[0,1,1]\nv_pk_fma_f32 v[176:177],v[180:181],v[174:175],v[176:177] op_sel:[0,0,0] op_sel_hi:[0,1,1]\nv_pk_fma_f32 v[196:197],v[200:201],v[194:195],v[196:197] op_sel:[0,0,0] op_sel_hi:[0,1,1]\nv_pk_fma_f32 v[156:157],v[160:161],v[154:155],v[156:157] op_sel:[1,1,0] op_sel_hi:[1,0,1] neg_lo:[1,0,0]\n"
"v_pk_fma_f32 v[176:177],v[180:181],v[174:175],v[176:177] op_sel:[1,1,0] op_sel_hi:[1,0,1] neg_lo:[1,0,0]\nv_pk_fma_f32 v[196:197],v[200:201],v[194:195],v[196:197] op_sel:[1,1,0] op_sel_hi:[1,0,1] neg_lo:[1,0,0]\nv_pk_fma_f32 v[158:159],v[160:161],v[156:157],v[158:159] op_sel:[0,0,0] op_sel_hi:[0,1,1]\nv_pk_fma_f32 v[178:179],v[180:181],v[176:177],v[178:179] op_sel:[0,0,0] op_sel_hi:[0,1,1]\nv_pk_fma_f32 v[198:199],v[200:201],v[196:197],v[198:199] op_sel:[0,0,0] op_sel_hi:[0,1,1]\nv_pk_fma_f32 v[158:159],v[160:161],v[156:157],v[158:159] op_sel:[1,1,0] op_sel_hi:[1,0,1] neg_lo:[1,0,0]\nv_pk_fma_f32 v[178:179],v[180:181],v[176:177],v[178:179] op_sel:[1,1,0] op_sel_hi:[1,0,1] neg_lo:[1,0,0]\nv_pk_fma_f32 v[198:199],v[200:201],v[196:197],v[198:199] op_sel:[1,1,0] op_sel_hi:[1,0,1] neg_lo:[1,0,0]\n"
"v_mov_b64 v[204:205],v[158:159]\nv_mov_b32 v206,v178\nv_mov_b64 v[208:209],v[198:199]\nv_mov_b32 v210,v179\ns_mov_b64 exec,0xffff\nbuffer_store_dwordx4 v[204:207],v235,s[12:15],0 offen sc1\nbuffer_store_dwordx4 v[208:211],v235,s[12:15],0 offen offset:256 sc1\ns_mov_b64 exec,-1\n"
"v_cndmask_b32 v120,0,v152,s[62:63]\nv_cndmask_b32 v121,0,v153,s[62:63]\nv_cndmask_b32 v128,0,v172,s[62:63]\nv_cndmask_b32 v129,0,v173,s[62:63]\nv_cndmask_b32 v136,0,v192,s[62:63]\nv_cndmask_b32 v137,0,v193,s[62:63]\nv_cndmask_b32 v120,v120,v154,s[64:65]\nv_cndmask_b32 v121,v121,v155,s[64:65]\n"
"v_cndmask_b32 v128,v128,v174,s[64:65]\nv_cndmask_b32 v129,v129,v175,s[64:65]\nv_cndmask_b32 v136,v136,v194,s[64:65]\nv_cndmask_b32 v137,v137,v195,s[64:65]\nv_cndmask_b32 v120,v120,v156,s[66:67]\nv_cndmask_b32 v121,v121,v157,s[66:67]\nv_cndmask_b32 v128,v128,v176,s[66:67]\nv_cndmask_b32 v129,v129,v177,s[66:67]\n"
"v_cndmask_b32 v136,v136,v196,s[66:67]\nv_cndmask_b32 v137,v137,v197,s[66:67]\nv_pk_mul_f32 v[122:123],v[104:105],v[148:149] op_sel:[0,0] op_sel_hi:[0,1]\nv_pk_mul_f32 v[130:131],v[244:245],v[168:169] op_sel:[0,0] op_sel_hi:[0,1]\nv_pk_mul_f32 v[138:139],v[254:255],v[188:189] op_sel:[0,0] op_sel_hi:[0,1]\nv_pk_fma_f32 v[122:123],v[104:105],v[148:149],v[122:123] op_sel:[1,1,0] op_sel_hi:[1,0,1] neg_lo:[1,0,0]\nv_pk_fma_f32 v[130:131],v[244:245],v[168:169],v[130:131] op_sel:[1,1,0] op_sel_hi:[1,0,1] neg_lo:[1,0,0]\nv_pk_fma_f32 v[138:139],v[254:255],v[188:189],v[138:139] op_sel:[1,1,0] op_sel_hi:[1,0,1] neg_lo:[1,0,0]\n"
"v_pk_mul_f32 v[162:163],v[160:161],v[160:161] op_sel:[0,0] op_sel_hi:[0,1]\nv_pk_mul_f32 v[182:183],v[180:181],v[180:181] op_sel:[0,0] op_sel_hi:[0,1]\nv_pk_mul_f32 v[202:203],v[200:201],v[200:201] op_sel:[0,0] op_sel_hi:[0,1]\nv_pk_fma_f32 v[162:163],v[160:161],v[160:161],v[162:163] op_sel:[1,1,0] op_sel_hi:[1,0,1] neg_lo:[1,0,0]\nv_pk_fma_f32 v[182:183],v[180:181],v[180:181],v[182:183] op_sel:[1,1,0] op_sel_hi:[1,0,1] neg_lo:[1,0,0]\nv_pk_fma_f32 v[202:203],v[200:201],v[200:201],v[202:203] op_sel:[1,1,0] op_sel_hi:[1,0,1] neg_lo:[1,0,0]\nv_pk_mul_f32 v[124:125],v[162:163],v[162:163] op_sel:[0,0] op_sel_hi:[0,1]\nv_pk_mul_f32 v[132:133],v[182:183],v[182:183] op_sel:[0,0] op_sel_hi:[0,1]\n"
"v_pk_mul_f32 v[140:141],v[202:203],v[202:203] op_sel:[0,0] op_sel_hi:[0,1]\nv_pk_fma_f32 v[124:125],v[162:163],v[162:163],v[124:125] op_sel:[1,1,0] op_sel_hi:[1,0,1] neg_lo:[1,0,0]\nv_pk_fma_f32 v[132:133],v[182:183],v[182:183],v[132:133] op_sel:[1,1,0] op_sel_hi:[1,0,1] neg_lo:[1,0,0]\nv_pk_fma_f32 v[140:141],v[202:203],v[202:203],v[140:141] op_sel:[1,1,0] op_sel_hi:[1,0,1] neg_lo:[1,0,0]\nv_pk_mul_f32 v[220:221],v[104:105],v[0:1] op_sel:[1,0] op_sel_hi:[1,1]\nv_pk_mul_f32 v[222:223],v[104:105],v[2:3] op_sel:[1,0] op_sel_hi:[1,1]\nv_pk_mul_f32 v[0:1],v[104:105],v[0:1] op_sel:[0,0] op_sel_hi:[0,1]\nv_pk_mul_f32 v[2:3],v[104:105],v[2:3] op_sel:[0,0] op_sel_hi:[0,1]\n"
"v_pk_fma_f32 v[0:1],v[104:105],v[16:17],v[0:1] op_sel:[1,0,0] op_sel_hi:[1,1,1] neg_lo:[1,0,0] neg_hi:[1,0,0]\nv_pk_fma_f32 v[2:3],v[104:105],v[18:19],v[2:3] op_sel:[1,0,0] op_sel_hi:[1,1,1] neg_lo:[1,0,0] neg_hi:[1,0,0]\nv_pk_fma_f32 v[16:17],v[104:105],v[16:17],v[220:221] op_sel:[0,0,0] op_sel_hi:[0,1,1]\nv_pk_fma_f32 v[18:19],v[104:105],v[18:19],v[222:223] op_sel:[0,0,0] op_sel_hi:[0,1,1]\nv_pk_mul_f32 v[220:221],v[104:105],v[4:5] op_sel:[1,0] op_sel_hi:[1,1]\nv_pk_mul_f32 v[222:223],v[104:105],v[6:7] op_sel:[1,0] op_sel_hi:[1,1]\nv_pk_mul_f32 v[4:5],v[104:105],v[4:5] op_sel:[0,0] op_sel_hi:[0,1]\nv_pk_mul_f32 v[6:7],v[104:105],v[6:7] op_sel:[0,0] op_sel_hi:[0,1]\n"
"v_pk_fma_f32 v[4:5],v[104:105],v[20:21],v[4:5] op_sel:[1,0,0] op_sel_hi:[1,1,1] neg_lo:[1,0,0] neg_hi:[1,0,0]\nv_pk_fma_f32 v[6:7],v[104:105],v[22:23],v[6:7] op_sel:[1,0,0] op_sel_hi:[1,1,1] neg_lo:[1,0,0] neg_hi:[1,0,0]\nv_pk_fma_f32 v[20:21],v[104:105],v[20:21],v[220:221] op_sel:[0,0,0] op_sel_hi:[0,1,1]\nv_pk_fma_f32 v[22:23],v[104:105],v[22:23],v[222:223] op_sel:[0,0,0] op_sel_hi:[0,1,1]\nv_pk_mul_f32 v[220:221],v[104:105],v[8:9] op_sel:[1,0] op_sel_hi:[1,1]\nv_pk_mul_f32 v[222:223],v[104:105],v[10:11] op_sel:[1,0] op_sel_hi:[1,1]\nv_pk_mul_f32 v[8:9],v[104:105],v[8:9] op_sel:[0,0] op_sel_hi:[0,1]\nv_pk_mul_f32 v[10:11],v[104:105],v[10:11] op_sel:[0,0] op_sel_hi:[0,1]\n"
"v_pk_fma_f32 v[8:9],v[104:105],v[24:25],v[8:9] op_sel:[1,0,0] op_sel_hi:[1,1,1] neg_lo:[1,0,0] neg_hi:[1,0,0]\nv_pk_fma_f32 v[10:11],v[104:105],v[26:27],v[10:11] op_sel:[1,0,0] op_sel_hi:[1,1,1] neg_lo:[1,0,0] neg_hi:[1,0,0]\nv_pk_fma_f32 v[24:25],v[104:105],v[24:25],v[220:221] op_sel:[0,0,0] op_sel_hi:[0,1,1]\nv_pk_fma_f32 v[26:27],v[104:105],v[26:27],v[222:223] op_sel:[0,0,0] op_sel_hi:[0,1,1]\nv_pk_mul_f32 v[220:221],v[104:105],v[12:13] op_sel:[1,0] op_sel_hi:[1,1]\nv_pk_mul_f32 v[222:223],v[104:105],v[14:15] op_sel:[1,0] op_sel_hi:[1,1]\nv_pk_mul_f32 v[12:13],v[104:105],v[12:13] op_sel:[0,0] op_sel_hi:[0,1]\nv_pk_mul_f32 v[14:15],v[104:105],v[14:15] op_sel:[0,0] op_sel_hi:[0,1]\n"
"v_pk_fma_f32 v[12:13],v[104:105],v[28:29],v[12:13] op_sel:[1,0,0] op_sel_hi:[1,1,1] neg_lo:[1,0,0] neg_hi:[1,0,0]\nv_pk_fma_f32 v[14:15],v[104:105],v[30:31],v[14:15] op_sel:[1,0,0] op_sel_hi:[1,1,1] neg_lo:[1,0,0] neg_hi:[1,0,0]\nv_pk_fma_f32 v[28:29],v[104:105],v[28:29],v[220:221] op_sel:[0,0,0] op_sel_hi:[0,1,1]\nv_pk_fma_f32 v[30:31],v[104:105],v[30:31],v[222:223] op_sel:[0,0,0] op_sel_hi:[0,1,1]\nv_pk_mul_f32 v[220:221],v[244:245],v[32:33] op_sel:[1,0] op_sel_hi:[1,1]\nv_pk_mul_f32 v[222:223],v[244:245],v[34:35] op_sel:[1,0] op_sel_hi:[1,1]\nv_pk_mul_f32 v[32:33],v[244:245],v[32:33] op_sel:[0,0] op_sel_hi:[0,1]\nv_pk_mul_f32 v[34:35],v[244:245],v[34:35] op_sel:[0,0] op_sel_hi:[0,1]\n"
"v_pk_fma_f32 v[32:33],v[244:245],v[48:49],v[32:33] op_sel:[1,0,0] op_sel_hi:[1,1,1] neg_lo:[1,0,0] neg_hi:[1,0,0]\nv_pk_fma_f32 v[34:35],v[244:245],v[50:51],v[34:35] op_sel:[1,0,0] op_sel_hi:[1,1,1] neg_lo:[1,0,0] neg_hi:[1,0,0]\nv_pk_fma_f32 v[48:49],v[244:245],v[48:49],v[220:221] op_sel:[0,0,0] op_sel_hi:[0,1,1]\nv_pk_fma_f32 v[50:51],v[244:245],v[50:51],v[222:223] op_sel:[0,0,0] op_sel_hi:[0,1,1]\nv_pk_mul_f32 v[220:221],v[244:245],v[36:37] op_sel:[1,0] op_sel_hi:[1,1]\nv_pk_mul_f32 v[222:223],v[244:245],v[38:39] op_sel:[1,0] op_sel_hi:[1,1]\nv_pk_mul_f32 v[36:37],v[244:245],v[36:37] op_sel:[0,0] op_sel_hi:[0,1]\nv_pk_mul_f32 v[38:39],v[244:245],v[38:39] op_sel:[0,0] op_sel_hi:[0,1]\n"
"v_pk_fma_f32 v[36:37],v[244:245],v[52:53],v[36:37] op_sel:[1,0,0] op_sel_hi:[1,1,1] neg_lo:[1,0,0] neg_hi:[1,0,0]\nv_pk_fma_f32 v[38:39],v[244:245],v[54:55],v[38:39] op_sel:[1,0,0] op_sel_hi:[1,1,1] neg_lo:[1,0,0] neg_hi:[1,0,0]\nv_pk_fma_f32 v[52:53],v[244:245],v[52:53],v[220:221] op_sel:[0,0,0] op_sel_hi:[0,1,1]\nv_pk_fma_f32 v[54:55],v[244:245],v[54:55],v[222:223] op_sel:[0,0,0] op_sel_hi:[0,1,1]\nv_pk_mul_f32 v[220:221],v[244:245],v[40:41] op_sel:[1,0] op_sel_hi:[1,1]\nv_pk_mul_f32 v[222:223],v[244:245],v[42:43] op_sel:[1,0] op_sel_hi:[1,1]\nv_pk_mul_f32 v[40:41],v[244:245],v[40:41] op_sel:[0,0] op_sel_hi:[0,1]\nv_pk_mul_f32 v[42:43],v[244:245],v[42:43] op_sel:[0,0] op_sel_hi:[0,1]\n"
"v_pk_fma_f32 v[40:41],v[244:245],v[56:57],v[40:41] op_sel:[1,0,0] op_sel_hi:[1,1,1] neg_lo:[1,0,0] neg_hi:[1,0,0]\nv_pk_fma_f32 v[42:43],v[244:245],v[58:59],v[42:43] op_sel:[1,0,0] op_sel_hi:[1,1,1] neg_lo:[1,0,0] neg_hi:[1,0,0]\nv_pk_fma_f32 v[56:57],v[244:245],v[56:57],v[220:221] op_sel:[0,0,0] op_sel_hi:[0,1,1]\nv_pk_fma_f32 v[58:59],v[244:245],v[58:59],v[222:223] op_sel:[0,0,0] op_sel_hi:[0,1,1]\nv_pk_mul_f32 v[220:221],v[244:245],v[44:45] op_sel:[1,0] op_sel_hi:[1,1]\nv_pk_mul_f32 v[222:223],v[244:245],v[46:47] op_sel:[1,0] op_sel_hi:[1,1]\nv_pk_mul_f32 v[44:45],v[244:245],v[44:45] op_sel:[0,0] op_sel_hi:[0,1]\nv_pk_mul_f32 v[46:47],v[244:245],v[46:47] op_sel:[0,0] op_sel_hi:[0,1]\n"
"v_pk_fma_f32 v[44:45],v[244:245],v[60:61],v[44:45] op_sel:[1,0,0] op_sel_hi:[1,1,1] neg_lo:[1,0,0] neg_hi:[1,0,0]\nv_pk_fma_f32 v[46:47],v[244:245],v[62:63],v[46:47] op_sel:[1,0,0] op_sel_hi:[1,1,1] neg_lo:[1,0,0] neg_hi:[1,0,0]\nv_pk_fma_f32 v[60:61],v[244:245],v[60:61],v[220:221] op_sel:[0,0,0] op_sel_hi:[0,1,1]\nv_pk_fma_f32 v[62:63],v[244:245],v[62:63],v[222:223] op_sel:[0,0,0] op_sel_hi:[0,1,1]\nbuffer_load_dwordx4 v[144:147],v112,s[12:15],0 offen sc1\nbuffer_load_dwordx4 v[148:151],v112,s[12:15],0 offen offset:256 sc1\nbuffer_load_dwordx4 v[152:155],v113,s[12:15],0 offen sc1\nbuffer_load_dwordx4 v[156:159],v113,s[12:15],0 offen offset:256 sc1\n"
"buffer_load_dwordx4 v[160:163],v114,s[12:15],0 offen sc1\nbuffer_load_dwordx4 v[164:167],v114,s[12:15],0 offen offset:256 sc1\nbuffer_load_dwordx4 v[168:171],v115,s[12:15],0 offen sc1\nbuffer_load_dwordx4 v[172:175],v115,s[12:15],0 offen offset:256 sc1\nbuffer_load_dwordx4 v[176:179],v116,s[12:15],0 offen sc1\nbuffer_load_dwordx4 v[180:183],v116,s[12:15],0 offen offset:256 sc1\nbuffer_load_dwordx4 v[184:187],v117,s[12:15],0 offen sc1\nbuffer_load_dwordx4 v[188:191],v117,s[12:15],0 offen offset:256 sc1\n"
"buffer_load_dwordx4 v[192:195],v118,s[12:15],0 offen sc1\nbuffer_load_dwordx4 v[196:199],v118,s[12:15],0 offen offset:256 sc1\nbuffer_load_dwordx4 v[200:203],v119,s[12:15],0 offen sc1\nbuffer_load_dwordx4 v[204:207],v119,s[12:15],0 offen offset:256 sc1\nv_pk_mul_f32 v[220:221],v[254:255],v[64:65] op_sel:[1,0] op_sel_hi:[1,1]\nv_pk_mul_f32 v[222:223],v[254:255],v[66:67] op_sel:[1,0] op_sel_hi:[1,1]\nv_pk_mul_f32 v[64:65],v[254:255],v[64:65] op_sel:[0,0] op_sel_hi:[0,1]\nv_pk_mul_f32 v[66:67],v[254:255],v[66:67] op_sel:[0,0] op_sel_hi:[0,1]\n"
"v_pk_fma_f32 v[64:65],v[254:255],v[80:81],v[64:65] op_sel:[1,0,0] op_sel_hi:[1,1,1] neg_lo:[1,0,0] neg_hi:[1,0,0]\nv_pk_fma_f32 v[66:67],v[254:255],v[82:83],v[66:67] op_sel:[1,0,0] op_sel_hi:[1,1,1] neg_lo:[1,0,0] neg_hi:[1,0,0]\nv_pk_fma_f32 v[80:81],v[254:255],v[80:81],v[220:221] op_sel:[0,0,0] op_sel_hi:[0,1,1]\nv_pk_fma_f32 v[82:83],v[254:255],v[82:83],v[222:223] op_sel:[0,0,0] op_sel_hi:[0,1,1]\nv_pk_mul_f32 v[220:221],v[254:255],v[68:69] op_sel:[1,0] op_sel_hi:[1,1]\nv_pk_mul_f32 v[222:223],v[254:255],v[70:71] op_sel:[1,0] op_sel_hi:[1,1]\nv_pk_mul_f32 v[68:69],v[254:255],v[68:69] op_sel:[0,0] op_sel_hi:[0,1]\nv_pk_mul_f32 v[70:71],v[254:255],v[70:71] op_sel:[0,0] op_sel_hi:[0,1]\n"
"v_pk_fma_f32 v[68:69],v[254:255],v[84:85],v[68:69] op_sel:[1,0,0] op_sel_hi:[1,1,1] neg_lo:[1,0,0] neg_hi:[1,0,0]\nv_pk_fma_f32 v[70:71],v[254:255],v[86:87],v[70:71] op_sel:[1,0,0] op_sel_hi:[1,1,1] neg_lo:[1,0,0] neg_hi:[1,0,0]\nv_pk_fma_f32 v[84:85],v[254:255],v[84:85],v[220:221] op_sel:[0,0,0] op_sel_hi:[0,1,1]\nv_pk_fma_f32 v[86:87],v[254:255],v[86:87],v[222:223] op_sel:[0,0,0] op_sel_hi:[0,1,1]\nv_pk_mul_f32 v[220:221],v[254:255],v[72:73] op_sel:[1,0] op_sel_hi:[1,1]\nv_pk_mul_f32 v[222:223],v[254:255],v[74:75] op_sel:[1,0] op_sel_hi:[1,1]\nv_pk_mul_f32 v[72:73],v[254:255],v[72:73] op_sel:[0,0] op_sel_hi:[0,1]\nv_pk_mul_f32 v[74:75],v[254:255],v[74:75] op_sel:[0,0] op_sel_hi:[0,1]\n"
"v_pk_fma_f32 v[72:73],v[254:255],v[88:89],v[72:73] op_sel:[1,0,0] op_sel_hi:[1,1,1] neg_lo:[1,0,0] neg_hi:[1,0,0]\nv_pk_fma_f32 v[74:75],v[254:255],v[90:91],v[74:75] op_sel:[1,0,0] op_sel_hi:[1,1,1] neg_lo:[1,0,0] neg_hi:[1,0,0]\nv_pk_fma_f32 v[88:89],v[254:255],v[88:89],v[220:221] op_sel:[0,0,0] op_sel_hi:[0,1,1]\nv_pk_fma_f32 v[90:91],v[254:255],v[90:91],v[222:223] op_sel:[0,0,0] op_sel_hi:[0,1,1]\nv_pk_mul_f32 v[220:221],v[254:255],v[76:77] op_sel:[1,0] op_sel_hi:[1,1]\nv_pk_mul_f32 v[222:223],v[254:255],v[78:79] op_sel:[1,0] op_sel_hi:[1,1]\nv_pk_mul_f32 v[76:77],v[254:255],v[76:77] op_sel:[0,0] op_sel_hi:[0,1]\nv_pk_mul_f32 v[78:79],v[254:255],v[78:79] op_sel:[0,0] op_sel_hi:[0,1]\n"
"v_pk_fma_f32 v[76:77],v[254:255],v[92:93],v[76:77] op_sel:[1,0,0] op_sel_hi:[1,1,1] neg_lo:[1,0,0] neg_hi:[1,0,0]\nv_pk_fma_f32 v[78:79],v[254:255],v[94:95],v[78:79] op_sel:[1,0,0] op_sel_hi:[1,1,1] neg_lo:[1,0,0] neg_hi:[1,0,0]\nv_pk_fma_f32 v[92:93],v[254:255],v[92:93],v[220:221] op_sel:[0,0,0] op_sel_hi:[0,1,1]\nv_pk_fma_f32 v[94:95],v[254:255],v[94:95],v[222:223] op_sel:[0,0,0] op_sel_hi:[0,1,1]\ns_waitcnt vmcnt(15)\nv_cmp_ne_u32 s[2:3],s96,v147\ns_waitcnt vmcnt(14)\nv_cmp_ne_u32 vcc,s96,v151\n"
"s_nop 0\ns_or_b64 vcc,vcc,s[2:3]\ns_and_b64 s[4:5],vcc,s[78:79]\ns_waitcnt vmcnt(13)\nv_cmp_ne_u32 s[2:3],s96,v155\ns_waitcnt vmcnt(12)\nv_cmp_ne_u32 vcc,s96,v159\ns_nop 0\n"
"s_or_b64 vcc,vcc,s[2:3]\ns_and_b64 vcc,vcc,s[80:81]\ns_or_b64 s[4:5],s[4:5],vcc\ns_waitcnt vmcnt(11)\nv_cmp_ne_u32 s[2:3],s96,v163\ns_waitcnt vmcnt(10)\nv_cmp_ne_u32 vcc,s96,v167\ns_nop 0\n"
"s_or_b64 vcc,vcc,s[2:3]\ns_and_b64 vcc,vcc,s[82:83]\ns_or_b64 s[4:5],s[4:5],vcc\ns_waitcnt vmcnt(9)\nv_cmp_ne_u32 s[2:3],s96,v171\ns_waitcnt vmcnt(8)\nv_cmp_ne_u32 vcc,s96,v175\ns_nop 0\n"
"s_or_b64 vcc,vcc,s[2:3]\ns_and_b64 vcc,vcc,s[84:85]\ns_or_b64 s[4:5],s[4:5],vcc\ns_waitcnt vmcnt(7)\nv_cmp_ne_u32 s[2:3],s96,v179\ns_waitcnt vmcnt(6)\nv_cmp_ne_u32 vcc,s96,v183\ns_nop 0\n"
"s_or_b64 vcc,vcc,s[2:3]\ns_and_b64 vcc,vcc,s[86:87]\ns_or_b64 s[4:5],s[4:5],vcc\ns_waitcnt vmcnt(5)\nv_cmp_ne_u32 s[2:3],s96,v187\ns_waitcnt vmcnt(4)\nv_cmp_ne_u32 vcc,s96,v191\ns_nop 0\n"
"s_or_b64 vcc,vcc,s[2:3]\ns_and_b64 vcc,vcc,s[88:89]\ns_or_b64 s[4:5],s[4:5],vcc\ns_waitcnt vmcnt(3)\nv_cmp_ne_u32 s[2:3],s96,v195\ns_waitcnt vmcnt(2)\nv_cmp_ne_u32 vcc,s96,v199\ns_nop 0\n"
"s_or_b64 vcc,vcc,s[2:3]\ns_and_b64 vcc,vcc,s[90:91]\ns_or_b64 s[4:5],s[4:5],vcc\ns_waitcnt vmcnt(1)\nv_cmp_ne_u32 s[2:3],s96,v203\ns_waitcnt vmcnt(0)\nv_cmp_ne_u32 vcc,s96,v207\ns_nop 0\n"
"s_or_b64 vcc,vcc,s[2:3]\ns_and_b64 vcc,vcc,s[92:93]\ns_or_b64 s[4:5],s[4:5],vcc\ns_cbranch_scc1 .sl79_%=\n.ok78_%=:\nv_fmac_f32 v154,v132,v146\nv_fmac_f32 v158,v132,v150\nv_pk_fma_f32 v[152:153],v[124:125],v[144:145],v[152:153] op_sel:[0,0,0] op_sel_hi:[0,1,1]\n"
"v_pk_fma_f32 v[156:157],v[140:141],v[148:149],v[156:157] op_sel:[0,0,0] op_sel_hi:[0,1,1]\nv_pk_fma_f32 v[152:153],v[124:125],v[144:145],v[152:153] op_sel:[1,1,0] op_sel_hi:[1,0,1] neg_lo:[1,0,0]\nv_pk_fma_f32 v[156:157],v[140:141],v[148:149],v[156:157] op_sel:[1,1,0] op_sel_hi:[1,0,1] neg_lo:[1,0,0]\nv_fma_f32 v154,-v133,v150,v154\nv_fmac_f32 v158,v133,v146\nv_fmac_f32 v162,v132,v154\nv_fmac_f32 v166,v132,v158\nv_pk_fma_f32 v[160:161],v[124:125],v[152:153],v[160:161] op_sel:[0,0,0] op_sel_hi:[0,1,1]\n"
"v_pk_fma_f32 v[164:165],v[140:141],v[156:157],v[164:165] op_sel:[0,0,0] op_sel_hi:[0,1,1]\nv_pk_fma_f32 v[160:161],v[124:125],v[152:153],v[160:161] op_sel:[1,1,0] op_sel_hi:[1,0,1] neg_lo:[1,0,0]\nv_pk_fma_f32 v[164:165],v[140:141],v[156:157],v[164:165] op_sel:[1,1,0] op_sel_hi:[1,0,1] neg_lo:[1,0,0]\nv_fma_f32 v162,-v133,v158,v162\nv_fmac_f32 v166,v133,v154\nv_fmac_f32 v170,v132,v162\nv_fmac_f32 v174,v132,v166\nv_pk_fma_f32 v[168:169],v[124:125],v[160:161],v[168:169] op_sel:[0,0,0] op_sel_hi:[0,1,1]\n"
"v_pk_fma_f32 v[172:173],v[140:141],v[164:165],v[172:173] op_sel:[0,0,0] op_sel_hi:[0,1,1]\nv_pk_fma_f32 v[168:169],v[124:125],v[160:161],v[168:169] op_sel:[1,1,0] op_sel_hi:[1,0,1] neg_lo:[1,0,0]\nv_pk_fma_f32 v[172:173],v[140:141],v[164:165],v[172:173] op_sel:[1,1,0] op_sel_hi:[1,0,1] neg_lo:[1,0,0]\nv_fma_f32 v170,-v133,v166,v170\nv_fmac_f32 v174,v133,v162\nv_fmac_f32 v178,v132,v170\nv_fmac_f32 v182,v132,v174\nv_pk_fma_f32 v[176:177],v[124:125],v[168:169],v[176:177] op_sel:[0,0,0] op_sel_hi:[0,1,1]\n"
"v_pk_fma_f32 v[180:181],v[140:141],v[172:173],v[180:181] op_sel:[0,0,0] op_sel_hi:[0,1,1]\nv_pk_fma_f32 v[176:177],v[124:125],v[168:169],v[176:177] op_sel:[1,1,0] op_sel_hi:[1,0,1] neg_lo:[1,0,0]\nv_pk_fma_f32 v[180:181],v[140:141],v[172:173],v[180:181] op_sel:[1,1,0] op_sel_hi:[1,0,1] neg_lo:[1,0,0]\nv_fma_f32 v178,-v133,v174,v178\nv_fmac_f32 v182,v133,v170\nv_fmac_f32 v186,v132,v178\nv_fmac_f32 v190,v132,v182\nv_pk_fma_f32 v[184:185],v[124:125],v[176:177],v[184:185] op_sel:[0,0,0] op_sel_hi:[0,1,1]\n"
"v_pk_fma_f32 v[188:189],v[140:141],v[180:181],v[188:189] op_sel:[0,0,0] op_sel_hi:[0,1,1]\nv_pk_fma_f32 v[184:185],v[124:125],v[176:177],v[184:185] op_sel:[1,1,0] op_sel_hi:[1,0,1] neg_lo:[1,0,0]\nv_pk_fma_f32 v[188:189],v[140:141],v[180:181],v[188:189] op_sel:[1,1,0] op_sel_hi:[1,0,1] neg_lo:[1,0,0]\nv_fma_f32 v186,-v133,v182,v186\nv_fmac_f32 v190,v133,v178\nv_fmac_f32 v194,v132,v186\nv_fmac_f32 v198,v132,v190\nv_pk_fma_f32 v[192:193],v[124:125],v[184:185],v[192:193] op_sel:[0,0,0] op_sel_hi:[0,1,1]\n"
"v_pk_fma_f32 v[196:197],v[140:141],v[188:189],v[196:197] op_sel:[0,0,0] op_sel_hi:[0,1,1]\nv_pk_fma_f32 v[192:193],v[124:125],v[184:185],v[192:193] op_sel:[1,1,0] op_sel_hi:[1,0,1] neg_lo:[1,0,0]\nv_pk_fma_f32 v[196:197],v[140:141],v[188:189],v[196:197] op_sel:[1,1,0] op_sel_hi:[1,0,1] neg_lo:[1,0,0]\nv_fma_f32 v194,-v133,v190,v194\nv_fmac_f32 v198,v133,v186\nv_fmac_f32 v202,v132,v194\nv_fmac_f32 v206,v132,v198\nv_pk_fma_f32 v[200:201],v[124:125],v[192:193],v[200:201] op_sel:[0,0,0] op_sel_hi:[0,1,1]\n"
"v_pk_fma_f32 v[204:205],v[140:141],v[196:197],v[204:205] op_sel:[0,0,0] op_sel_hi:[0,1,1]\nv_pk_fma_f32 v[200:201],v[124:125],v[192:193],v[200:201] op_sel:[1,1,0] op_sel_hi:[1,0,1] neg_lo:[1,0,0]\nv_pk_fma_f32 v[204:205],v[140:141],v[196:197],v[204:205] op_sel:[1,1,0] op_sel_hi:[1,0,1] neg_lo:[1,0,0]\nv_fma_f32 v202,-v133,v198,v202\nv_fmac_f32 v206,v133,v194\nv_mov_b32 v203,v206\nv_pk_mul_f32 v[126:127],v[100:101],v[200:201] op_sel:[0,0] op_sel_hi:[0,1]\nv_pk_mul_f32 v[134:135],v[110:111],v[202:203] op_sel:[0,0] op_sel_hi:[0,1]\n"
"v_pk_mul_f32 v[142:143],v[250:251],v[204:205] op_sel:[0,0] op_sel_hi:[0,1]\nv_pk_fma_f32 v[126:127],v[100:101],v[200:201],v[126:127] op_sel:[1,1,0] op_sel_hi:[1,0,1] neg_lo:[1,0,0]\nv_pk_fma_f32 v[134:135],v[110:111],v[202:203],v[134:135] op_sel:[1,1,0] op_sel_hi:[1,0,1] neg_lo:[1,0,0]\nv_pk_fma_f32 v[142:143],v[250:251],v[204:205],v[142:143] op_sel:[1,1,0] op_sel_hi:[1,0,1] neg_lo:[1,0,0]\nv_mov_b64 v[144:145],v[126:127]\nv_mov_b64 v[146:147],v[134:135]\nv_mov_b64 v[148:149],v[142:143]\nv_permlane32_swap_b32 v126,v144\n"
"v_permlane32_swap_b32 v127,v145\nv_permlane32_swap_b32 v134,v146\nv_permlane32_swap_b32 v135,v147\nv_permlane32_swap_b32 v142,v148\nv_permlane32_swap_b32 v143,v149\nv_pk_add_f32 v[126:127],v[126:127],v[144:145]\nv_pk_add_f32 v[134:135],v[134:135],v[146:147]\nv_pk_add_f32 v[142:143],v[142:143],v[148:149]\n"
"v_mov_b64 v[144:145],v[126:127]\nv_mov_b64 v[146:147],v[134:135]\nv_mov_b64 v[148:149],v[142:143]\nv_permlane16_swap_b32 v126,v144\nv_permlane16_swap_b32 v127,v145\nv_permlane16_swap_b32 v134,v146\nv_permlane16_swap_b32 v135,v147\nv_permlane16_swap_b32 v142,v148\n"
"v_permlane16_swap_b32 v143,v149\nv_pk_add_f32 v[126:127],v[126:127],v[144:145]\nv_pk_add_f32 v[134:135],v[134:135],v[146:147]\nv_pk_add_f32 v[142:143],v[142:143],v[148:149]\nv_pk_fma_f32 v[120:121],v[102:103],v[126:127],v[120:121] op_sel:[0,0,0] op_sel_hi:[0,1,1]\nv_pk_fma_f32 v[128:129],v[242:243],v[134:135],v[128:129] op_sel:[0,0,0] op_sel_hi:[0,1,1]\nv_pk_fma_f32 v[136:137],v[252:253],v[142:143],v[136:137] op_sel:[0,0,0] op_sel_hi:[0,1,1]\nv_pk_fma_f32 v[120:121],v[102:103],v[126:127],v[120:121] op_sel:[1,1,0] op_sel_hi:[1,0,1] neg_lo:[1,0,0]\n"
"v_pk_fma_f32 v[128:129],v[242:243],v[134:135],v[128:129] op_sel:[1,1,0] op_sel_hi:[1,0,1] neg_lo:[1,0,0]\nv_pk_fma_f32 v[136:137],v[252:253],v[142:143],v[136:137] op_sel:[1,1,0] op_sel_hi:[1,0,1] neg_lo:[1,0,0]\nv_pk_mul_f32 v[124:125],v[104:105],v[120:121] op_sel:[0,0] op_sel_hi:[0,1]\nv_pk_mul_f32 v[132:133],v[244:245],v[128:129] op_sel:[0,0] op_sel_hi:[0,1]\nv_pk_mul_f32 v[140:141],v[254:255],v[136:137] op_sel:[0,0] op_sel_hi:[0,1]\nv_pk_fma_f32 v[124:125],v[104:105],v[120:121],v[124:125] op_sel:[1,1,0] op_sel_hi:[1,0,1] neg_lo:[1,0,0]\nv_pk_fma_f32 v[132:133],v[244:245],v[128:129],v[132:133] op_sel:[1,1,0] op_sel_hi:[1,0,1] neg_lo:[1,0,0]\nv_pk_fma_f32 v[140:141],v[254:255],v[136:137],v[140:141] op_sel:[1,1,0] op_sel_hi:[1,0,1] neg_lo:[1,0,0]\n"
"v_pk_fma_f32 v[122:123],v[98:99],v[124:125],v[122:123] op_sel:[0,0,0] op_sel_hi:[0,1,1]\nv_pk_fma_f32 v[130:131],v[108:109],v[132:133],v[130:131] op_sel:[0,0,0] op_sel_hi:[0,1,1]\nv_pk_fma_f32 v[138:139],v[248:249],v[140:141],v[138:139] op_sel:[0,0,0] op_sel_hi:[0,1,1]\nv_pk_fma_f32 v[122:123],v[98:99],v[124:125],v[122:123] op_sel:[1,1,0] op_sel_hi:[1,0,1] neg_lo:[1,0,0]\nv_pk_fma_f32 v[130:131],v[108:109],v[132:133],v[130:131] op_sel:[1,1,0] op_sel_hi:[1,0,1] neg_lo:[1,0,0]\nv_pk_fma_f32 v[138:139],v[248:249],v[140:141],v[138:139] op_sel:[1,1,0] op_sel_hi:[1,0,1] neg_lo:[1,0,0]\nv_mov_b32 v120,v124\nv_mov_b32 v121,v122\n"
"v_mov_b32 v122,v125\nv_mov_b32 v128,v132\nv_mov_b32 v129,v130\nv_mov_b32 v130,v133\nv_mov_b32 v136,v140\nv_mov_b32 v137,v138\nv_mov_b32 v138,v141\ns_mul_i32 s6,s0,96\n"
"s_mul_i32 s4,s57,384\ns_lshl_b32 s5,s60,11\ns_lshl_b32 s3,s1,8\ns_add_u32 s5,s5,s3\ns_add_u32 s5,s5,4096\nv_lshrrev_b32 v235,8,v224\nv_lshl_add_u32 v235,v235,3,s6\nv_and_b32 v239,255,v224\n"
"v_pk_fma_f32 v[0:1],v[96:97],v[120:121],v[0:1] op_sel:[0,0,0] op_sel_hi:[0,1,1]\nv_pk_fma_f32 v[16:17],v[96:97],v[122:123],v[16:17] op_sel:[0,0,0] op_sel_hi:[0,1,1]\nv_pk_fma_f32 v[0:1],v[96:97],v[122:123],v[0:1] op_sel:[1,0,0] op_sel_hi:[1,1,1] neg_lo:[1,0,0] neg_hi:[1,0,0]\nv_pk_fma_f32 v[16:17],v[96:97],v[120:121],v[16:17] op_sel:[1,0,0] op_sel_hi:[1,1,1]\nbuffer_load_dwordx2 v[160:161],v235,s[72:75],s4 offen\nv_pk_fma_f32 v[2:3],v[96:97],v[0:1],v[2:3] op_sel:[0,0,0] op_sel_hi:[0,1,1]\nv_pk_fma_f32 v[18:19],v[96:97],v[16:17],v[18:19] op_sel:[0,0,0] op_sel_hi:[0,1,1]\nv_pk_fma_f32 v[2:3],v[96:97],v[16:17],v[2:3] op_sel:[1,0,0] op_sel_hi:[1,1,1] neg_lo:[1,0,0] neg_hi:[1,0,0]\n"
"v_pk_fma_f32 v[18:19],v[96:97],v[0:1],v[18:19] op_sel:[1,0,0] op_sel_hi:[1,1,1]\nbuffer_load_dwordx2 v[162:163],v235,s[72:75],s4 offen offset:32\nv_pk_fma_f32 v[4:5],v[96:97],v[2:3],v[4:5] op_sel:[0,0,0] op_sel_hi:[0,1,1]\nv_pk_fma_f32 v[20:21],v[96:97],v[18:19],v[20:21] op_sel:[0,0,0] op_sel_hi:[0,1,1]\nv_pk_fma_f32 v[4:5],v[96:97],v[18:19],v[4:5] op_sel:[1,0,0] op_sel_hi:[1,1,1] neg_lo:[1,0,0] neg_hi:[1,0,0]\nv_pk_fma_f32 v[20:21],v[96:97],v[2:3],v[20:21] op_sel:[1,0,0] op_sel_hi:[1,1,1]\nbuffer_load_dwordx2 v[164:165],v235,s[72:75],s4 offen offset:64\nv_pk_fma_f32 v[6:7],v[96:97],v[4:5],v[6:7] op_sel:[0,0,0] op_sel_hi:[0,1,1]\n"
"v_pk_fma_f32 v[22:23],v[96:97],v[20:21],v[22:23] op_sel:[0,0,0] op_sel_hi:[0,1,1]\nv_pk_fma_f32 v[6:7],v[96:97],v[20:21],v[6:7] op_sel:[1,0,0] op_sel_hi:[1,1,1] neg_lo:[1,0,0] neg_hi:[1,0,0]\nv_pk_fma_f32 v[22:23],v[96:97],v[4:5],v[22:23] op_sel:[1,0,0] op_sel_hi:[1,1,1]\nbuffer_load_dwordx4 v[144:147],v239,s[12:15],s5 offen sc1\nv_pk_fma_f32 v[8:9],v[96:97],v[6:7],v[8:9] op_sel:[0,0,0] op_sel_hi:[0,1,1]\nv_pk_fma_f32 v[24:25],v[96:97],v[22:23],v[24:25] op_sel:[0,0,0] op_sel_hi:[0,1,1]\nv_pk_fma_f32 v[8:9],v[96:97],v[22:23],v[8:9] op_sel:[1,0,0] op_sel_hi:[1,1,1] neg_lo:[1,0,0] neg_hi:[1,0,0]\nv_pk_fma_f32 v[24:25],v[96:97],v[6:7],v[24:25] op_sel:[1,0,0] op_sel_hi:[1,1,1]\n"
"buffer_load_dwordx4 v[148:151],v239,s[12:15],s5 offen offset:512 sc1\nv_pk_fma_f32 v[10:11],v[96:97],v[8:9],v[10:11] op_sel:[0,0,0] op_sel_hi:[0,1,1]\nv_pk_fma_f32 v[26:27],v[96:97],v[24:25],v[26:27] op_sel:[0,0,0] op_sel_hi:[0,1,1]\nv_pk_fma_f32 v[10:11],v[96:97],v[24:25],v[10:11] op_sel:[1,0,0] op_sel_hi:[1,1,1] neg_lo:[1,0,0] neg_hi:[1,0,0]\nv_pk_fma_f32 v[26:27],v[96:97],v[8:9],v[26:27] op_sel:[1,0,0] op_sel_hi:[1,1,1]\nbuffer_load_dwordx4 v[152:155],v239,s[12:15],s5 offen offset:1024 sc1\nv_pk_fma_f32 v[12:13],v[96:97],v[10:11],v[12:13] op_sel:[0,0,0] op_sel_hi:[0,1,1]\nv_pk_fma_f32 v[28:29],v[96:97],v[26:27],v[28:29] op_sel:[0,0,0] op_sel_hi:[0,1,1]\n"
"v_pk_fma_f32 v[12:13],v[96:97],v[26:27],v[12:13] op_sel:[1,0,0] op_sel_hi:[1,1,1] neg_lo:[1,0,0] neg_hi:[1,0,0]\nv_pk_fma_f32 v[28:29],v[96:97],v[10:11],v[28:29] op_sel:[1,0,0] op_sel_hi:[1,1,1]\nbuffer_load_dwordx4 v[156:159],v239,s[12:15],s5 offen offset:1536 sc1\nv_pk_fma_f32 v[14:15],v[96:97],v[12:13],v[14:15] op_sel:[0,0,0] op_sel_hi:[0,1,1]\nv_pk_fma_f32 v[30:31],v[96:97],v[28:29],v[30:31] op_sel:[0,0,0] op_sel_hi:[0,1,1]\nv_pk_fma_f32 v[14:15],v[96:97],v[28:29],v[14:15] op_sel:[1,0,0] op_sel_hi:[1,1,1] neg_lo:[1,0,0] neg_hi:[1,0,0]\nv_pk_fma_f32 v[30:31],v[96:97],v[12:13],v[30:31] op_sel:[1,0,0] op_sel_hi:[1,1,1]\nv_xor_b32 v112,64,v238\n"
"s_add_u32 s7,s30,0\nv_cvt_pk_bf16_f32 v168,v0,v2\nv_cvt_pk_bf16_f32 v169,v4,v6\nv_cvt_pk_bf16_f32 v170,v8,v10\nv_cvt_pk_bf16_f32 v171,v12,v14\nds_write_b128 v238,v[168:171]\nbuffer_store_dwordx4 v[168:171],v238,s[12:15],s7 offen sc1\nv_cvt_pk_bf16_f32 v172,v1,v3\n"
"v_cvt_pk_bf16_f32 v173,v5,v7\nv_cvt_pk_bf16_f32 v174,v9,v11\nv_cvt_pk_bf16_f32 v175,v13,v15\nds_write_b128 v112,v[172:175] offset:1024\nbuffer_store_dwordx4 v[172:175],v112,s[12:15],s7 offen offset:1024 sc1\ns_add_u32 s7,s30,16384\nv_cvt_pk_bf16_f32 v176,-v16,-v18\nv_cvt_pk_bf16_f32 v177,-v20,-v22\n"
"v_cvt_pk_bf16_f32 v178,-v24,-v26\nv_cvt_pk_bf16_f32 v179,-v28,-v30\nds_write_b128 v238,v[176:179] offset:16384\nbuffer_store_dwordx4 v[176:179],v238,s[12:15],s7 offen sc1\nv_cvt_pk_bf16_f32 v180,-v17,-v19\nv_cvt_pk_bf16_f32 v181,-v21,-v23\nv_cvt_pk_bf16_f32 v182,-v25,-v27\nv_cvt_pk_bf16_f32 v183,-v29,-v31\n"
"ds_write_b128 v112,v[180:183] offset:17408\nbuffer_store_dwordx4 v[180:183],v112,s[12:15],s7 offen offset:1024 sc1\nv_pk_fma_f32 v[32:33],v[106:107],v[128:129],v[32:33] op_sel:[0,0,0] op_sel_hi:[0,1,1]\nv_pk_fma_f32 v[48:49],v[106:107],v[130:131],v[48:49] op_sel:[0,0,0] op_sel_hi:[0,1,1]\nv_pk_fma_f32 v[32:33],v[106:107],v[130:131],v[32:33] op_sel:[1,0,0] op_sel_hi:[1,1,1] neg_lo:[1,0,0] neg_hi:[1,0,0]\nv_pk_fma_f32 v[48:49],v[106:107],v[128:129],v[48:49] op_sel:[1,0,0] op_sel_hi:[1,1,1]\nv_pk_fma_f32 v[34:35],v[106:107],v[32:33],v[34:35] op_sel:[0,0,0] op_sel_hi:[0,1,1]\nv_pk_fma_f32 v[50:51],v[106:107],v[48:49],v[50:51] op_sel:[0,0,0] op_sel_hi:[0,1,1]\n"
"v_pk_fma_f32 v[34:35],v[106:107],v[48:49],v[34:35] op_sel:[1,0,0] op_sel_hi:[1,1,1] neg_lo:[1,0,0] neg_hi:[1,0,0]\nv_pk_fma_f32 v[50:51],v[106:107],v[32:33],v[50:51] op_sel:[1,0,0] op_sel_hi:[1,1,1]\nv_pk_fma_f32 v[36:37],v[106:107],v[34:35],v[36:37] op_sel:[0,0,0] op_sel_hi:[0,1,1]\nv_pk_fma_f32 v[52:53],v[106:107],v[50:51],v[52:53] op_sel:[0,0,0] op_sel_hi:[0,1,1]\nv_pk_fma_f32 v[36:37],v[106:107],v[50:51],v[36:37] op_sel:[1,0,0] op_sel_hi:[1,1,1] neg_lo:[1,0,0] neg_hi:[1,0,0]\nv_pk_fma_f32 v[52:53],v[106:107],v[34:35],v[52:53] op_sel:[1,0,0] op_sel_hi:[1,1,1]\nv_pk_fma_f32 v[38:39],v[106:107],v[36:37],v[38:39] op_sel:[0,0,0] op_sel_hi:[0,1,1]\nv_pk_fma_f32 v[54:55],v[106:107],v[52:53],v[54:55] op_sel:[0,0,0] op_sel_hi:[0,1,1]\n"
"v_pk_fma_f32 v[38:39],v[106:107],v[52:53],v[38:39] op_sel:[1,0,0] op_sel_hi:[1,1,1] neg_lo:[1,0,0] neg_hi:[1,0,0]\nv_pk_fma_f32 v[54:55],v[106:107],v[36:37],v[54:55] op_sel:[1,0,0] op_sel_hi:[1,1,1]\nv_pk_fma_f32 v[40:41],v[106:107],v[38:39],v[40:41] op_sel:[0,0,0] op_sel_hi:[0,1,1]\nv_pk_fma_f32 v[56:57],v[106:107],v[54:55],v[56:57] op_sel:[0,0,0] op_sel_hi:[0,1,1]\nv_pk_fma_f32 v[40:41],v[106:107],v[54:55],v[40:41] op_sel:[1,0,0] op_sel_hi:[1,1,1] neg_lo:[1,0,0] neg_hi:[1,0,0]\nv_pk_fma_f32 v[56:57],v[106:107],v[38:39],v[56:57] op_sel:[1,0,0] op_sel_hi:[1,1,1]\nv_pk_fma_f32 v[42:43],v[106:107],v[40:41],v[42:43] op_sel:[0,0,0] op_sel_hi:[0,1,1]\nv_pk_fma_f32 v[58:59],v[106:107],v[56:57],v[58:59] op_sel:[0,0,0] op_sel_hi:[0,1,1]\n"
"v_pk_fma_f32 v[42:43],v[106:107],v[56:57],v[42:43] op_sel:[1,0,0] op_sel_hi:[1,1,1] neg_lo:[1,0,0] neg_hi:[1,0,0]\nv_pk_fma_f32 v[58:59],v[106:107],v[40:41],v[58:59] op_sel:[1,0,0] op_sel_hi:[1,1,1]\nv_pk_fma_f32 v[44:45],v[106:107],v[42:43],v[44:45] op_sel:[0,0,0] op_sel_hi:[0,1,1]\nv_pk_fma_f32 v[60:61],v[106:107],v[58:59],v[60:61] op_sel:[0,0,0] op_sel_hi:[0,1,1]\nv_pk_fma_f32 v[44:45],v[106:107],v[58:59],v[44:45] op_sel:[1,0,0] op_sel_hi:[1,1,1] neg_lo:[1,0,0] neg_hi:[1,0,0]\nv_pk_fma_f32 v[60:61],v[106:107],v[42:43],v[60:61] op_sel:[1,0,0] op_sel_hi:[1,1,1]\nv_pk_fma_f32 v[46:47],v[106:107],v[44:45],v[46:47] op_sel:[0,0,0] op_sel_hi:[0,1,1]\nv_pk_fma_f32 v[62:63],v[106:107],v[60:61],v[62:63] op_sel:[0,0,0] op_sel_hi:[0,1,1]\n"
"v_pk_fma_f32 v[46:47],v[106:107],v[60:61],v[46:47] op_sel:[1,0,0] op_sel_hi:[1,1,1] neg_lo:[1,0,0] neg_hi:[1,0,0]\nv_pk_fma_f32 v[62:63],v[106:107],v[44:45],v[62:63] op_sel:[1,0,0] op_sel_hi:[1,1,1]\ns_add_u32 s7,s30,32768\nv_cvt_pk_bf16_f32 v184,v32,v34\nv_cvt_pk_bf16_f32 v185,v36,v38\nv_cvt_pk_bf16_f32 v186,v40,v42\nv_cvt_pk_bf16_f32 v187,v44,v46\nds_write_b128 v238,v[184:187] offset:32768\n"
"buffer_store_dwordx4 v[184:187],v238,s[12:15],s7 offen sc1\nv_cvt_pk_bf16_f32 v188,v33,v35\nv_cvt_pk_bf16_f32 v189,v37,v39\nv_cvt_pk_bf16_f32 v190,v41,v43\nv_cvt_pk_bf16_f32 v191,v45,v47\nds_write_b128 v112,v[188:191] offset:33792\nbuffer_store_dwordx4 v[188:191],v112,s[12:15],s7 offen offset:1024 sc1\ns_add_u32 s7,s30,49152\n"
"v_cvt_pk_bf16_f32 v192,-v48,-v50\nv_cvt_pk_bf16_f32 v193,-v52,-v54\nv_cvt_pk_bf16_f32 v194,-v56,-v58\nv_cvt_pk_bf16_f32 v195,-v60,-v62\nds_write_b128 v238,v[192:195] offset:49152\nbuffer_store_dwordx4 v[192:195],v238,s[12:15],s7 offen sc1\nv_cvt_pk_bf16_f32 v196,-v49,-v51\nv_cvt_pk_bf16_f32 v197,-v53,-v55\n"
"v_cvt_pk_bf16_f32 v198,-v57,-v59\nv_cvt_pk_bf16_f32 v199,-v61,-v63\nds_write_b128 v112,v[196:199] offset:50176\nbuffer_store_dwordx4 v[196:199],v112,s[12:15],s7 offen offset:1024 sc1\nv_pk_fma_f32 v[64:65],v[246:247],v[136:137],v[64:65] op_sel:[0,0,0] op_sel_hi:[0,1,1]\nv_pk_fma_f32 v[80:81],v[246:247],v[138:139],v[80:81] op_sel:[0,0,0] op_sel_hi:[0,1,1]\nv_pk_fma_f32 v[64:65],v[246:247],v[138:139],v[64:65] op_sel:[1,0,0] op_sel_hi:[1,1,1] neg_lo:[1,0,0] neg_hi:[1,0,0]\nv_pk_fma_f32 v[80:81],v[246:247],v[136:137],v[80:81] op_sel:[1,0,0] op_sel_hi:[1,1,1]\n"
"v_pk_fma_f32 v[66:67],v[246:247],v[64:65],v[66:67] op_sel:[0,0,0] op_sel_hi:[0,1,1]\nv_pk_fma_f32 v[82:83],v[246:247],v[80:81],v[82:83] op_sel:[0,0,0] op_sel_hi:[0,1,1]\nv_pk_fma_f32 v[66:67],v[246:247],v[80:81],v[66:67] op_sel:[1,0,0] op_sel_hi:[1,1,1] neg_lo:[1,0,0] neg_hi:[1,0,0]\nv_pk_fma_f32 v[82:83],v[246:247],v[64:65],v[82:83] op_sel:[1,0,0] op_sel_hi:[1,1,1]\nv_pk_fma_f32 v[68:69],v[246:247],v[66:67],v[68:69] op_sel:[0,0,0] op_sel_hi:[0,1,1]\nv_pk_fma_f32 v[84:85],v[246:247],v[82:83],v[84:85] op_sel:[0,0,0] op_sel_hi:[0,1,1]\nv_pk_fma_f32 v[68:69],v[246:247],v[82:83],v[68:69] op_sel:[1,0,0] op_sel_hi:[1,1,1] neg_lo:[1,0,0] neg_hi:[1,0,0]\nv_pk_fma_f32 v[84:85],v[246:247],v[66:67],v[84:85] op_sel:[1,0,0] op_sel_hi:[1,1,1]\n"
"v_pk_fma_f32 v[70:71],v[246:247],v[68:69],v[70:71] op_sel:[0,0,0] op_sel_hi:[0,1,1]\nv_pk_fma_f32 v[86:87],v[246:247],v[84:85],v[86:87] op_sel:[0,0,0] op_sel_hi:[0,1,1]\nv_pk_fma_f32 v[70:71],v[246:247],v[84:85],v[70:71] op_sel:[1,0,0] op_sel_hi:[1,1,1] neg_lo:[1,0,0] neg_hi:[1,0,0]\nv_pk_fma_f32 v[86:87],v[246:247],v[68:69],v[86:87] op_sel:[1,0,0] op_sel_hi:[1,1,1]\nv_pk_fma_f32 v[72:73],v[246:247],v[70:71],v[72:73] op_sel:[0,0,0] op_sel_hi:[0,1,1]\nv_pk_fma_f32 v[88:89],v[246:247],v[86:87],v[88:89] op_sel:[0,0,0] op_sel_hi:[0,1,1]\nv_pk_fma_f32 v[72:73],v[246:247],v[86:87],v[72:73] op_sel:[1,0,0] op_sel_hi:[1,1,1] neg_lo:[1,0,0] neg_hi:[1,0,0]\nv_pk_fma_f32 v[88:89],v[246:247],v[70:71],v[88:89] op_sel:[1,0,0] op_sel_hi:[1,1,1]\n"
"v_pk_fma_f32 v[74:75],v[246:247],v[72:73],v[74:75] op_sel:[0,0,0] op_sel_hi:[0,1,1]\nv_pk_fma_f32 v[90:91],v[246:247],v[88:89],v[90:91] op_sel:[0,0,0] op_sel_hi:[0,1,1]\nv_pk_fma_f32 v[74:75],v[246:247],v[88:89],v[74:75] op_sel:[1,0,0] op_sel_hi:[1,1,1] neg_lo:[1,0,0] neg_hi:[1,0,0]\nv_pk_fma_f32 v[90:91],v[246:247],v[72:73],v[90:91] op_sel:[1,0,0] op_sel_hi:[1,1,1]\nv_pk_fma_f32 v[76:77],v[246:247],v[74:75],v[76:77] op_sel:[0,0,0] op_sel_hi:[0,1,1]\nv_pk_fma_f32 v[92:93],v[246:247],v[90:91],v[92:93] op_sel:[0,0,0] op_sel_hi:[0,1,1]\nv_pk_fma_f32 v[76:77],v[246:247],v[90:91],v[76:77] op_sel:[1,0,0] op_sel_hi:[1,1,1] neg_lo:[1,0,0] neg_hi:[1,0,0]\nv_pk_fma_f32 v[92:93],v[246:247],v[74:75],v[92:93] op_sel:[1,0,0] op_sel_hi:[1,1,1]\n"
"v_pk_fma_f32 v[78:79],v[246:247],v[76:77],v[78:79] op_sel:[0,0,0] op_sel_hi:[0,1,1]\nv_pk_fma_f32 v[94:95],v[246:247],v[92:93],v[94:95] op_sel:[0,0,0] op_sel_hi:[0,1,1]\nv_pk_fma_f32 v[78:79],v[246:247],v[92:93],v[78:79] op_sel:[1,0,0] op_sel_hi:[1,1,1] neg_lo:[1,0,0] neg_hi:[1,0,0]\nv_pk_fma_f32 v[94:95],v[246:247],v[76:77],v[94:95] op_sel:[1,0,0] op_sel_hi:[1,1,1]\nv_add_u32 v238,65536,v238\nv_xor_b32 v112,64,v238\ns_add_u32 s7,s30,0\nv_cvt_pk_bf16_f32 v200,v64,v66\n"
"v_cvt_pk_bf16_f32 v201,v68,v70\nv_cvt_pk_bf16_f32 v202,v72,v74\nv_cvt_pk_bf16_f32 v203,v76,v78\nds_write_b128 v238,v[200:203]\nbuffer_store_dwordx4 v[200:203],v238,s[12:15],s7 offen sc1\nv_cvt_pk_bf16_f32 v204,v65,v67\nv_cvt_pk_bf16_f32 v205,v69,v71\nv_cvt_pk_bf16_f32 v206,v73,v75\n"
"v_cvt_pk_bf16_f32 v207,v77,v79\nds_write_b128 v112,v[204:207] offset:1024\nbuffer_store_dwordx4 v[204:207],v112,s[12:15],s7 offen offset:1024 sc1\ns_add_u32 s7,s30,16384\nv_cvt_pk_bf16_f32 v208,-v80,-v82\nv_cvt_pk_bf16_f32 v209,-v84,-v86\nv_cvt_pk_bf16_f32 v210,-v88,-v90\nv_cvt_pk_bf16_f32 v211,-v92,-v94\n"
"ds_write_b128 v238,v[208:211] offset:16384\nbuffer_store_dwordx4 v[208:211],v238,s[12:15],s7 offen sc1\nv_cvt_pk_bf16_f32 v212,-v81,-v83\nv_cvt_pk_bf16_f32 v213,-v85,-v87\nv_cvt_pk_bf16_f32 v214,-v89,-v91\nv_cvt_pk_bf16_f32 v215,-v93,-v95\nds_write_b128 v112,v[212:215] offset:17408\nbuffer_store_dwordx4 v[212:215],v112,s[12:15],s7 offen offset:1024 sc1\n"
"s_waitcnt lgkmcnt(0)\ns_barrier\nv_add_u32 v122,65536,v227\nv_add_u32 v118,65536,v236\nv_add_u32 v119,65536,v237\nv_add_u32 v123,131072,v227\nv_add_u32 v120,131072,v236\nv_add_u32 v121,131072,v237\n"
"ds_read_b128 v[200:203],v230 offset:32768\nds_read_b64_tr_b16 v[168:169],v236\nds_read_b64_tr_b16 v[170:171],v237\nds_read_b64_tr_b16 v[172:173],v236 offset:512\nds_read_b64_tr_b16 v[174:175],v237 offset:512\nds_read_b64_tr_b16 v[176:177],v236 offset:4096\nds_read_b64_tr_b16 v[178:179],v237 offset:4096\nds_read_b64_tr_b16 v[180:181],v236 offset:4608\n"
"ds_read_b64_tr_b16 v[182:183],v237 offset:4608\nds_read_b128 v[204:207],v230 offset:40960\nds_read_b128 v[208:211],v230 offset:49152\nds_read_b64_tr_b16 v[184:185],v236 offset:8192\nds_read_b64_tr_b16 v[186:187],v237 offset:8192\nds_read_b64_tr_b16 v[188:189],v236 offset:8704\nds_read_b64_tr_b16 v[190:191],v237 offset:8704\nds_read_b64_tr_b16 v[192:193],v236 offset:12288\n"
"ds_read_b64_tr_b16 v[194:195],v237 offset:12288\nds_read_b64_tr_b16 v[196:197],v236 offset:12800\nds_read_b64_tr_b16 v[198:199],v237 offset:12800\nds_read_b128 v[212:215],v231 offset:32768\nds_read_b128 v[216:219],v231 offset:40960\nds_read_b128 v[220:223],v231 offset:49152\ns_waitcnt lgkmcnt(15)\nZ_ 0,200,168\n"
"s_waitcnt vmcnt(18)\nv_lshlrev_b32 v242,16,v160\nZ_ 12,200,172\nv_mov_b32 v114,s96\nv_mov_b32 v115,s97\ns_lshl_b32 s30,s60,8\ns_add_u32 s30,s30,392448\nv_and_b32 v243,0xffff0000,v160\n"
"v_lshlrev_b32 v244,16,v161\nZ_ 24,200,176\nv_and_b32 v245,0xffff0000,v161\ns_waitcnt lgkmcnt(13)\nZ_ 36,200,180\nv_lshrrev_b32 v113,1,v224\nv_min_u32 v113,184,v113\ns_waitcnt vmcnt(17)\n"
"v_lshlrev_b32 v246,16,v162\nv_and_b32 v247,0xffff0000,v162\ns_waitcnt lgkmcnt(12)\nZ_ 4,204,168\nv_lshlrev_b32 v248,16,v163\nZ_ 16,204,172\nv_and_b32 v249,0xffff0000,v163\ns_waitcnt vmcnt(16)\n"
"v_lshlrev_b32 v250,16,v164\nZ_ 28,204,176\ns_waitcnt vmcnt(0)\ns_lshl_b32 s3,s57,6\ns_lshl_b32 s2,s76,3\ns_add_u32 s2,s2,s3\ns_add_u32 s2,s2,s30\ns_mov_b64 exec,1\n"
"buffer_store_dwordx2 v[114:115],v224,s[12:15],s2 offen sc1\ns_mov_b64 exec,-1\nv_and_b32 v251,0xffff0000,v164\nZ_ 40,204,180\nv_lshlrev_b32 v252,16,v165\nv_and_b32 v253,0xffff0000,v165\ns_waitcnt lgkmcnt(11)\nZ_ 8,208,168\n"
"ds_read_b64_tr_b16 v[168:169],v236 offset:16384\nds_read_b64_tr_b16 v[170:171],v237 offset:16384\nv_mul_f32 v144,-v144,v146\nZ_ 20,208,172\nds_read_b64_tr_b16 v[172:173],v236 offset:16896\nds_read_b64_tr_b16 v[174:175],v237 offset:16896\nv_mul_f32 v148,-v148,v150\nv_mul_f32 v152,-v152,v154\n"
"Z_ 32,208,176\nds_read_b64_tr_b16 v[176:177],v236 offset:20480\nds_read_b64_tr_b16 v[178:179],v237 offset:20480\nv_mul_f32 v156,-v156,v158\nZ_ 44,208,180\nds_read_b64_tr_b16 v[180:181],v236 offset:20992\nds_read_b64_tr_b16 v[182:183],v237 offset:20992\ns_waitcnt lgkmcnt(10)\n"
"M_ 0,212,184\nM_ 12,212,188\nM_ 24,212,192\ns_waitcnt lgkmcnt(0)\ns_barrier\nM_ 36,212,196\nds_read_b128 v[200:203],v230 offset:57344\ns_add_u32 m0,s77,0\n"
"s_add_u32 s7,s16,18560\nbuffer_load_dwordx4 v241,s[40:43],s7 offen lds\nM_ 4,216,184\nds_read_b128 v[204:207],v232\nM_ 16,216,188\nds_read_b128 v[208:211],v232 offset:8192\nM_ 28,216,192\nds_read_b128 v[212:215],v231 offset:57344\n"
"s_add_u32 m0,s77,8192\ns_add_u32 s7,s16,36992\nbuffer_load_dwordx4 v241,s[40:43],s7 offen lds\nM_ 40,216,196\nds_read_b128 v[216:219],v233\nM_ 8,220,184\nds_read_b64 v[160:161],v123 offset:16384\nds_read_b64 v[162:163],v123 offset:20480\n"
"ds_read_b64_tr_b16 v[184:185],v236 offset:24576\nds_read_b64_tr_b16 v[186:187],v237 offset:24576\nM_ 20,220,188\nds_read_b64_tr_b16 v[188:189],v236 offset:25088\nds_read_b64_tr_b16 v[190:191],v237 offset:25088\ns_add_u32 m0,s77,98304\ns_add_u32 s7,s16,128\nbuffer_load_dwordx4 v241,s[44:47],s7 offen lds\n"
"M_ 32,220,192\nds_read_b64_tr_b16 v[192:193],v236 offset:28672\nds_read_b64_tr_b16 v[194:195],v237 offset:28672\nM_ 44,220,196\nds_read_b128 v[220:223],v233 offset:8192\nds_read_b64_tr_b16 v[196:197],v236 offset:29184\nds_read_b64_tr_b16 v[198:199],v237 offset:29184\ns_waitcnt lgkmcnt(15)\n"
"M_ 0,200,168\ns_add_u32 m0,s77,106496\ns_add_u32 s7,s16,18560\nbuffer_load_dwordx4 v241,s[44:47],s7 offen lds\nM_ 12,200,172\nM_ 24,200,176\ns_waitcnt lgkmcnt(10)\nv_lshlrev_b32 v48,16,v160\n"
"v_and_b32 v49,0xffff0000,v160\nM_ 36,200,180\ns_add_u32 m0,s77,114688\ns_add_u32 s7,s16,36992\nbuffer_load_dwordx4 v241,s[44:47],s7 offen lds\nv_lshlrev_b32 v50,16,v161\nv_and_b32 v51,0xffff0000,v161\nM_ 4,204,168\n"
"v_fma_f32 v164,v48,v146,v144\nM_ 16,204,172\nv_fma_f32 v165,v49,v146,v144\nv_fma_f32 v166,v50,v146,v144\nM_ 28,204,176\nv_fma_f32 v167,v51,v146,v144\nM_ 40,204,180\nv_fmac_f32 v48,v164,v242\n"
"v_fmac_f32 v49,v165,v243\nM_ 8,208,168\nds_read_b64_tr_b16 v[168:169],v236 offset:32768\nds_read_b64_tr_b16 v[170:171],v237 offset:32768\nv_fmac_f32 v50,v166,v244\nM_ 20,208,172\nds_read_b64_tr_b16 v[172:173],v236 offset:33280\nds_read_b64_tr_b16 v[174:175],v237 offset:33280\n"
"v_fmac_f32 v51,v167,v245\ns_waitcnt lgkmcnt(13)\nv_lshlrev_b32 v60,16,v162\nM_ 32,208,176\nds_read_b64_tr_b16 v[176:177],v236 offset:36864\nds_read_b64_tr_b16 v[178:179],v237 offset:36864\nv_and_b32 v61,0xffff0000,v162\nM_ 44,208,180\n"
"ds_read_b64_tr_b16 v[180:181],v236 offset:37376\nds_read_b64_tr_b16 v[182:183],v237 offset:37376\nv_lshlrev_b32 v62,16,v163\nv_and_b32 v63,0xffff0000,v163\ns_waitcnt lgkmcnt(15)\nM_ 0,212,184\nbuffer_load_dwordx2 v[116:117],v113,s[12:15],s30 offen sc1\nv_fma_f32 v164,v60,v150,v148\n"
"s_waitcnt lgkmcnt(13)\nM_ 12,212,188\nv_fma_f32 v165,v61,v150,v148\nv_fma_f32 v166,v62,v150,v148\ns_waitcnt lgkmcnt(11)\nM_ 24,212,192\ns_waitcnt vmcnt(4) lgkmcnt(0)\ns_barrier\n"
"v_fma_f32 v167,v63,v150,v148\nM_ 36,212,196\nds_read_b128 v[200:203],v232 offset:24576\ns_add_u32 m0,s77,16384\ns_add_u32 s7,s28,32\nbuffer_load_dwordx4 v234,s[8:11],s7 offen lds\nv_fmac_f32 v60,v164,v242\nv_fmac_f32 v61,v165,v243\n"
"M_ 4,216,184\nds_read_b128 v[204:207],v228\nv_fmac_f32 v62,v166,v244\nM_ 16,216,188\nds_read_b128 v[208:211],v228 offset:8192\nv_fmac_f32 v63,v167,v245\nM_ 28,216,192\nds_read_b128 v[212:215],v233 offset:24576\n"
"s_add_u32 m0,s77,24576\ns_add_u32 s7,s16,256\nbuffer_load_dwordx4 v241,s[40:43],s7 offen lds\nM_ 40,216,196\nds_read_b128 v[216:219],v229\nM_ 8,220,184\nds_read_b64_tr_b16 v[184:185],v236 offset:40960\nds_read_b64_tr_b16 v[186:187],v237 offset:40960\n"
"M_ 20,220,188\nds_read_b64_tr_b16 v[188:189],v236 offset:41472\nds_read_b64_tr_b16 v[190:191],v237 offset:41472\ns_add_u32 m0,s77,122880\ns_add_u32 s7,s16,18688\nbuffer_load_dwordx4 v241,s[40:43],s7 offen lds\nM_ 32,220,192\nds_read_b64_tr_b16 v[192:193],v236 offset:45056\n"
"ds_read_b64_tr_b16 v[194:195],v237 offset:45056\nM_ 44,220,196\nds_read_b128 v[220:223],v229 offset:8192\nds_read_b64_tr_b16 v[196:197],v236 offset:45568\nds_read_b64_tr_b16 v[198:199],v237 offset:45568\ns_waitcnt lgkmcnt(13)\nM_ 0,200,168\ns_add_u32 m0,s77,131072\n"
"s_add_u32 s7,s16,37120\nbuffer_load_dwordx4 v241,s[40:43],s7 offen lds\nM_ 12,200,172\nM_ 24,200,176\nM_ 36,200,180\ns_add_u32 m0,s77,139264\ns_add_u32 s7,s16,256\nbuffer_load_dwordx4 v241,s[44:47],s7 offen lds\n"
"s_waitcnt lgkmcnt(12)\nM_ 4,204,168\nM_ 16,204,172\nM_ 28,204,176\ns_add_u32 m0,s77,147456\ns_add_u32 s7,s16,18688\nbuffer_load_dwordx4 v241,s[44:47],s7 offen lds\nM_ 40,204,180\n"
"s_waitcnt lgkmcnt(11)\nM_ 8,208,168\nds_read_b64_tr_b16 v[168:169],v236 offset:49152\nds_read_b64_tr_b16 v[170:171],v237 offset:49152\nM_ 20,208,172\nds_read_b64_tr_b16 v[172:173],v236 offset:49664\nds_read_b64_tr_b16 v[174:175],v237 offset:49664\nM_ 32,208,176\n"
"ds_read_b64_tr_b16 v[176:177],v236 offset:53248\nds_read_b64_tr_b16 v[178:179],v237 offset:53248\nM_ 44,208,180\nds_read_b64_tr_b16 v[180:181],v236 offset:53760\nds_read_b64_tr_b16 v[182:183],v237 offset:53760\ns_waitcnt lgkmcnt(15)\nM_ 0,212,184\ns_waitcnt vmcnt(6)\n"
"v_cmp_ne_u64 vcc,v[116:117],v[114:115]\ns_nop 0\ns_cmp_lg_u64 vcc,0\ns_cbranch_scc1 .sl82_%=\n.ok81_%=:\ns_waitcnt lgkmcnt(13)\nM_ 12,212,188\ns_waitcnt lgkmcnt(11)\n"
"M_ 24,212,192\ns_waitcnt vmcnt(5) lgkmcnt(0)\ns_barrier\nM_ 36,212,196\nds_read_b128 v[200:203],v230 offset:32768\ns_add_u32 m0,s77,0\ns_add_u32 s7,s16,37120\nbuffer_load_dwordx4 v241,s[44:47],s7 offen lds\n"
"M_ 4,216,184\nds_read_b128 v[204:207],v230 offset:40960\nM_ 16,216,188\nds_read_b128 v[208:211],v230 offset:49152\nM_ 28,216,192\nds_read_b128 v[212:215],v231 offset:32768\ns_add_u32 m0,s77,8192\ns_add_u32 s7,s28,64\n"
"buffer_load_dwordx4 v234,s[8:11],s7 offen lds\nM_ 40,216,196\nds_read_b128 v[216:219],v231 offset:40960\nM_ 8,220,184\nds_read_b64 v[160:161],v227 offset:16384\nds_read_b64 v[162:163],v227 offset:20480\nds_read_b64_tr_b16 v[184:185],v236 offset:57344\nds_read_b64_tr_b16 v[186:187],v237 offset:57344\n"
"M_ 20,220,188\nds_read_b64_tr_b16 v[188:189],v236 offset:57856\nds_read_b64_tr_b16 v[190:191],v237 offset:57856\ns_add_u32 m0,s77,32768\ns_add_u32 s7,s17,0\nbuffer_load_dwordx4 v241,s[40:43],s7 offen lds\nM_ 32,220,192\nds_read_b64_tr_b16 v[192:193],v236 offset:61440\n"
"ds_read_b64_tr_b16 v[194:195],v237 offset:61440\nM_ 44,220,196\nds_read_b128 v[220:223],v231 offset:49152\nds_read_b64_tr_b16 v[196:197],v236 offset:61952\nds_read_b64_tr_b16 v[198:199],v237 offset:61952\ns_waitcnt lgkmcnt(15)\nM_ 0,200,168\ns_add_u32 m0,s77,40960\n"
"s_add_u32 s7,s17,18432\nbuffer_load_dwordx4 v241,s[40:43],s7 offen lds\nM_ 12,200,172\nM_ 24,200,176\ns_waitcnt lgkmcnt(10)\nv_lshlrev_b32 v52,16,v160\nv_and_b32 v53,0xffff0000,v160\nM_ 36,200,180\n"
"s_add_u32 m0,s77,155648\ns_add_u32 s7,s17,36864\nbuffer_load_dwordx4 v241,s[40:43],s7 offen lds\nv_lshlrev_b32 v54,16,v161\nv_and_b32 v55,0xffff0000,v161\nM_ 4,204,168\nv_fma_f32 v164,v52,v146,v144\nM_ 16,204,172\n"
"v_fma_f32 v165,v53,v146,v144\nv_fma_f32 v166,v54,v146,v144\nM_ 28,204,176\nv_fma_f32 v167,v55,v146,v144\nM_ 40,204,180\nv_fmac_f32 v52,v164,v246\nv_fmac_f32 v53,v165,v247\nM_ 8,208,168\n"
"ds_read_b64_tr_b16 v[168:169],v118\nds_read_b64_tr_b16 v[170:171],v119\nv_fmac_f32 v54,v166,v248\nM_ 20,208,172\nds_read_b64_tr_b16 v[172:173],v118 offset:512\nds_read_b64_tr_b16 v[174:175],v119 offset:512\nv_fmac_f32 v55,v167,v249\ns_waitcnt lgkmcnt(13)\n"
"v_lshlrev_b32 v64,16,v162\nM_ 32,208,176\nds_read_b64_tr_b16 v[176:177],v118 offset:4096\nds_read_b64_tr_b16 v[178:179],v119 offset:4096\nv_and_b32 v65,0xffff0000,v162\nM_ 44,208,180\nds_read_b64_tr_b16 v[180:181],v118 offset:4608\nds_read_b64_tr_b16 v[182:183],v119 offset:4608\n"
"v_lshlrev_b32 v66,16,v163\nv_and_b32 v67,0xffff0000,v163\ns_waitcnt lgkmcnt(15)\nM_ 0,212,184\nv_fma_f32 v164,v64,v150,v148\ns_waitcnt lgkmcnt(13)\nM_ 12,212,188\nv_fma_f32 v165,v65,v150,v148\n"
"v_fma_f32 v166,v66,v150,v148\ns_waitcnt lgkmcnt(11)\nM_ 24,212,192\ns_waitcnt vmcnt(7) lgkmcnt(0)\ns_barrier\nv_fma_f32 v167,v67,v150,v148\nM_ 36,212,196\nds_read_b128 v[200:203],v228 offset:24576\n"
"s_add_u32 m0,s77,16384\ns_add_u32 s7,s20,0\nbuffer_load_dwordx4 v224,s[12:15],s7 offen sc1 lds\nv_fmac_f32 v64,v164,v246\nv_fmac_f32 v65,v165,v247\nM_ 4,216,184\nds_read_b128 v[204:207],v230 offset:57344\nv_fmac_f32 v66,v166,v248\n"
"M_ 16,216,188\nds_read_b128 v[208:211],v232\nv_fmac_f32 v67,v167,v249\nM_ 28,216,192\nds_read_b128 v[212:215],v229 offset:24576\ns_add_u32 m0,s77,49152\ns_add_u32 s7,s20,8192\nbuffer_load_dwordx4 v224,s[12:15],s7 offen sc1 lds\n"
"M_ 40,216,196\nds_read_b128 v[216:219],v231 offset:57344\nM_ 8,220,184\nds_read_b64_tr_b16 v[184:185],v118 offset:8192\nds_read_b64_tr_b16 v[186:187],v119 offset:8192\nM_ 20,220,188\nds_read_b64_tr_b16 v[188:189],v118 offset:8704\nds_read_b64_tr_b16 v[190:191],v119 offset:8704\n"
"s_add_u32 m0,s77,57344\ns_add_u32 s7,s17,0\nbuffer_load_dwordx4 v241,s[44:47],s7 offen lds\nM_ 32,220,192\nds_read_b64_tr_b16 v[192:193],v118 offset:12288\nds_read_b64_tr_b16 v[194:195],v119 offset:12288\nM_ 44,220,196\nds_read_b128 v[220:223],v233\n"
"ds_read_b64_tr_b16 v[196:197],v118 offset:12800\nds_read_b64_tr_b16 v[198:199],v119 offset:12800\ns_waitcnt lgkmcnt(13)\nM_ 0,200,168\ns_add_u32 m0,s77,98304\ns_add_u32 s7,s17,18432\nbuffer_load_dwordx4 v241,s[44:47],s7 offen lds\nM_ 12,200,172\n"
"M_ 24,200,176\nM_ 36,200,180\ns_add_u32 m0,s77,106496\ns_add_u32 s7,s17,36864\nbuffer_load_dwordx4 v241,s[44:47],s7 offen lds\ns_waitcnt lgkmcnt(12)\nM_ 4,204,168\nM_ 16,204,172\n"
"M_ 28,204,176\ns_add_u32 m0,s77,114688\ns_add_u32 s7,s28,73728\nbuffer_load_dwordx4 v234,s[8:11],s7 offen lds\nM_ 40,204,180\ns_waitcnt lgkmcnt(11)\nM_ 8,208,168\nds_read_b64_tr_b16 v[168:169],v118 offset:16384\n"
"ds_read_b64_tr_b16 v[170:171],v119 offset:16384\nM_ 20,208,172\nds_read_b64_tr_b16 v[172:173],v118 offset:16896\nds_read_b64_tr_b16 v[174:175],v119 offset:16896\nM_ 32,208,176\nds_read_b64_tr_b16 v[176:177],v118 offset:20480\nds_read_b64_tr_b16 v[178:179],v119 offset:20480\nM_ 44,208,180\n"
"ds_read_b64_tr_b16 v[180:181],v118 offset:20992\nds_read_b64_tr_b16 v[182:183],v119 offset:20992\ns_waitcnt lgkmcnt(15)\nM_ 0,212,184\ns_waitcnt lgkmcnt(13)\nM_ 12,212,188\ns_waitcnt lgkmcnt(11)\nM_ 24,212,192\n"
"s_waitcnt vmcnt(9) lgkmcnt(0)\ns_barrier\nM_ 36,212,196\nds_read_b128 v[200:203],v232 offset:8192\ns_add_u32 m0,s77,24576\ns_add_u32 s7,s20,16384\nbuffer_load_dwordx4 v224,s[12:15],s7 offen sc1 lds\nM_ 4,216,184\n"
"ds_read_b128 v[204:207],v232 offset:16384\nM_ 16,216,188\nds_read_b128 v[208:211],v228\nM_ 28,216,192\nds_read_b128 v[212:215],v233 offset:8192\ns_add_u32 m0,s77,65536\ns_add_u32 s7,s20,24576\nbuffer_load_dwordx4 v224,s[12:15],s7 offen sc1 lds\n"
"M_ 40,216,196\nds_read_b128 v[216:219],v233 offset:16384\nM_ 8,220,184\nds_read_b64 v[160:161],v227 offset:8192\nds_read_b64 v[162:163],v227 offset:12288\nds_read_b64_tr_b16 v[184:185],v118 offset:24576\nds_read_b64_tr_b16 v[186:187],v119 offset:24576\nM_ 20,220,188\n"
"ds_read_b64_tr_b16 v[188:189],v118 offset:25088\nds_read_b64_tr_b16 v[190:191],v119 offset:25088\ns_add_u32 m0,s77,73728\ns_add_u32 s7,s17,128\nbuffer_load_dwordx4 v241,s[40:43],s7 offen lds\nM_ 32,220,192\nds_read_b64_tr_b16 v[192:193],v118 offset:28672\nds_read_b64_tr_b16 v[194:195],v119 offset:28672\n"
"M_ 44,220,196\nds_read_b128 v[220:223],v229\nds_read_b64_tr_b16 v[196:197],v118 offset:29184\nds_read_b64_tr_b16 v[198:199],v119 offset:29184\ns_waitcnt lgkmcnt(15)\nM_ 0,200,168\ns_add_u32 m0,s77,122880\ns_add_u32 s7,s17,18560\n"
"buffer_load_dwordx4 v241,s[40:43],s7 offen lds\nM_ 12,200,172\nM_ 24,200,176\ns_waitcnt lgkmcnt(10)\nv_lshlrev_b32 v56,16,v160\nv_and_b32 v57,0xffff0000,v160\nM_ 36,200,180\ns_add_u32 m0,s77,131072\n"
"s_add_u32 s7,s17,36992\nbuffer_load_dwordx4 v241,s[40:43],s7 offen lds\nv_lshlrev_b32 v58,16,v161\nv_and_b32 v59,0xffff0000,v161\nM_ 4,204,168\nv_fma_f32 v164,v56,v146,v144\nM_ 16,204,172\nv_fma_f32 v165,v57,v146,v144\n"
"v_fma_f32 v166,v58,v146,v144\nM_ 28,204,176\nv_fma_f32 v167,v59,v146,v144\nM_ 40,204,180\nv_fmac_f32 v56,v164,v250\nv_fmac_f32 v57,v165,v251\nM_ 8,208,168\nv_fmac_f32 v58,v166,v252\n"
"M_ 20,208,172\nv_fmac_f32 v59,v167,v253\ns_waitcnt lgkmcnt(9)\nv_lshlrev_b32 v68,16,v162\nM_ 32,208,176\nv_and_b32 v69,0xffff0000,v162\nM_ 44,208,180\nv_lshlrev_b32 v70,16,v163\n"
"v_and_b32 v71,0xffff0000,v163\ns_waitcnt lgkmcnt(7)\nM_ 0,212,184\nv_fma_f32 v164,v68,v150,v148\ns_waitcnt lgkmcnt(5)\nM_ 12,212,188\nv_fma_f32 v165,v69,v150,v148\nv_fma_f32 v166,v70,v150,v148\n"
"s_waitcnt lgkmcnt(3)\nM_ 24,212,192\ns_waitcnt vmcnt(9) lgkmcnt(0)\ns_barrier\nv_fma_f32 v167,v71,v150,v148\nM_ 36,212,196\nds_read_b128 v[200:203],v228 offset:32768\nds_read_b64_tr_b16 v[168:169],v236 offset:16384\n"
"ds_read_b64_tr_b16 v[170:171],v237 offset:16384\ns_add_u32 m0,s77,0\ns_add_u32 s7,s20,32768\nbuffer_load_dwordx4 v224,s[12:15],s7 offen sc1 lds\nv_fmac_f32 v68,v164,v250\nv_fmac_f32 v69,v165,v251\nM_ 4,216,184\nds_read_b128 v[204:207],v228 offset:40960\n"
"ds_read_b64_tr_b16 v[172:173],v236 offset:16896\nds_read_b64_tr_b16 v[174:175],v237 offset:16896\nv_fmac_f32 v70,v166,v252\nM_ 16,216,188\nds_read_b128 v[208:211],v232 offset:24576\nds_read_b64_tr_b16 v[176:177],v236 offset:20480\nds_read_b64_tr_b16 v[178:179],v237 offset:20480\nv_fmac_f32 v71,v167,v253\n"
"M_ 28,216,192\nds_read_b128 v[212:215],v229 offset:32768\nds_read_b64_tr_b16 v[180:181],v236 offset:20992\nds_read_b64_tr_b16 v[182:183],v237 offset:20992\ns_add_u32 m0,s77,8192\ns_add_u32 s7,s20,40960\nbuffer_load_dwordx4 v224,s[12:15],s7 offen sc1 lds\nM_ 40,216,196\n"
"ds_read_b128 v[216:219],v229 offset:40960\nM_ 8,220,184\nds_read_b64_tr_b16 v[184:185],v236 offset:49152\nds_read_b64_tr_b16 v[186:187],v237 offset:49152\nM_ 20,220,188\nds_read_b64_tr_b16 v[188:189],v236 offset:49664\nds_read_b64_tr_b16 v[190:191],v237 offset:49664\ns_add_u32 m0,s77,81920\n"
"s_add_u32 s7,s17,128\nbuffer_load_dwordx4 v241,s[44:47],s7 offen lds\nM_ 32,220,192\nds_read_b64_tr_b16 v[192:193],v236 offset:53248\nds_read_b64_tr_b16 v[194:195],v237 offset:53248\nM_ 44,220,196\nds_read_b128 v[220:223],v233 offset:24576\nds_read_b64_tr_b16 v[196:197],v236 offset:53760\n"
"ds_read_b64_tr_b16 v[198:199],v237 offset:53760\ns_waitcnt lgkmcnt(15)\nM_ 0,200,168\ns_add_u32 m0,s77,90112\ns_add_u32 s7,s17,18560\nbuffer_load_dwordx4 v241,s[44:47],s7 offen lds\nM_ 12,200,172\ns_waitcnt lgkmcnt(13)\n"
"M_ 24,200,176\ns_waitcnt lgkmcnt(10)\nM_ 36,200,180\ns_add_u32 m0,s77,139264\ns_add_u32 s7,s17,36992\nbuffer_load_dwordx4 v241,s[44:47],s7 offen lds\nM_ 4,204,168\nM_ 16,204,172\n"
"M_ 28,204,176\ns_add_u32 m0,s77,147456\ns_add_u32 s7,s28,73760\nbuffer_load_dwordx4 v234,s[8:11],s7 offen lds\nM_ 40,204,180\nM_ 8,208,168\nM_ 20,208,172\nM_ 32,208,176\n"
"M_ 44,208,180\ns_waitcnt lgkmcnt(7)\nM_ 0,212,184\ns_waitcnt lgkmcnt(5)\nM_ 12,212,188\ns_waitcnt lgkmcnt(3)\nM_ 24,212,192\ns_waitcnt vmcnt(9) lgkmcnt(0)\n"
"s_barrier\nM_ 36,212,196\nds_read_b128 v[200:203],v228 offset:57344\nds_read_b64_tr_b16 v[168:169],v236 offset:24576\nds_read_b64_tr_b16 v[170:171],v237 offset:24576\ns_add_u32 m0,s77,16384\ns_add_u32 s7,s20,49152\nbuffer_load_dwordx4 v224,s[12:15],s7 offen sc1 lds\n"
"M_ 4,216,184\nds_read_b128 v[204:207],v230 offset:32768\nds_read_b64_tr_b16 v[172:173],v236 offset:25088\nds_read_b64_tr_b16 v[174:175],v237 offset:25088\nM_ 16,216,188\nds_read_b128 v[208:211],v230 offset:40960\nds_read_b64_tr_b16 v[176:177],v236 offset:28672\nds_read_b64_tr_b16 v[178:179],v237 offset:28672\n"
"M_ 28,216,192\nds_read_b128 v[212:215],v229 offset:57344\nds_read_b64_tr_b16 v[180:181],v236 offset:29184\nds_read_b64_tr_b16 v[182:183],v237 offset:29184\ns_add_u32 m0,s77,32768\ns_add_u32 s7,s20,57344\nbuffer_load_dwordx4 v224,s[12:15],s7 offen sc1 lds\nM_ 40,216,196\n"
"ds_read_b128 v[216:219],v231 offset:32768\nM_ 8,220,184\nds_read_b64 v[160:161],v122 offset:49152\nds_read_b64 v[162:163],v122 offset:53248\nds_read_b64_tr_b16 v[184:185],v118\nds_read_b64_tr_b16 v[186:187],v119\nM_ 20,220,188\nds_read_b64_tr_b16 v[188:189],v118 offset:512\n"
"ds_read_b64_tr_b16 v[190:191],v119 offset:512\ns_add_u32 m0,s77,40960\ns_add_u32 s7,s17,256\nbuffer_load_dwordx4 v241,s[40:43],s7 offen lds\nM_ 32,220,192\nds_read_b64_tr_b16 v[192:193],v118 offset:4096\nds_read_b64_tr_b16 v[194:195],v119 offset:4096\nM_ 44,220,196\n"
"ds_read_b128 v[220:223],v231 offset:40960\nds_read_b64_tr_b16 v[196:197],v118 offset:4608\nds_read_b64_tr_b16 v[198:199],v119 offset:4608\ns_waitcnt lgkmcnt(15)\nM_ 0,200,168\ns_add_u32 m0,s77,49152\ns_add_u32 s7,s17,18688\nbuffer_load_dwordx4 v241,s[40:43],s7 offen lds\n"
"M_ 12,200,172\nM_ 24,200,176\ns_waitcnt lgkmcnt(10)\nv_lshlrev_b32 v72,16,v160\nv_and_b32 v73,0xffff0000,v160\nM_ 36,200,180\ns_add_u32 m0,s77,155648\ns_add_u32 s7,s17,37120\n"
"buffer_load_dwordx4 v241,s[40:43],s7 offen lds\nv_lshlrev_b32 v74,16,v161\nv_and_b32 v75,0xffff0000,v161\nM_ 4,204,168\nv_fma_f32 v164,v72,v154,v152\nM_ 16,204,172\nv_fma_f32 v165,v73,v154,v152\nv_fma_f32 v166,v74,v154,v152\n"
"M_ 28,204,176\nv_fma_f32 v167,v75,v154,v152\nM_ 40,204,180\nv_fmac_f32 v72,v164,v242\nv_fmac_f32 v73,v165,v243\nM_ 8,208,168\nv_fmac_f32 v74,v166,v244\nM_ 20,208,172\n"
"v_fmac_f32 v75,v167,v245\ns_waitcnt lgkmcnt(9)\nv_lshlrev_b32 v84,16,v162\nM_ 32,208,176\nv_and_b32 v85,0xffff0000,v162\nM_ 44,208,180\nv_lshlrev_b32 v86,16,v163\nv_and_b32 v87,0xffff0000,v163\n"
"s_waitcnt lgkmcnt(7)\nM_ 0,212,184\nv_fma_f32 v164,v84,v158,v156\ns_waitcnt lgkmcnt(5)\nM_ 12,212,188\nv_fma_f32 v165,v85,v158,v156\nv_fma_f32 v166,v86,v158,v156\ns_waitcnt lgkmcnt(3)\n"
"M_ 24,212,192\ns_waitcnt vmcnt(9) lgkmcnt(0)\ns_barrier\nv_fma_f32 v167,v87,v158,v156\nM_ 36,212,196\nds_read_b128 v[200:203],v230 offset:8192\nds_read_b64_tr_b16 v[168:169],v236\nds_read_b64_tr_b16 v[170:171],v237\n"
"s_add_u32 m0,s77,24576\ns_add_u32 s7,s20,65536\nbuffer_load_dwordx4 v224,s[12:15],s7 offen sc1 lds\nv_fmac_f32 v84,v164,v242\nv_fmac_f32 v85,v165,v243\nM_ 4,216,184\nds_read_b128 v[204:207],v230 offset:57344\nds_read_b64_tr_b16 v[172:173],v236 offset:512\n"
"ds_read_b64_tr_b16 v[174:175],v237 offset:512\nv_fmac_f32 v86,v166,v244\nM_ 16,216,188\nds_read_b128 v[208:211],v232\nds_read_b64_tr_b16 v[176:177],v236 offset:4096\nds_read_b64_tr_b16 v[178:179],v237 offset:4096\nv_fmac_f32 v87,v167,v245\nM_ 28,216,192\n"
"ds_read_b128 v[212:215],v231 offset:8192\nds_read_b64_tr_b16 v[180:181],v236 offset:4608\nds_read_b64_tr_b16 v[182:183],v237 offset:4608\ns_add_u32 m0,s77,57344\ns_add_u32 s7,s20,73728\nbuffer_load_dwordx4 v224,s[12:15],s7 offen sc1 lds\nM_ 40,216,196\nds_read_b128 v[216:219],v231 offset:57344\n"
"M_ 8,220,184\nds_read_b64_tr_b16 v[184:185],v236 offset:8192\nds_read_b64_tr_b16 v[186:187],v237 offset:8192\nM_ 20,220,188\nds_read_b64_tr_b16 v[188:189],v236 offset:8704\nds_read_b64_tr_b16 v[190:191],v237 offset:8704\ns_add_u32 m0,s77,65536\ns_add_u32 s7,s17,256\n"
"buffer_load_dwordx4 v241,s[44:47],s7 offen lds\nM_ 32,220,192\nds_read_b64_tr_b16 v[192:193],v236 offset:12288\nds_read_b64_tr_b16 v[194:195],v237 offset:12288\nM_ 44,220,196\nds_read_b128 v[220:223],v233\nds_read_b64_tr_b16 v[196:197],v236 offset:12800\nds_read_b64_tr_b16 v[198:199],v237 offset:12800\n"
"s_waitcnt lgkmcnt(15)\nM_ 0,200,168\ns_add_u32 m0,s77,98304\ns_add_u32 s7,s17,18688\nbuffer_load_dwordx4 v241,s[44:47],s7 offen lds\nM_ 12,200,172\ns_waitcnt lgkmcnt(13)\nM_ 24,200,176\n"
"s_waitcnt lgkmcnt(10)\nM_ 36,200,180\ns_add_u32 m0,s77,106496\ns_add_u32 s7,s17,37120\nbuffer_load_dwordx4 v241,s[44:47],s7 offen lds\nM_ 4,204,168\nM_ 16,204,172\nM_ 28,204,176\n"
"s_add_u32 m0,s77,114688\ns_add_u32 s7,s28,73792\nbuffer_load_dwordx4 v234,s[8:11],s7 offen lds\nM_ 40,204,180\nM_ 8,208,168\nM_ 20,208,172\nM_ 32,208,176\nM_ 44,208,180\n"
"s_waitcnt lgkmcnt(7)\nM_ 0,212,184\ns_waitcnt lgkmcnt(5)\nM_ 12,212,188\ns_waitcnt lgkmcnt(3)\nM_ 24,212,192\ns_waitcnt vmcnt(9) lgkmcnt(0)\ns_barrier\n"
"M_ 36,212,196\nds_read_b128 v[200:203],v230 offset:16384\nds_read_b64_tr_b16 v[168:169],v236 offset:16384\nds_read_b64_tr_b16 v[170:171],v237 offset:16384\ns_add_u32 m0,s77,0\ns_add_u32 s7,s20,81920\nbuffer_load_dwordx4 v224,s[12:15],s7 offen sc1 lds\nM_ 4,216,184\n"
"ds_read_b128 v[204:207],v230 offset:24576\nds_read_b64_tr_b16 v[172:173],v236 offset:16896\nds_read_b64_tr_b16 v[174:175],v237 offset:16896\nM_ 16,216,188\nds_read_b128 v[208:211],v232 offset:8192\nds_read_b64_tr_b16 v[176:177],v236 offset:20480\nds_read_b64_tr_b16 v[178:179],v237 offset:20480\nM_ 28,216,192\n"
"ds_read_b128 v[212:215],v231 offset:16384\nds_read_b64_tr_b16 v[180:181],v236 offset:20992\nds_read_b64_tr_b16 v[182:183],v237 offset:20992\ns_add_u32 m0,s77,8192\ns_add_u32 s7,s20,90112\nbuffer_load_dwordx4 v224,s[12:15],s7 offen sc1 lds\nM_ 40,216,196\nds_read_b128 v[216:219],v231 offset:24576\n"
"M_ 8,220,184\nds_read_b64 v[160:161],v123 offset:16384\nds_read_b64 v[162:163],v123 offset:20480\nds_read_b64_tr_b16 v[184:185],v236 offset:32768\nds_read_b64_tr_b16 v[186:187],v237 offset:32768\nM_ 20,220,188\nds_read_b64_tr_b16 v[188:189],v236 offset:33280\nds_read_b64_tr_b16 v[190:191],v237 offset:33280\n"
"s_add_u32 m0,s77,73728\ns_add_u32 s7,s18,0\nbuffer_load_dwordx4 v241,s[40:43],s7 offen lds\nM_ 32,220,192\nds_read_b64_tr_b16 v[192:193],v236 offset:36864\nds_read_b64_tr_b16 v[194:195],v237 offset:36864\nM_ 44,220,196\nds_read_b128 v[220:223],v233 offset:8192\n"
"ds_read_b64_tr_b16 v[196:197],v236 offset:37376\nds_read_b64_tr_b16 v[198:199],v237 offset:37376\ns_waitcnt lgkmcnt(15)\nM_ 0,200,168\ns_add_u32 m0,s77,122880\ns_add_u32 s7,s18,18432\nbuffer_load_dwordx4 v241,s[40:43],s7 offen lds\nM_ 12,200,172\n"
"M_ 24,200,176\ns_waitcnt lgkmcnt(10)\nv_lshlrev_b32 v76,16,v160\nv_and_b32 v77,0xffff0000,v160\nM_ 36,200,180\ns_add_u32 m0,s77,131072\ns_add_u32 s7,s18,36864\nbuffer_load_dwordx4 v241,s[40:43],s7 offen lds\n"
"v_lshlrev_b32 v78,16,v161\nv_and_b32 v79,0xffff0000,v161\nM_ 4,204,168\nv_fma_f32 v164,v76,v154,v152\nM_ 16,204,172\nv_fma_f32 v165,v77,v154,v152\nv_fma_f32 v166,v78,v154,v152\nM_ 28,204,176\n"
"v_fma_f32 v167,v79,v154,v152\nM_ 40,204,180\nv_fmac_f32 v76,v164,v246\nv_fmac_f32 v77,v165,v247\nM_ 8,208,168\nv_fmac_f32 v78,v166,v248\nM_ 20,208,172\nv_fmac_f32 v79,v167,v249\n"
"s_waitcnt lgkmcnt(9)\nv_lshlrev_b32 v88,16,v162\nM_ 32,208,176\nv_and_b32 v89,0xffff0000,v162\nM_ 44,208,180\nv_lshlrev_b32 v90,16,v163\nv_and_b32 v91,0xffff0000,v163\ns_waitcnt lgkmcnt(7)\n"
"M_ 0,212,184\nv_fma_f32 v164,v88,v158,v156\ns_waitcnt lgkmcnt(5)\nM_ 12,212,188\nv_fma_f32 v165,v89,v158,v156\nv_fma_f32 v166,v90,v158,v156\ns_waitcnt lgkmcnt(3)\nM_ 24,212,192\n"
"s_waitcnt vmcnt(9) lgkmcnt(0)\ns_barrier\nv_fma_f32 v167,v91,v158,v156\nM_ 36,212,196\nds_read_b128 v[200:203],v228 offset:40960\nds_read_b64_tr_b16 v[168:169],v236 offset:24576\nds_read_b64_tr_b16 v[170:171],v237 offset:24576\ns_add_u32 m0,s77,16384\n"
"s_add_u32 s7,s21,0\nbuffer_load_dwordx4 v224,s[12:15],s7 offen sc1 lds\nv_fmac_f32 v88,v164,v246\nv_fmac_f32 v89,v165,v247\nM_ 4,216,184\nds_read_b128 v[204:207],v228 offset:49152\nds_read_b64_tr_b16 v[172:173],v236 offset:25088\nds_read_b64_tr_b16 v[174:175],v237 offset:25088\n"
"v_fmac_f32 v90,v166,v248\nM_ 16,216,188\nds_read_b128 v[208:211],v232 offset:24576\nds_read_b64_tr_b16 v[176:177],v236 offset:28672\nds_read_b64_tr_b16 v[178:179],v237 offset:28672\nv_fmac_f32 v91,v167,v249\nM_ 28,216,192\nds_read_b128 v[212:215],v229 offset:40960\n"
"ds_read_b64_tr_b16 v[180:181],v236 offset:29184\nds_read_b64_tr_b16 v[182:183],v237 offset:29184\ns_add_u32 m0,s77,32768\ns_add_u32 s7,s21,8192\nbuffer_load_dwordx4 v224,s[12:15],s7 offen sc1 lds\nM_ 40,216,196\nds_read_b128 v[216:219],v229 offset:49152\nM_ 8,220,184\n"
"ds_read_b64_tr_b16 v[184:185],v236 offset:57344\nds_read_b64_tr_b16 v[186:187],v237 offset:57344\nM_ 20,220,188\nds_read_b64_tr_b16 v[188:189],v236 offset:57856\nds_read_b64_tr_b16 v[190:191],v237 offset:57856\ns_add_u32 m0,s77,81920\ns_add_u32 s7,s18,0\nbuffer_load_dwordx4 v241,s[44:47],s7 offen lds\n"
"M_ 32,220,192\nds_read_b64_tr_b16 v[192:193],v236 offset:61440\nds_read_b64_tr_b16 v[194:195],v237 offset:61440\nM_ 44,220,196\nds_read_b128 v[220:223],v233 offset:24576\nds_read_b64_tr_b16 v[196:197],v236 offset:61952\nds_read_b64_tr_b16 v[198:199],v237 offset:61952\ns_waitcnt lgkmcnt(15)\n"
"M_ 0,200,168\ns_add_u32 m0,s77,90112\ns_add_u32 s7,s18,18432\nbuffer_load_dwordx4 v241,s[44:47],s7 offen lds\nM_ 12,200,172\ns_waitcnt lgkmcnt(13)\nM_ 24,200,176\ns_waitcnt lgkmcnt(10)\n"
"M_ 36,200,180\ns_add_u32 m0,s77,139264\ns_add_u32 s7,s18,36864\nbuffer_load_dwordx4 v241,s[44:47],s7 offen lds\nM_ 4,204,168\nM_ 16,204,172\nM_ 28,204,176\ns_add_u32 m0,s77,147456\n"
"s_add_u32 s7,s21,16384\nbuffer_load_dwordx4 v224,s[12:15],s7 offen sc1 lds\nM_ 40,204,180\nM_ 8,208,168\nM_ 20,208,172\nM_ 32,208,176\nM_ 44,208,180\ns_waitcnt lgkmcnt(7)\n"
"M_ 0,212,184\ns_waitcnt lgkmcnt(5)\nM_ 12,212,188\ns_waitcnt lgkmcnt(3)\nM_ 24,212,192\ns_waitcnt vmcnt(9) lgkmcnt(0)\ns_barrier\nM_ 36,212,196\n"
"ds_read_b128 v[200:203],v230\nds_read_b64_tr_b16 v[168:169],v236\nds_read_b64_tr_b16 v[170:171],v237\ns_add_u32 m0,s77,24576\ns_add_u32 s7,s21,24576\nbuffer_load_dwordx4 v224,s[12:15],s7 offen sc1 lds\nM_ 4,216,184\nds_read_b128 v[204:207],v230 offset:32768\n"
"ds_read_b64_tr_b16 v[172:173],v236 offset:512\nds_read_b64_tr_b16 v[174:175],v237 offset:512\nM_ 16,216,188\nds_read_b128 v[208:211],v230 offset:40960\nds_read_b64_tr_b16 v[176:177],v236 offset:4096\nds_read_b64_tr_b16 v[178:179],v237 offset:4096\nM_ 28,216,192\nds_read_b128 v[212:215],v231\n"
"ds_read_b64_tr_b16 v[180:181],v236 offset:4608\nds_read_b64_tr_b16 v[182:183],v237 offset:4608\ns_add_u32 m0,s77,40960\ns_add_u32 s7,s18,128\nbuffer_load_dwordx4 v241,s[40:43],s7 offen lds\nM_ 40,216,196\nds_read_b128 v[216:219],v231 offset:32768\nM_ 8,220,184\n"
"ds_read_b64 v[160:161],v122 offset:49152\nds_read_b64 v[162:163],v122 offset:53248\nds_read_b64_tr_b16 v[184:185],v236 offset:8192\nds_read_b64_tr_b16 v[186:187],v237 offset:8192\nM_ 20,220,188\nds_read_b64_tr_b16 v[188:189],v236 offset:8704\nds_read_b64_tr_b16 v[190:191],v237 offset:8704\ns_add_u32 m0,s77,49152\n"
"s_add_u32 s7,s18,18560\nbuffer_load_dwordx4 v241,s[40:43],s7 offen lds\nM_ 32,220,192\nds_read_b64_tr_b16 v[192:193],v236 offset:12288\nds_read_b64_tr_b16 v[194:195],v237 offset:12288\nM_ 44,220,196\nds_read_b128 v[220:223],v231 offset:40960\nds_read_b64_tr_b16 v[196:197],v236 offset:12800\n"
"ds_read_b64_tr_b16 v[198:199],v237 offset:12800\ns_waitcnt lgkmcnt(15)\nM_ 0,200,168\ns_add_u32 m0,s77,57344\ns_add_u32 s7,s18,36992\nbuffer_load_dwordx4 v241,s[40:43],s7 offen lds\nM_ 12,200,172\nM_ 24,200,176\n"
"s_waitcnt lgkmcnt(10)\nv_lshlrev_b32 v80,16,v160\nv_and_b32 v81,0xffff0000,v160\nM_ 36,200,180\ns_add_u32 m0,s77,155648\ns_add_u32 s7,s21,32768\nbuffer_load_dwordx4 v224,s[12:15],s7 offen sc1 lds\nv_lshlrev_b32 v82,16,v161\n"
"v_and_b32 v83,0xffff0000,v161\nM_ 4,204,168\nv_fma_f32 v164,v80,v154,v152\nM_ 16,204,172\nv_fma_f32 v165,v81,v154,v152\nv_fma_f32 v166,v82,v154,v152\nM_ 28,204,176\nv_fma_f32 v167,v83,v154,v152\n"
"M_ 40,204,180\nv_fmac_f32 v80,v164,v250\nv_fmac_f32 v81,v165,v251\nM_ 8,208,168\nv_fmac_f32 v82,v166,v252\nM_ 20,208,172\nv_fmac_f32 v83,v167,v253\ns_waitcnt lgkmcnt(9)\n"
"v_lshlrev_b32 v92,16,v162\nM_ 32,208,176\nv_and_b32 v93,0xffff0000,v162\nM_ 44,208,180\nv_lshlrev_b32 v94,16,v163\nv_and_b32 v95,0xffff0000,v163\ns_waitcnt lgkmcnt(7)\nM_ 0,212,184\n"
"v_fma_f32 v164,v92,v158,v156\ns_waitcnt lgkmcnt(5)\nM_ 12,212,188\nv_fma_f32 v165,v93,v158,v156\nv_fma_f32 v166,v94,v158,v156\ns_waitcnt lgkmcnt(3)\nM_ 24,212,192\ns_waitcnt vmcnt(9) lgkmcnt(0)\n"
"s_barrier\nv_fma_f32 v167,v95,v158,v156\nM_ 36,212,196\nds_read_b128 v[200:203],v230 offset:8192\nds_read_b64_tr_b16 v[168:169],v236 offset:16384\nds_read_b64_tr_b16 v[170:171],v237 offset:16384\ns_add_u32 m0,s77,0\ns_add_u32 s7,s21,40960\n"
"buffer_load_dwordx4 v224,s[12:15],s7 offen sc1 lds\nv_fmac_f32 v92,v164,v250\nv_fmac_f32 v93,v165,v251\nM_ 4,216,184\nds_read_b128 v[204:207],v230 offset:57344\nds_read_b64_tr_b16 v[172:173],v236 offset:16896\nds_read_b64_tr_b16 v[174:175],v237 offset:16896\nv_fmac_f32 v94,v166,v252\n"
"M_ 16,216,188\nds_read_b128 v[208:211],v232\nds_read_b64_tr_b16 v[176:177],v236 offset:20480\nds_read_b64_tr_b16 v[178:179],v237 offset:20480\nv_fmac_f32 v95,v167,v253\nM_ 28,216,192\nds_read_b128 v[212:215],v231 offset:8192\nds_read_b64_tr_b16 v[180:181],v236 offset:20992\n"
"ds_read_b64_tr_b16 v[182:183],v237 offset:20992\ns_add_u32 m0,s77,8192\ns_add_u32 s7,s18,128\nbuffer_load_dwordx4 v241,s[44:47],s7 offen lds\nM_ 40,216,196\nds_read_b128 v[216:219],v231 offset:57344\nM_ 8,220,184\nds_read_b64_tr_b16 v[184:185],v236 offset:32768\n"
"ds_read_b64_tr_b16 v[186:187],v237 offset:32768\nM_ 20,220,188\nds_read_b64_tr_b16 v[188:189],v236 offset:33280\nds_read_b64_tr_b16 v[190:191],v237 offset:33280\ns_add_u32 m0,s77,65536\ns_add_u32 s7,s18,18560\nbuffer_load_dwordx4 v241,s[44:47],s7 offen lds\nM_ 32,220,192\n"
"ds_read_b64_tr_b16 v[192:193],v236 offset:36864\nds_read_b64_tr_b16 v[194:195],v237 offset:36864\nv_lshrrev_b32 v116,1,v224\nM_ 44,220,196\nds_read_b128 v[220:223],v233\nds_read_b64_tr_b16 v[196:197],v236 offset:37376\nds_read_b64_tr_b16 v[198:199],v237 offset:37376\nv_lshrrev_b32 v117,5,v224\n"
"v_lshlrev_b32 v117,2,v117\ns_waitcnt lgkmcnt(15)\nM_ 0,200,168\ns_add_u32 m0,s77,98304\ns_add_u32 s7,s18,36992\nbuffer_load_dwordx4 v241,s[44:47],s7 offen lds\nv_mov_b32 v112,v117\nM_ 12,200,172\n"
"s_waitcnt lgkmcnt(13)\nM_ 24,200,176\nv_lshrrev_b32 v115,1,v224\nv_add_u32 v115,s77,v115\ns_waitcnt lgkmcnt(10)\nM_ 36,200,180\ns_add_u32 m0,s77,106496\ns_add_u32 s7,s21,49152\n"
"buffer_load_dwordx4 v224,s[12:15],s7 offen sc1 lds\nv_and_b32 v114,255,v224\nv_lshl_add_u32 v114,v114,1,s77\nM_ 4,204,168\nv_lshrrev_b32 v113,8,v224\nM_ 16,204,172\nv_add_f32 v0,v0,v48\nv_add_f32 v1,v1,v49\n"
"v_add_f32 v2,v2,v50\nv_add_f32 v3,v3,v51\nv_lshl_add_u32 v114,v113,3,v114\nM_ 28,204,176\ns_add_u32 m0,s77,114688\ns_add_u32 s7,s21,57344\nbuffer_load_dwordx4 v224,s[12:15],s7 offen sc1 lds\nM_ 40,204,180\n"
"M_ 8,208,168\nM_ 20,208,172\nv_add_f32 v4,v4,v52\nv_add_f32 v5,v5,v53\nv_add_f32 v6,v6,v54\nv_add_f32 v7,v7,v55\nM_ 32,208,176\nM_ 44,208,180\n"
"s_waitcnt lgkmcnt(7)\nM_ 0,212,184\ns_waitcnt lgkmcnt(5)\nM_ 12,212,188\nv_add_f32 v8,v8,v56\nv_add_f32 v9,v9,v57\nv_add_f32 v10,v10,v58\nv_add_f32 v11,v11,v59\n"
"s_waitcnt lgkmcnt(3)\nM_ 24,212,192\ns_waitcnt vmcnt(10) lgkmcnt(0)\ns_barrier\nM_ 36,212,196\nds_read_b128 v[200:203],v230 offset:16384\nds_read_b64_tr_b16 v[168:169],v120 offset:16384\nds_read_b64_tr_b16 v[170:171],v121 offset:16384\n"
"s_add_u32 m0,s77,16384\ns_add_u32 s7,s18,256\nbuffer_load_dwordx4 v241,s[40:43],s7 offen lds\nM_ 4,216,184\nds_read_b128 v[204:207],v230 offset:24576\nds_read_b64_tr_b16 v[172:173],v120 offset:16896\nds_read_b64_tr_b16 v[174:175],v121 offset:16896\nM_ 16,216,188\n"
"ds_read_b128 v[208:211],v232 offset:8192\nds_read_b64_tr_b16 v[176:177],v120 offset:20480\nds_read_b64_tr_b16 v[178:179],v121 offset:20480\nM_ 28,216,192\nds_read_b128 v[212:215],v231 offset:16384\nds_read_b64_tr_b16 v[180:181],v120 offset:20992\nds_read_b64_tr_b16 v[182:183],v121 offset:20992\ns_add_u32 m0,s77,32768\n"
"s_add_u32 s7,s18,18688\nbuffer_load_dwordx4 v241,s[40:43],s7 offen lds\nM_ 40,216,196\nds_read_b128 v[216:219],v231 offset:24576\nM_ 8,220,184\nds_read_b64_tr_b16 v[184:185],v236 offset:24576\nds_read_b64_tr_b16 v[186:187],v237 offset:24576\nM_ 20,220,188\n"
"ds_read_b64_tr_b16 v[188:189],v236 offset:25088\nds_read_b64_tr_b16 v[190:191],v237 offset:25088\ns_add_u32 m0,s77,73728\ns_add_u32 s7,s18,37120\nbuffer_load_dwordx4 v241,s[40:43],s7 offen lds\nM_ 32,220,192\nds_read_b64_tr_b16 v[192:193],v236 offset:28672\nds_read_b64_tr_b16 v[194:195],v237 offset:28672\n"
"M_ 44,220,196\nds_read_b128 v[220:223],v233 offset:8192\nds_read_b64_tr_b16 v[196:197],v236 offset:29184\nds_read_b64_tr_b16 v[198:199],v237 offset:29184\ns_waitcnt lgkmcnt(15)\nM_ 0,200,168\ns_add_u32 m0,s77,122880\ns_add_u32 s7,s21,65536\n"
"buffer_load_dwordx4 v224,s[12:15],s7 offen sc1 lds\nM_ 12,200,172\ns_waitcnt lgkmcnt(13)\nM_ 24,200,176\ns_waitcnt lgkmcnt(10)\nM_ 36,200,180\ns_add_u32 m0,s77,131072\ns_add_u32 s7,s21,73728\n"
"buffer_load_dwordx4 v224,s[12:15],s7 offen sc1 lds\nM_ 4,204,168\nM_ 16,204,172\nM_ 28,204,176\nv_add_f32 v12,v12,v60\nv_add_f32 v13,v13,v61\nv_add_f32 v14,v14,v62\nv_add_f32 v15,v15,v63\n"
"M_ 40,204,180\nM_ 8,208,168\nM_ 20,208,172\nM_ 32,208,176\nv_add_f32 v16,v16,v64\nv_add_f32 v17,v17,v65\nv_add_f32 v18,v18,v66\nv_add_f32 v19,v19,v67\n"
"M_ 44,208,180\ns_waitcnt lgkmcnt(7)\nM_ 0,212,184\ns_waitcnt lgkmcnt(5)\nM_ 12,212,188\ns_waitcnt lgkmcnt(3)\nM_ 24,212,192\nv_add_f32 v20,v20,v68\n"
"v_add_f32 v21,v21,v69\nv_add_f32 v22,v22,v70\nv_add_f32 v23,v23,v71\ns_waitcnt vmcnt(10) lgkmcnt(0)\ns_barrier\nM_ 36,212,196\nds_read_b128 v[200:203],v228 offset:40960\nds_read_b64_tr_b16 v[168:169],v120 offset:24576\n"
"ds_read_b64_tr_b16 v[170:171],v121 offset:24576\ns_add_u32 m0,s77,24576\ns_add_u32 s7,s18,256\nbuffer_load_dwordx4 v241,s[44:47],s7 offen lds\nM_ 4,216,184\nds_read_b128 v[204:207],v228 offset:49152\nds_read_b64_tr_b16 v[172:173],v120 offset:25088\nds_read_b64_tr_b16 v[174:175],v121 offset:25088\n"
"M_ 16,216,188\nds_read_b128 v[208:211],v228 offset:57344\nds_read_b64_tr_b16 v[176:177],v120 offset:28672\nds_read_b64_tr_b16 v[178:179],v121 offset:28672\nM_ 28,216,192\nds_read_b128 v[212:215],v229 offset:40960\nds_read_b64_tr_b16 v[180:181],v120 offset:29184\nds_read_b64_tr_b16 v[182:183],v121 offset:29184\n"
"s_add_u32 m0,s77,81920\ns_add_u32 s7,s18,18688\nbuffer_load_dwordx4 v241,s[44:47],s7 offen lds\nM_ 40,216,196\nds_read_b128 v[216:219],v229 offset:49152\nM_ 8,220,184\nds_read_b64_tr_b16 v[184:185],v236\nds_read_b64_tr_b16 v[186:187],v237\n"
"M_ 20,220,188\nds_read_b64_tr_b16 v[188:189],v236 offset:512\nds_read_b64_tr_b16 v[190:191],v237 offset:512\ns_add_u32 m0,s77,90112\ns_add_u32 s7,s18,37120\nbuffer_load_dwordx4 v241,s[44:47],s7 offen lds\nM_ 32,220,192\nds_read_b64_tr_b16 v[192:193],v236 offset:4096\n"
"ds_read_b64_tr_b16 v[194:195],v237 offset:4096\nM_ 44,220,196\nds_read_b128 v[220:223],v229 offset:57344\nds_read_b64_tr_b16 v[196:197],v236 offset:4608\nds_read_b64_tr_b16 v[198:199],v237 offset:4608\ns_waitcnt lgkmcnt(15)\nM_ 0,200,168\ns_add_u32 m0,s77,139264\n"
"s_add_u32 s7,s21,81920\nbuffer_load_dwordx4 v224,s[12:15],s7 offen sc1 lds\nM_ 12,200,172\ns_waitcnt lgkmcnt(13)\nM_ 24,200,176\ns_waitcnt lgkmcnt(10)\nM_ 36,200,180\ns_add_u32 m0,s77,147456\n"
"s_add_u32 s7,s21,90112\nbuffer_load_dwordx4 v224,s[12:15],s7 offen sc1 lds\nM_ 4,204,168\nM_ 16,204,172\nM_ 28,204,176\nM_ 40,204,180\nv_add_f32 v24,v24,v72\nv_add_f32 v25,v25,v73\n"
"v_add_f32 v26,v26,v74\nv_add_f32 v27,v27,v75\nM_ 8,208,168\nM_ 20,208,172\nM_ 32,208,176\nM_ 44,208,180\nv_add_f32 v28,v28,v76\nv_add_f32 v29,v29,v77\n"
"v_add_f32 v30,v30,v78\nv_add_f32 v31,v31,v79\ns_waitcnt lgkmcnt(7)\nM_ 0,212,184\ns_waitcnt lgkmcnt(5)\nM_ 12,212,188\ns_waitcnt lgkmcnt(3)\nM_ 24,212,192\n"
"s_waitcnt vmcnt(10) lgkmcnt(0)\ns_barrier\nM_ 36,212,196\nv_add_f32 v32,v32,v80\nv_add_f32 v33,v33,v81\nv_add_f32 v34,v34,v82\nv_add_f32 v35,v35,v83\nds_read_b128 v[200:203],v228 offset:8192\n"
"ds_read_b64_tr_b16 v[168:169],v118 offset:40960\nds_read_b64_tr_b16 v[170:171],v119 offset:40960\nM_ 4,216,184\nds_read_b128 v[204:207],v230\nds_read_b64_tr_b16 v[172:173],v118 offset:41472\nds_read_b64_tr_b16 v[174:175],v119 offset:41472\nM_ 16,216,188\nds_read_b128 v[208:211],v230 offset:32768\n"
"ds_read_b64_tr_b16 v[176:177],v118 offset:45056\nds_read_b64_tr_b16 v[178:179],v119 offset:45056\nM_ 28,216,192\nds_read_b128 v[212:215],v229 offset:8192\nds_read_b64_tr_b16 v[180:181],v118 offset:45568\nds_read_b64_tr_b16 v[182:183],v119 offset:45568\nM_ 40,216,196\nds_read_b128 v[216:219],v231\n"
"M_ 8,220,184\nds_read_b64_tr_b16 v[184:185],v118 offset:49152\nds_read_b64_tr_b16 v[186:187],v119 offset:49152\nM_ 20,220,188\nds_read_b64_tr_b16 v[188:189],v118 offset:49664\nds_read_b64_tr_b16 v[190:191],v119 offset:49664\nM_ 32,220,192\nds_read_b64_tr_b16 v[192:193],v118 offset:53248\n"
"ds_read_b64_tr_b16 v[194:195],v119 offset:53248\nM_ 44,220,196\nds_read_b128 v[220:223],v231 offset:32768\nds_read_b64_tr_b16 v[196:197],v118 offset:53760\nds_read_b64_tr_b16 v[198:199],v119 offset:53760\ns_waitcnt lgkmcnt(15)\nM_ 0,200,168\nM_ 12,200,172\n"
"s_waitcnt lgkmcnt(13)\nM_ 24,200,176\ns_waitcnt lgkmcnt(10)\nM_ 36,200,180\nM_ 4,204,168\nM_ 16,204,172\nM_ 28,204,176\nM_ 40,204,180\n"
"M_ 8,208,168\ns_nop 2\nv_add_f32 v36,v36,v84\nv_add_f32 v37,v37,v85\nv_add_f32 v38,v38,v86\nv_add_f32 v39,v39,v87\nM_ 20,208,172\nM_ 32,208,176\n"
"M_ 44,208,180\ns_waitcnt lgkmcnt(7)\nM_ 0,212,184\nv_add_f32 v40,v40,v88\nv_add_f32 v41,v41,v89\nv_add_f32 v42,v42,v90\nv_add_f32 v43,v43,v91\ns_waitcnt lgkmcnt(5)\n"
"M_ 12,212,188\ns_waitcnt lgkmcnt(3)\nM_ 24,212,192\ns_waitcnt vmcnt(5) lgkmcnt(0)\ns_barrier\nM_ 36,212,196\nds_read_b128 v[200:203],v228 offset:16384\nds_read_b64_tr_b16 v[168:169],v118 offset:57344\n"
"ds_read_b64_tr_b16 v[170:171],v119 offset:57344\nM_ 4,216,184\nv_add_f32 v44,v44,v92\nv_add_f32 v45,v45,v93\nv_add_f32 v46,v46,v94\nv_add_f32 v47,v47,v95\nds_read_b128 v[204:207],v228 offset:32768\nds_read_b64_tr_b16 v[172:173],v118 offset:57856\n"
"ds_read_b64_tr_b16 v[174:175],v119 offset:57856\nM_ 16,216,188\nds_read_b128 v[208:211],v230 offset:8192\nds_read_b64_tr_b16 v[176:177],v118 offset:61440\nds_read_b64_tr_b16 v[178:179],v119 offset:61440\nM_ 28,216,192\nds_read_b128 v[212:215],v229 offset:16384\nds_read_b64_tr_b16 v[180:181],v118 offset:61952\n"
"ds_read_b64_tr_b16 v[182:183],v119 offset:61952\nM_ 40,216,196\nds_read_b128 v[216:219],v229 offset:32768\nM_ 8,220,184\nds_read_b64_tr_b16 v[184:185],v120\nds_read_b64_tr_b16 v[186:187],v121\nM_ 20,220,188\nds_read_b64_tr_b16 v[188:189],v120 offset:512\n"
"ds_read_b64_tr_b16 v[190:191],v121 offset:512\nM_ 32,220,192\nds_read_b64_tr_b16 v[192:193],v120 offset:4096\nds_read_b64_tr_b16 v[194:195],v121 offset:4096\nM_ 44,220,196\nds_read_b128 v[220:223],v231 offset:8192\nds_read_b64_tr_b16 v[196:197],v120 offset:4608\nds_read_b64_tr_b16 v[198:199],v121 offset:4608\n"
"s_waitcnt lgkmcnt(15)\nM_ 0,200,168\nM_ 12,200,172\ns_waitcnt lgkmcnt(13)\nM_ 24,200,176\ns_waitcnt lgkmcnt(10)\nM_ 36,200,180\nM_ 4,204,168\n"
"M_ 16,204,172\nM_ 28,204,176\nM_ 40,204,180\nM_ 8,208,168\nM_ 20,208,172\nM_ 32,208,176\nM_ 44,208,180\ns_waitcnt lgkmcnt(7)\n"
"M_ 0,212,184\ns_waitcnt lgkmcnt(5)\nM_ 12,212,188\ns_waitcnt lgkmcnt(3)\nM_ 24,212,192\ns_waitcnt vmcnt(0) lgkmcnt(0)\ns_barrier\nM_ 36,212,196\n"
"ds_read_b128 v[200:203],v228 offset:24576\nds_read_b64_tr_b16 v[168:169],v120 offset:8192\nds_read_b64_tr_b16 v[170:171],v121 offset:8192\nM_ 4,216,184\nds_read_b128 v[204:207],v230 offset:16384\nds_read_b64_tr_b16 v[172:173],v120 offset:8704\nds_read_b64_tr_b16 v[174:175],v121 offset:8704\nM_ 16,216,188\n"
"ds_read_b128 v[208:211],v230 offset:24576\nds_read_b64_tr_b16 v[176:177],v120 offset:12288\nds_read_b64_tr_b16 v[178:179],v121 offset:12288\nM_ 28,216,192\nds_read_b128 v[212:215],v229 offset:24576\nds_read_b64_tr_b16 v[180:181],v120 offset:12800\nds_read_b64_tr_b16 v[182:183],v121 offset:12800\nM_ 40,216,196\n"
"ds_read_b128 v[216:219],v231 offset:16384\nM_ 8,220,184\nds_read_b64_tr_b16 v[184:185],v120 offset:16384\nds_read_b64_tr_b16 v[186:187],v121 offset:16384\nM_ 20,220,188\nds_read_b64_tr_b16 v[188:189],v120 offset:16896\nds_read_b64_tr_b16 v[190:191],v121 offset:16896\nM_ 32,220,192\n"
"ds_read_b64_tr_b16 v[192:193],v120 offset:20480\nds_read_b64_tr_b16 v[194:195],v121 offset:20480\nM_ 44,220,196\nds_read_b128 v[220:223],v231 offset:24576\nds_read_b64_tr_b16 v[196:197],v120 offset:20992\nds_read_b64_tr_b16 v[198:199],v121 offset:20992\ns_waitcnt lgkmcnt(15)\nM_ 0,200,168\n"
"M_ 4,204,168\nM_ 8,208,168\ns_waitcnt lgkmcnt(7)\nM_ 0,212,184\nM_ 4,216,184\ns_waitcnt lgkmcnt(2)\nM_ 8,220,184\nM_ 12,200,172\n"
"M_ 16,204,172\nM_ 20,208,172\ns_nop 1\nv_cvt_pk_bf16_f32 v0,v0,v1\nv_cvt_pk_bf16_f32 v1,v2,v3\nds_write_b64 v114,v[0:1]\nv_cvt_pk_bf16_f32 v2,v4,v5\nM_ 12,212,188\n"
"v_cvt_pk_bf16_f32 v3,v6,v7\nds_write_b64 v114,v[2:3] offset:512\nv_cvt_pk_bf16_f32 v4,v8,v9\nM_ 16,216,188\nv_cvt_pk_bf16_f32 v5,v10,v11\nds_write_b64 v114,v[4:5] offset:8192\nds_read_b64_tr_b16 v[6:7],v115\nds_read_b64_tr_b16 v[8:9],v115 offset:512\n"
"ds_read_b64_tr_b16 v[10:11],v115 offset:8192\nM_ 20,220,188\ns_add_u32 s2,s19,0\ns_waitcnt lgkmcnt(1)\nbuffer_store_dwordx4 v[6:9],v224,s[12:15],s2 offen sc1\nv_add3_u32 v0,v6,v7,v8\nM_ 24,200,176\ns_waitcnt lgkmcnt(0)\n"
"v_add3_u32 v0,v0,v9,v10\nv_add3_u32 v0,v0,v11,s96\nbuffer_store_dwordx2 v[10:11],v116,s[12:15],s2 offen offset:1024 sc1\nM_ 28,204,176\nv_add_u32_dpp v0,v0,v0 quad_perm:[1,0,3,2] row_mask:0xf bank_mask:0xf\nbuffer_store_dword v0,v112,s[12:15],s2 offen offset:1536 sc1\nM_ 32,208,176\nv_cvt_pk_bf16_f32 v12,v12,v13\n"
"v_cvt_pk_bf16_f32 v13,v14,v15\nds_write_b64 v114,v[12:13]\nv_cvt_pk_bf16_f32 v14,v16,v17\nM_ 24,212,192\nv_cvt_pk_bf16_f32 v15,v18,v19\nds_write_b64 v114,v[14:15] offset:512\nv_cvt_pk_bf16_f32 v16,v20,v21\nM_ 28,216,192\n"
"v_cvt_pk_bf16_f32 v17,v22,v23\nds_write_b64 v114,v[16:17] offset:8192\nds_read_b64_tr_b16 v[18:19],v115\nds_read_b64_tr_b16 v[20:21],v115 offset:512\nds_read_b64_tr_b16 v[22:23],v115 offset:8192\nM_ 32,220,192\ns_add_u32 s2,s19,442368\ns_waitcnt lgkmcnt(1)\n"
"buffer_store_dwordx4 v[18:21],v224,s[12:15],s2 offen sc1\nv_add3_u32 v12,v18,v19,v20\nM_ 36,200,180\ns_waitcnt lgkmcnt(0)\nv_add3_u32 v12,v12,v21,v22\nv_add3_u32 v12,v12,v23,s96\nbuffer_store_dwordx2 v[22:23],v116,s[12:15],s2 offen offset:1024 sc1\nM_ 40,204,180\n"
"v_add_u32_dpp v12,v12,v12 quad_perm:[1,0,3,2] row_mask:0xf bank_mask:0xf\nbuffer_store_dword v12,v112,s[12:15],s2 offen offset:1536 sc1\nM_ 44,208,180\nv_cvt_pk_bf16_f32 v24,v24,v25\nv_cvt_pk_bf16_f32 v25,v26,v27\nds_write_b64 v114,v[24:25]\nv_cvt_pk_bf16_f32 v26,v28,v29\nM_ 36,212,196\n"
"v_cvt_pk_bf16_f32 v27,v30,v31\nds_write_b64 v114,v[26:27] offset:512\nv_cvt_pk_bf16_f32 v28,v32,v33\nM_ 40,216,196\nv_cvt_pk_bf16_f32 v29,v34,v35\nds_write_b64 v114,v[28:29] offset:8192\nds_read_b64_tr_b16 v[30:31],v115\nds_read_b64_tr_b16 v[32:33],v115 offset:512\n"
"ds_read_b64_tr_b16 v[34:35],v115 offset:8192\nM_ 44,220,196\ns_add_u32 s2,s19,884736\ns_waitcnt lgkmcnt(1)\nbuffer_store_dwordx4 v[30:33],v224,s[12:15],s2 offen sc1\nv_add3_u32 v24,v30,v31,v32\ns_waitcnt lgkmcnt(0)\nv_add3_u32 v24,v24,v33,v34\n"
"v_add3_u32 v24,v24,v35,s96\nbuffer_store_dwordx2 v[34:35],v116,s[12:15],s2 offen offset:1024 sc1\ns_nop 0\nv_add_u32_dpp v24,v24,v24 quad_perm:[1,0,3,2] row_mask:0xf bank_mask:0xf\nbuffer_store_dword v24,v112,s[12:15],s2 offen offset:1536 sc1\nv_cvt_pk_bf16_f32 v36,v36,v37\nv_cvt_pk_bf16_f32 v37,v38,v39\nds_write_b64 v114,v[36:37]\n"
"v_cvt_pk_bf16_f32 v38,v40,v41\nv_cvt_pk_bf16_f32 v39,v42,v43\nds_write_b64 v114,v[38:39] offset:512\nv_cvt_pk_bf16_f32 v40,v44,v45\nv_cvt_pk_bf16_f32 v41,v46,v47\nds_write_b64 v114,v[40:41] offset:8192\nds_read_b64_tr_b16 v[42:43],v115\nds_read_b64_tr_b16 v[44:45],v115 offset:512\n"
"ds_read_b64_tr_b16 v[46:47],v115 offset:8192\ns_add_u32 s2,s19,1327104\ns_waitcnt lgkmcnt(1)\nbuffer_store_dwordx4 v[42:45],v224,s[12:15],s2 offen sc1\nv_add3_u32 v36,v42,v43,v44\ns_waitcnt lgkmcnt(0)\nv_add3_u32 v36,v36,v45,v46\nv_add3_u32 v36,v36,v47,s96\n"
"buffer_store_dwordx2 v[46:47],v116,s[12:15],s2 offen offset:1024 sc1\ns_nop 0\nv_add_u32_dpp v36,v36,v36 quad_perm:[1,0,3,2] row_mask:0xf bank_mask:0xf\nbuffer_store_dword v36,v112,s[12:15],s2 offen offset:1536 sc1\ns_endpgm\n.sl73_%=:\ns_mov_b32 s6,200000\n.lp74_%=:\n"
"s_sleep 1\ns_nop 3\nbuffer_load_dwordx4 v[128:131],v234,s[12:15],0 offen sc1\ns_waitcnt vmcnt(0)\nv_cmp_eq_u32 s[4:5],s96,v129\nv_cmp_eq_u32 vcc,s97,v131\ns_nop 0\ns_and_b64 s[4:5],s[4:5],vcc\n"
"s_cmp_eq_u64 s[4:5],-1\ns_cbranch_scc1 .ok72_%=\ns_sub_u32 s6,s6,1\ns_cmp_lg_u32 s6,0\ns_cbranch_scc1 .lp74_%=\ns_branch .ok72_%=\n.sl76_%=:\ns_mov_b32 s6,200000\n"
".lp77_%=:\ns_sleep 1\ns_nop 3\nbuffer_load_dwordx2 v[144:145],v121,s[12:15],s22 offen sc1\nbuffer_load_dwordx2 v[146:147],v121,s[12:15],s23 offen sc1\nbuffer_load_dwordx2 v[148:149],v121,s[12:15],s22 offen offset:512 sc1\nbuffer_load_dwordx2 v[150:151],v121,s[12:15],s23 offen offset:512 sc1\nbuffer_load_dwordx2 v[152:153],v121,s[12:15],s22 offen offset:1024 sc1\n"
"buffer_load_dwordx2 v[154:155],v121,s[12:15],s23 offen offset:1024 sc1\ns_waitcnt vmcnt(0)\nv_cmp_ne_u32 s[4:5],s96,v145\nv_cmp_ne_u32 vcc,s96,v147\ns_nop 0\ns_or_b64 s[4:5],s[4:5],vcc\nv_cmp_ne_u32 vcc,s96,v149\ns_nop 0\n"
"s_or_b64 s[4:5],s[4:5],vcc\nv_cmp_ne_u32 vcc,s96,v151\ns_nop 0\ns_or_b64 s[4:5],s[4:5],vcc\nv_cmp_ne_u32 vcc,s96,v153\ns_nop 0\ns_or_b64 s[4:5],s[4:5],vcc\nv_cmp_ne_u32 vcc,s96,v155\n"
"s_nop 0\ns_or_b64 s[4:5],s[4:5],vcc\ns_cbranch_scc0 .ok75_%=\ns_sub_u32 s6,s6,1\ns_cmp_lg_u32 s6,0\ns_cbranch_scc1 .lp77_%=\ns_branch .ok75_%=\n.sl79_%=:\n"
"s_mov_b32 s6,200000\n.lp80_%=:\ns_sleep 1\ns_nop 3\nbuffer_load_dwordx4 v[144:147],v112,s[12:15],0 offen sc1\nbuffer_load_dwordx4 v[148:151],v112,s[12:15],0 offen offset:256 sc1\nbuffer_load_dwordx4 v[152:155],v113,s[12:15],0 offen sc1\nbuffer_load_dwordx4 v[156:159],v113,s[12:15],0 offen offset:256 sc1\n"
"buffer_load_dwordx4 v[160:163],v114,s[12:15],0 offen sc1\nbuffer_load_dwordx4 v[164:167],v114,s[12:15],0 offen offset:256 sc1\nbuffer_load_dwordx4 v[168:171],v115,s[12:15],0 offen sc1\nbuffer_load_dwordx4 v[172:175],v115,s[12:15],0 offen offset:256 sc1\nbuffer_load_dwordx4 v[176:179],v116,s[12:15],0 offen sc1\nbuffer_load_dwordx4 v[180:183],v116,s[12:15],0 offen offset:256 sc1\nbuffer_load_dwordx4 v[184:187],v117,s[12:15],0 offen sc1\nbuffer_load_dwordx4 v[188:191],v117,s[12:15],0 offen offset:256 sc1\n"
"buffer_load_dwordx4 v[192:195],v118,s[12:15],0 offen sc1\nbuffer_load_dwordx4 v[196:199],v118,s[12:15],0 offen offset:256 sc1\nbuffer_load_dwordx4 v[200:203],v119,s[12:15],0 offen sc1\nbuffer_load_dwordx4 v[204:207],v119,s[12:15],0 offen offset:256 sc1\ns_waitcnt vmcnt(0)\nv_cmp_ne_u32 s[2:3],s96,v147\nv_cmp_ne_u32 vcc,s96,v151\ns_nop 0\n"
"s_or_b64 vcc,vcc,s[2:3]\ns_and_b64 s[4:5],vcc,s[78:79]\nv_cmp_ne_u32 s[2:3],s96,v155\nv_cmp_ne_u32 vcc,s96,v159\ns_nop 0\ns_or_b64 vcc,vcc,s[2:3]\ns_and_b64 vcc,vcc,s[80:81]\ns_or_b64 s[4:5],s[4:5],vcc\n"
"v_cmp_ne_u32 s[2:3],s96,v163\nv_cmp_ne_u32 vcc,s96,v167\ns_nop 0\ns_or_b64 vcc,vcc,s[2:3]\ns_and_b64 vcc,vcc,s[82:83]\ns_or_b64 s[4:5],s[4:5],vcc\nv_cmp_ne_u32 s[2:3],s96,v171\nv_cmp_ne_u32 vcc,s96,v175\n"
"s_nop 0\ns_or_b64 vcc,vcc,s[2:3]\ns_and_b64 vcc,vcc,s[84:85]\ns_or_b64 s[4:5],s[4:5],vcc\nv_cmp_ne_u32 s[2:3],s96,v179\nv_cmp_ne_u32 vcc,s96,v183\ns_nop 0\ns_or_b64 vcc,vcc,s[2:3]\n"
"s_and_b64 vcc,vcc,s[86:87]\ns_or_b64 s[4:5],s[4:5],vcc\nv_cmp_ne_u32 s[2:3],s96,v187\nv_cmp_ne_u32 vcc,s96,v191\ns_nop 0\ns_or_b64 vcc,vcc,s[2:3]\ns_and_b64 vcc,vcc,s[88:89]\ns_or_b64 s[4:5],s[4:5],vcc\n"
"v_cmp_ne_u32 s[2:3],s96,v195\nv_cmp_ne_u32 vcc,s96,v199\ns_nop 0\ns_or_b64 vcc,vcc,s[2:3]\ns_and_b64 vcc,vcc,s[90:91]\ns_or_b64 s[4:5],s[4:5],vcc\nv_cmp_ne_u32 s[2:3],s96,v203\nv_cmp_ne_u32 vcc,s96,v207\n"
"s_nop 0\ns_or_b64 vcc,vcc,s[2:3]\ns_and_b64 vcc,vcc,s[92:93]\ns_or_b64 s[4:5],s[4:5],vcc\ns_cbranch_scc0 .ok78_%=\ns_sub_u32 s6,s6,1\ns_cmp_lg_u32 s6,0\ns_cbranch_scc1 .lp80_%=\n"
"s_branch .ok78_%=\n.sl82_%=:\ns_mov_b32 s6,200000\n.lp83_%=:\ns_sleep 1\ns_nop 3\nbuffer_load_dwordx2 v[116:117],v113,s[12:15],s30 offen sc1\ns_waitcnt vmcnt(0)\n"
"v_cmp_ne_u64 vcc,v[116:117],v[114:115]\ns_nop 0\ns_cmp_lg_u64 vcc,0\ns_cbranch_scc0 .ok81_%=\ns_sub_u32 s6,s6,1\ns_cmp_lg_u32 s6,0\ns_cbranch_scc1 .lp83_%=\ns_branch .ok81_%=\n"
".end_%=:\n"
      :
      : "s"(__builtin_amdgcn_kernarg_segment_ptr()), "s"(blockIdx.x), "{v0}"(threadIdx.x), "s"((u32)(size_t)smem), "s"(work_tab)
      : "vcc", "scc", "memory", "s95", "s98", "s99", "s101", "v255");
}

extern "C" void kernel_launch(void* const* d_in, const int* in_sizes, int n_in, void* d_out, int out_size, void* d_ws, size_t ws_size,
                              hipStream_t stream) {
  const u32 F = 0x00020000u;
  Args a = {{{d_in[0], 2048 * 1152, F}, {(char*)d_ws, WS_END, F}, {d_in[10], 576 * 9216, F}, {d_in[11], 576 * 9216, F}, {d_in[12], 4608 * 1152, F},
             {d_out, 2048 * 2304, F}, {d_in[3], 576 * 1152, F}, {d_in[4], 576 * 1152, F}, {d_in[5], 576 * 1152, F}, {d_in[6], 576 * 1152, F}},
            {d_in[1], d_in[2], d_in[7], d_in[8]}};
  fused_k<<<256, 512, 0, stream>>>(a);
}
```
